```python
import torch

def _softmax(x, axis=-1):
    return torch.softmax(x, dim=axis)
def _gelu(x):
    return torch.nn.functional.gelu(x, approximate="tanh")
def _silu(x):
    return torch.nn.functional.silu(x)
def _sigmoid(x):
    return torch.sigmoid(x)
def _layernorm(x, axis=-1, eps=1e-5):
    m = x.mean(dim=axis, keepdim=True)
    v = x.var(dim=axis, keepdim=True, unbiased=False)
    return (x - m) / torch.sqrt(v + eps)
def _scatter_add(tgt, idx, src, axis=0):
    return tgt.clone().index_add_(axis, idx, src)
def _segment_sum(src, seg, n):
    out = torch.zeros((n,) + src.shape[1:], dtype=src.dtype)
    return out.index_add_(0, seg, src)
def _pad(x, pw):
    flat = tuple(v for pair in reversed(pw) for v in pair)
    return torch.nn.functional.pad(x, flat)

kernel_name = 'interleave_layers__spectral_fnet__moe_arch_mod__prompt'

def setup_inputs():

    _w = (lambda a: (lambda t: t.to(torch.bfloat16) if t.is_floating_point() else t)(torch.as_tensor(a)))
    def _A_setup():
        B, S, D = 1, 2048, 512
        ns = torch.arange(S)[:, None] * torch.arange(S)[None, :]
        nd = torch.arange(D)[:, None] * torch.arange(D)[None, :]
        Ws_re = torch.cos(-2.0 * torch.pi * ns / S).to(float)
        Ws_im = torch.sin(-2.0 * torch.pi * ns / S).to(float)
        Wd_re = torch.cos(-2.0 * torch.pi * nd / D).to(float)
        Wd_im = torch.sin(-2.0 * torch.pi * nd / D).to(float)
        return {
            "x": _w((lambda *s: torch.randn(*s, dtype=torch.bfloat16) * 0.05)(B, S, D)),
            "dft_seq_re": _w(Ws_re),
            "dft_seq_im": _w(Ws_im),
            "dft_hid_re": _w(Wd_re),
            "dft_hid_im": _w(Wd_im),
        }
    _A_in = _A_setup()

    def _B_setup():
        B, S, D, F, C = 1, 2048, 512, 1792, 2
        return {
            "x": _w((lambda *s: torch.randn(*s, dtype=torch.bfloat16) * 0.05)(B, S, D)),
            "w_r": _w((lambda *s: torch.randn(*s, dtype=torch.bfloat16) * 1.6)(D)),
            "w_gate": _w((lambda *s: torch.randn(*s, dtype=torch.bfloat16) * 0.4)(D, F)),
            "w_up": _w((lambda *s: torch.randn(*s, dtype=torch.bfloat16) * 0.4)(D, F)),
            "w_down": _w((lambda *s: torch.randn(*s, dtype=torch.bfloat16) * 0.4)(F, D)),
        }
    _B_in = _B_setup()

    return {
        "A_x": _A_in["x"],
        "A_dft_seq_re": _A_in["dft_seq_re"],
        "A_dft_seq_im": _A_in["dft_seq_im"],
        "A_dft_hid_re": _A_in["dft_hid_re"],
        "A_dft_hid_im": _A_in["dft_hid_im"],
        "B_x": _B_in["x"],
        "B_w_r": _B_in["w_r"],
        "B_w_gate": _B_in["w_gate"],
        "B_w_up": _B_in["w_up"],
        "B_w_down": _B_in["w_down"],
    }

def reference(A_x, A_dft_seq_re, A_dft_seq_im, A_dft_hid_re, A_dft_hid_im, B_x, B_w_r, B_w_gate, B_w_up, B_w_down):
    def _A_ref(x, dft_seq_re, dft_seq_im, dft_hid_re, dft_hid_im):
        a = torch.einsum("bsd,ed->bse", x, dft_hid_re)
        b = torch.einsum("bsd,ed->bse", x, dft_hid_im)
        y = torch.einsum("ts,bse->bte", dft_seq_re, a) - torch.einsum("ts,bse->bte", dft_seq_im, b)
        return y

    def _B_ref(x, w_r, w_gate, w_up, w_down):
        B, S, D = x.shape
        C = 2
        r = x @ w_r
        idx = torch.argsort(-r, axis=-1)[:, :C]
        idx_e = (lambda x, n, axis: torch.repeat_interleave(x, n, dim=axis))(idx.reshape(B, C, 1), D, axis=2)
        x_sel = (lambda x, i, axis: torch.take_along_dim(x, i, dim=axis))(x, idx_e, axis=1)
        g = _silu(x_sel @ w_gate)
        u = x_sel @ w_up
        y = (g * u) @ w_down
        r_sel = _sigmoid((lambda x, i, axis: torch.take_along_dim(x, i, dim=axis))(r, idx, axis=1))
        y = y * r_sel.reshape(B, C, 1)
        b_off = (torch.arange(B) * S).reshape(B, 1)
        flat_idx = (idx + b_off).reshape(B * C)
        zero = (x * 0.0).reshape(B * S, D)
        upd = _scatter_add(zero, flat_idx, y.reshape(B * C, D), axis=0)
        return x + upd.reshape(B, S, D)

    x = A_x
    x = x + _A_ref(_layernorm(x, axis=-1), A_dft_seq_re, A_dft_seq_im, A_dft_hid_re, A_dft_hid_im)
    x = x + _B_ref(_layernorm(x, axis=-1), B_w_r, B_w_gate, B_w_up, B_w_down)
    x = x + _A_ref(_layernorm(x, axis=-1), A_dft_seq_re, A_dft_seq_im, A_dft_hid_re, A_dft_hid_im)
    x = x + _B_ref(_layernorm(x, axis=-1), B_w_r, B_w_gate, B_w_up, B_w_down)
    return x
```

```cpp
#include <hip/hip_runtime.h>
#include <hip/hip_bf16.h>
#include <stdint.h>

typedef unsigned short u16;
typedef short bf16x8 __attribute__((ext_vector_type(8)));
typedef float f32x16 __attribute__((ext_vector_type(16)));
typedef float f32x4 __attribute__((ext_vector_type(4)));

constexpr int S = 2048, D = 512, F = 1792;

__device__ __forceinline__ float bf2f(u16 b) { return __builtin_bit_cast(float, (unsigned)b << 16); }
__device__ __forceinline__ u16 f2bf(float f) { __hip_bfloat16 h = __float2bfloat16(f); return __builtin_bit_cast(u16, h); }

__device__ __forceinline__ float wave_sum(float v) {
  for (int o = 32; o > 0; o >>= 1) v += __shfl_xor(v, o);
  return v;
}

__global__ void k_ln(const u16* __restrict__ x, u16* __restrict__ h, const u16* __restrict__ w, float* __restrict__ r) {
  int row = blockIdx.x * 4 + (threadIdx.x >> 6), lane = threadIdx.x & 63;
  bf16x8 v = *(const bf16x8*)(x + row * D + lane * 8);
  float f[8], s = 0;
  for (int j = 0; j < 8; ++j) { f[j] = bf2f((u16)v[j]); s += f[j]; }
  float m = wave_sum(s) * (1.f / D);
  float q = 0;
  for (int j = 0; j < 8; ++j) { f[j] -= m; q += f[j] * f[j]; }
  float rs = rsqrtf(wave_sum(q) * (1.f / D) + 1e-5f);
  bf16x8 o; float d = 0;
  bf16x8 wv = *(const bf16x8*)(w + lane * 8);
  for (int j = 0; j < 8; ++j) { u16 b = f2bf(f[j] * rs); o[j] = (short)b; d += bf2f(b) * bf2f((u16)wv[j]); }
  *(bf16x8*)(h + row * D + lane * 8) = o;
  d = wave_sum(d);
  if (lane == 0) r[row] = d;
}

__global__ void k_gemm1(const u16* __restrict__ dre, const u16* __restrict__ dim_, const u16* __restrict__ h, u16* __restrict__ abT) {
  int w = blockIdx.x * 4 + (threadIdx.x >> 6), lane = threadIdx.x & 63;
  int te = w / (S / 32), ts = w % (S / 32);
  const u16* A = (te < 16 ? dre + te * 32 * D : dim_ + (te - 16) * 32 * D) + (lane & 31) * D + (lane >> 5) * 8;
  const u16* B = h + (ts * 32 + (lane & 31)) * D + (lane >> 5) * 8;
  f32x16 acc = {};
  for (int k = 0; k < D; k += 16)
    acc = __builtin_amdgcn_mfma_f32_32x32x16_bf16(*(const bf16x8*)(A + k), *(const bf16x8*)(B + k), acc, 0, 0, 0);
  for (int rg = 0; rg < 16; ++rg) {
    int row = (rg & 3) + 8 * (rg >> 2) + 4 * (lane >> 5);
    abT[(te * 32 + row) * S + ts * 32 + (lane & 31)] = f2bf(acc[rg]);
  }
}

__global__ void k_gemm2(const u16* __restrict__ cr, const u16* __restrict__ ci, const u16* __restrict__ abT, const u16* __restrict__ xi, u16* __restrict__ xo) {
  int w = blockIdx.x * 4 + (threadIdx.x >> 6), lane = threadIdx.x & 63;
  int tt = w / 16, te = w % 16;
  const u16* A0 = cr + (tt * 32 + (lane & 31)) * S + (lane >> 5) * 8;
  const u16* A1 = ci + (tt * 32 + (lane & 31)) * S + (lane >> 5) * 8;
  const u16* B0 = abT + (te * 32 + (lane & 31)) * S + (lane >> 5) * 8;
  const u16* B1 = B0 + 512 * S;
  f32x16 p = {}, q = {};
#pragma unroll 4
  for (int k = 0; k < S; k += 16) {
    p = __builtin_amdgcn_mfma_f32_32x32x16_bf16(*(const bf16x8*)(A0 + k), *(const bf16x8*)(B0 + k), p, 0, 0, 0);
    q = __builtin_amdgcn_mfma_f32_32x32x16_bf16(*(const bf16x8*)(A1 + k), *(const bf16x8*)(B1 + k), q, 0, 0, 0);
  }
  for (int rg = 0; rg < 16; ++rg) {
    int row = (rg & 3) + 8 * (rg >> 2) + 4 * (lane >> 5);
    int o = (tt * 32 + row) * D + te * 32 + (lane & 31);
    xo[o] = f2bf(bf2f(xi[o]) + (p[rg] - q[rg]));
  }
}

__device__ void top2(const float* r, int* idx, float* val, float* sv, int* si) {
  int t = threadIdx.x;
  float b0 = -INFINITY, b1 = -INFINITY; int i0 = 0, i1 = 0;
  for (int i = t; i < S; i += 256) { float v = r[i]; if (v > b0) { b1 = b0; i1 = i0; b0 = v; i0 = i; } else if (v > b1) { b1 = v; i1 = i; } }
  sv[2 * t] = b0; sv[2 * t + 1] = b1; si[2 * t] = i0; si[2 * t + 1] = i1;
  __syncthreads();
  if (t == 0) {
    float c0 = -INFINITY, c1 = -INFINITY; int j0 = 0, j1 = 1;
    for (int i = 0; i < 512; ++i) { float v = sv[i]; int id = si[i];
      if (v > c0 || (v == c0 && id < j0)) { c1 = c0; j1 = j0; c0 = v; j0 = id; } else if (v > c1 || (v == c1 && id < j1)) { c1 = v; j1 = id; } }
    idx[0] = j0; idx[1] = j1; val[0] = c0; val[1] = c1;
  }
  __syncthreads();
}
__global__ void k_gateup(const float* __restrict__ r, const u16* __restrict__ h, const u16* __restrict__ wg, const u16* __restrict__ wu, float* __restrict__ gu, int* __restrict__ sel) {
  __shared__ float sv[512]; __shared__ int si[512]; __shared__ int idx[2]; __shared__ float val[2];
  top2(r, idx, val, sv, si);
  if (blockIdx.x == 0 && threadIdx.x == 0) { sel[0] = idx[0]; sel[1] = idx[1]; ((float*)sel)[2] = val[0]; ((float*)sel)[3] = val[1]; }
  int f = blockIdx.x * 64 + (threadIdx.x & 63), part = threadIdx.x >> 6;
  float g0 = 0, g1 = 0, u0 = 0, u1 = 0;
  for (int d = part * 128; d < part * 128 + 128; ++d) {
    float a = bf2f(h[idx[0] * D + d]), b = bf2f(h[idx[1] * D + d]);
    float g = bf2f(wg[d * F + f]), u = bf2f(wu[d * F + f]);
    g0 += a * g; g1 += b * g; u0 += a * u; u1 += b * u;
  }
  __shared__ float red[4][4][64];
  red[part][0][threadIdx.x & 63] = g0; red[part][1][threadIdx.x & 63] = g1; red[part][2][threadIdx.x & 63] = u0; red[part][3][threadIdx.x & 63] = u1;
  __syncthreads();
  if (part < 2) {
    int l = threadIdx.x & 63;
    float g = red[0][part][l] + red[1][part][l] + red[2][part][l] + red[3][part][l];
    float u = red[0][2 + part][l] + red[1][2 + part][l] + red[2][2 + part][l] + red[3][2 + part][l];
    gu[part * F + f] = g / (1.f + __expf(-g)) * u;
  }
}
__global__ void k_down(const u16* __restrict__ x, const u16* __restrict__ h, const float* __restrict__ gu, const u16* __restrict__ wd, const int* __restrict__ sel, u16* __restrict__ xo) {
  int row = blockIdx.x, t = threadIdx.x;
  int c = row == sel[0] ? 0 : (row == sel[1] ? 1 : -1);
  float y = 0;
  if (c >= 0) {
    for (int f = 0; f < F; ++f) y += gu[c * F + f] * bf2f(wd[f * D + t]);
    float rv = ((const float*)sel)[2 + c];
    y *= 1.f / (1.f + __expf(-rv));
  }
  xo[row * D + t] = f2bf(bf2f(x[row * D + t]) + (bf2f(h[row * D + t]) + y));
}

extern "C" void kernel_launch(void* const* d_in, const int* in_sizes, int n_in, void* d_out, int out_size, void* d_ws, size_t ws_size, hipStream_t stream) {
  const u16* x0 = (const u16*)d_in[0];
  const u16* cr = (const u16*)d_in[1];
  const u16* ci = (const u16*)d_in[2];
  const u16* dre = (const u16*)d_in[3];
  const u16* dim_ = (const u16*)d_in[4];
  const u16* wr = (const u16*)d_in[6];
  const u16* wg = (const u16*)d_in[7];
  const u16* wu = (const u16*)d_in[8];
  const u16* wd = (const u16*)d_in[9];
  char* ws = (char*)d_ws;
  u16* h = (u16*)ws; u16* abT = (u16*)(ws + (2 << 20)); u16* x1 = (u16*)(ws + (6 << 20)); u16* x2 = (u16*)(ws + (8 << 20));
  float* r = (float*)(ws + (10 << 20)); float* gu = r + 4096; int* sel = (int*)(gu + 4096);
  const u16* xi = x0;
  for (int l = 0; l < 2; ++l) {
    k_ln<<<S / 4, 256, 0, stream>>>(xi, h, wr, r);
    k_gemm1<<<32 * 64 / 4, 256, 0, stream>>>(dre, dim_, h, abT);
    k_gemm2<<<64 * 16 / 4, 256, 0, stream>>>(cr, ci, abT, xi, x1);
    k_ln<<<S / 4, 256, 0, stream>>>(x1, h, wr, r);
    k_gateup<<<F / 64, 256, 0, stream>>>(r, h, wg, wu, gu, sel);
    u16* xo = l == 0 ? x2 : (u16*)d_out;
    k_down<<<S, 512, 0, stream>>>(x1, h, gu, wd, sel, xo);
    xi = xo;
  }
}
```

```cpp
#include <hip/hip_runtime.h>
#include <hip/hip_bf16.h>

struct Args { const void *x, *cr, *ci, *dre, *dim, *wr, *wg, *wu, *wd; void *out, *ws; };

__global__ void __launch_bounds__(256, 1) fnet_mod_kernel(Args args) {
  __shared__ __attribute__((aligned(16))) unsigned char lds[163840];
  unsigned tid = threadIdx.x, lbase = (unsigned)(size_t)lds;
  asm volatile(
"s_load_dwordx16 s[4:19], s[0:1], 0x0\n"
"s_load_dwordx4 s[20:23], s[0:1], 0x40\n"
"s_load_dwordx2 s[24:25], s[0:1], 0x50\n"
"v_lshrrev_b32 v1, 6, v0\n"
"v_and_b32 v127, 63, v0\n"
"v_readfirstlane_b32 s71, v1\n"
"v_lshlrev_b32 v126, 4, v127\n"
"s_mov_b32 s72, s2\n"
"s_and_b32 s85, s2, 7\n"
"s_lshr_b32 s84, s2, 3\n"
"s_and_b32 s70, s84, 7\n"
"s_lshr_b32 s73, s84, 3\n"
"s_lshl_b32 s69, s85, 2\n"
"s_add_u32 s69, s69, s73\n"
"s_lshl_b32 s81, s71, 11\n"
"s_lshl_b32 s80, s71, 12\n"
"s_lshl_b32 s75, s69, 16\n"
"s_lshl_b32 s73, s70, 13\n"
"s_add_u32 s75, s75, s73\n"
"s_add_u32 s75, s75, s81\n"
"s_and_b32 s73, s71, 1\n"
"s_lshl_b32 s73, s73, 17\n"
"s_lshl_b32 s78, s69, 18\n"
"s_add_u32 s78, s78, s73\n"
"s_and_b32 s73, s70, 3\n"
"s_mul_i32 s73, s73, 1024\n"
"s_add_u32 s2, s78, s73\n"
"v_lshrrev_b32 v1, 3, v127\n"
"v_lshlrev_b32 v1, 12, v1\n"
"v_and_b32 v2, 7, v127\n"
"v_lshrrev_b32 v3, 4, v127\n"
"v_xor_b32 v2, v2, v3\n"
"v_lshl_add_u32 v120, v2, 4, v1\n"
"v_xor_b32 v2, 64, v120\n"
"v_add_u32 v121, 31744, v2\n"
"v_add_u32 v122, 63488, v120\n"
"v_add_u32 v123, 95232, v2\n"
"s_mov_b32 s30, 0x200000\n"
"s_mov_b32 s31, 0x20000\n"
"s_mov_b32 s34, 0x800000\n"
"s_mov_b32 s35, 0x20000\n"
"s_cmp_lt_u32 s71, 2\n"
"s_waitcnt lgkmcnt(0)\n"
"s_mov_b32 s28, s4\n"
"s_cselect_b32 s32, s6, s8\n"
"s_cselect_b32 s33, s7, s9\n"
"s_cselect_b32 s83, 0, 0x80008000\n"
"s_and_b32 s29, s5, 0xffff\n"
"s_and_b32 s33, s33, 0xffff\n"
"buffer_load_dwordx4 v[32:35], v126, s[28:31], s75 offen\n"
"buffer_load_dwordx4 v[36:39], v126, s[28:31], s75 offen offset:1024\n"
"s_add_u32 s24, s24, 0x1fffff\n"
"s_addc_u32 s25, s25, 0\n"
"s_and_b32 s24, s24, 0xffe00000\n"
"s_load_dword s68, s[24:25], 0x0\n"
"s_add_u32 m0, s80, 32768\n"
"s_xor_b32 s73, s2, 256\n"
"buffer_load_dwordx4 v120, s[32:35], s73 offen offset:0 lds\n"
"buffer_load_dwordx4 v121, s[32:35], s73 offen offset:1024 lds\n"
"buffer_load_dwordx4 v122, s[32:35], s73 offen offset:2048 lds\n"
"buffer_load_dwordx4 v123, s[32:35], s73 offen offset:3072 lds\n"
"s_add_u32 m0, s80, 49152\n"
"s_xor_b32 s73, s2, 384\n"
"buffer_load_dwordx4 v120, s[32:35], s73 offen offset:0 lds\n"
"buffer_load_dwordx4 v121, s[32:35], s73 offen offset:1024 lds\n"
"buffer_load_dwordx4 v122, s[32:35], s73 offen offset:2048 lds\n"
"buffer_load_dwordx4 v123, s[32:35], s73 offen offset:3072 lds\n"
"s_mov_b32 s36, s24\n"
"s_and_b32 s37, s25, 0xffff\n"
"s_mov_b32 s38, 0x2800000\n"
"s_mov_b32 s39, 0x20000\n"
"s_mov_b32 s40, s10\n"
"s_and_b32 s41, s11, 0xffff\n"
"s_mov_b32 s42, 0x80000\n"
"s_mov_b32 s43, 0x20000\n"
"s_mov_b32 s44, s12\n"
"s_and_b32 s45, s13, 0xffff\n"
"s_mov_b32 s46, 0x80000\n"
"s_mov_b32 s47, 0x20000\n"
"s_mov_b32 s64, s14\n"
"s_and_b32 s65, s15, 0xffff\n"
"s_mov_b32 s66, 0x400\n"
"s_mov_b32 s67, 0x20000\n"
"s_mov_b32 s52, s16\n"
"s_and_b32 s53, s17, 0xffff\n"
"s_mov_b32 s54, 0x1c0000\n"
"s_mov_b32 s55, 0x20000\n"
"s_mov_b32 s56, s18\n"
"s_and_b32 s57, s19, 0xffff\n"
"s_mov_b32 s58, 0x1c0000\n"
"s_mov_b32 s59, 0x20000\n"
"s_mov_b32 s60, s20\n"
"s_and_b32 s61, s21, 0xffff\n"
"s_mov_b32 s62, 0x1c0000\n"
"s_mov_b32 s63, 0x20000\n"
"s_mov_b32 s48, s22\n"
"s_and_b32 s49, s23, 0xffff\n"
"s_mov_b32 s50, 0x200000\n"
"s_mov_b32 s51, 0x20000\n"
"v_lshrrev_b32 v1, 4, v127\n"
"v_and_b32 v2, 15, v127\n"
"v_lshlrev_b32 v3, 4, v2\n"
"v_lshl_add_u32 v124, v1, 10, v3\n"
"v_lshrrev_b32 v3, 1, v2\n"
"v_xor_b32 v1, v1, v3\n"
"v_lshlrev_b32 v1, 4, v1\n"
"v_lshl_add_u32 v2, v2, 7, s80\n"
"s_mov_b32 s73, 0x10000\n"
"v_xor_b32 v3, 0, v1\n"
"v_add_u32 v44, v2, v3\n"
"v_add_u32 v116, s73, v44\n"
"v_add_u32 v118, s73, v116\n"
"v_xor_b32 v3, 64, v1\n"
"v_add_u32 v45, v2, v3\n"
"v_add_u32 v117, s73, v45\n"
"v_add_u32 v119, s73, v117\n"
"s_lshl_b32 s3, s69, 2\n"
"s_add_u32 s3, s3, s71\n"
"s_cmp_eq_u32 s71, 3\n"
"s_cselect_b64 s[92:93], -1, 0\n"
"s_lshl_b32 s79, s70, 18\n"
"s_add_u32 s79, s79, s81\n"
"s_add_u32 s79, s79, 0x200000\n"
"s_lshl_b32 s73, s84, 2\n"
"s_add_u32 s73, s73, s71\n"
"s_mul_i32 s73, s73, 512\n"
"v_lshl_add_u32 v40, v127, 7, s73\n"
"v_min_u32 v40, END_%=-PC1_%=, v40\n"
"s_getpc_b64 s[26:27]\n"
"PC1_%=:\n"
"s_mov_b64 exec, 15\n"
"global_load_dword v41, v40, s[26:27]\n"
"s_mov_b64 exec, -1\n"
"s_lshl_b32 s73, s72, 2\n"
"s_add_u32 s73, s73, s71\n"
"s_lshl_b32 s73, s73, 9\n"
"v_lshlrev_b32 v40, 7, v127\n"
"s_mov_b64 exec, 15\n"
"buffer_load_dword v41, v40, s[40:43], s73 offen\n"
"buffer_load_dword v41, v40, s[44:47], s73 offen\n"
"s_mov_b64 exec, -1\n"
"s_mul_i32 s96, s71, 2176\n"
"v_add_u32 v30, s96, v126\n"
"v_bfe_u32 v28, v127, 2, 2\n"
"v_mul_u32_u24 v28, 1088, v28\n"
"v_lshrrev_b32 v29, 5, v127\n"
"v_lshl_add_u32 v28, v29, 6, v28\n"
"v_and_b32 v29, 3, v127\n"
"v_lshl_add_u32 v28, v29, 4, v28\n"
"v_bfe_u32 v29, v127, 4, 1\n"
"v_lshl_add_u32 v28, v29, 3, v28\n"
"s_lshl_b32 s96, s71, 8\n"
"v_add_u32 v28, s96, v28\n"
"v_mov_b32 v20, 0x3b000000\n"
"s_lshl_b32 s76, s69, 3\n"
"s_add_u32 s76, s76, s70\n"
"s_lshl_b32 s74, s76, 10\n"
"s_lshl_b32 s75, s71, 19\n"
"s_add_u32 s74, s74, s75\n"
"s_add_u32 s74, s74, 0x200000\n"
"s_add_u32 s75, s74, 0x40000\n"
"s_waitcnt vmcnt(11)\n"
"v_lshlrev_b32 v0, 16, v32\n"
"v_and_b32 v1, 0xffff0000, v32\n"
"v_lshlrev_b32 v2, 16, v33\n"
"v_and_b32 v3, 0xffff0000, v33\n"
"v_lshlrev_b32 v4, 16, v34\n"
"v_and_b32 v5, 0xffff0000, v34\n"
"v_lshlrev_b32 v6, 16, v35\n"
"v_and_b32 v7, 0xffff0000, v35\n"
"v_lshlrev_b32 v8, 16, v36\n"
"v_and_b32 v9, 0xffff0000, v36\n"
"v_lshlrev_b32 v10, 16, v37\n"
"v_and_b32 v11, 0xffff0000, v37\n"
"s_add_u32 m0, s80, 65536\n"
"s_xor_b32 s73, s2, 512\n"
"buffer_load_dwordx4 v120, s[32:35], s73 offen offset:0 lds\n"
"v_lshlrev_b32 v12, 16, v38\n"
"v_and_b32 v13, 0xffff0000, v38\n"
"v_lshlrev_b32 v14, 16, v39\n"
"v_and_b32 v15, 0xffff0000, v39\n"
"v_add_f32 v16, v0, v1\n"
"v_mul_f32 v17, v0, v0\n"
"v_add_f32 v18, v8, v9\n"
"v_mul_f32 v19, v8, v8\n"
"v_fmac_f32 v17, v1, v1\n"
"v_fmac_f32 v19, v9, v9\n"
"v_add_f32 v16, v16, v2\n"
"v_fmac_f32 v17, v2, v2\n"
"buffer_load_dwordx4 v121, s[32:35], s73 offen offset:1024 lds\n"
"v_add_f32 v18, v18, v10\n"
"v_fmac_f32 v19, v10, v10\n"
"v_add_f32 v16, v16, v3\n"
"v_fmac_f32 v17, v3, v3\n"
"v_add_f32 v18, v18, v11\n"
"v_fmac_f32 v19, v11, v11\n"
"v_add_f32 v16, v16, v4\n"
"v_fmac_f32 v17, v4, v4\n"
"v_add_f32 v18, v18, v12\n"
"v_fmac_f32 v19, v12, v12\n"
"v_add_f32 v16, v16, v5\n"
"v_fmac_f32 v17, v5, v5\n"
"buffer_load_dwordx4 v122, s[32:35], s73 offen offset:2048 lds\n"
"v_add_f32 v18, v18, v13\n"
"v_fmac_f32 v19, v13, v13\n"
"v_add_f32 v16, v16, v6\n"
"v_fmac_f32 v17, v6, v6\n"
"v_add_f32 v18, v18, v14\n"
"v_fmac_f32 v19, v14, v14\n"
"v_add_f32 v16, v16, v7\n"
"v_fmac_f32 v17, v7, v7\n"
"v_add_f32 v18, v18, v15\n"
"v_fmac_f32 v19, v15, v15\n"
"v_add_f32_dpp v16, v16, v16 row_shr:1 row_mask:0xf bank_mask:0xf bound_ctrl:1\n"
"v_add_f32_dpp v17, v17, v17 row_shr:1 row_mask:0xf bank_mask:0xf bound_ctrl:1\n"
"buffer_load_dwordx4 v123, s[32:35], s73 offen offset:3072 lds\n"
"v_add_f32_dpp v18, v18, v18 row_shr:1 row_mask:0xf bank_mask:0xf bound_ctrl:1\n"
"v_add_f32_dpp v19, v19, v19 row_shr:1 row_mask:0xf bank_mask:0xf bound_ctrl:1\n"
"v_add_f32_dpp v16, v16, v16 row_shr:2 row_mask:0xf bank_mask:0xf bound_ctrl:1\n"
"v_add_f32_dpp v17, v17, v17 row_shr:2 row_mask:0xf bank_mask:0xf bound_ctrl:1\n"
"v_add_f32_dpp v18, v18, v18 row_shr:2 row_mask:0xf bank_mask:0xf bound_ctrl:1\n"
"v_add_f32_dpp v19, v19, v19 row_shr:2 row_mask:0xf bank_mask:0xf bound_ctrl:1\n"
"v_add_f32_dpp v16, v16, v16 row_shr:4 row_mask:0xf bank_mask:0xf bound_ctrl:1\n"
"v_add_f32_dpp v17, v17, v17 row_shr:4 row_mask:0xf bank_mask:0xf bound_ctrl:1\n"
"v_add_f32_dpp v18, v18, v18 row_shr:4 row_mask:0xf bank_mask:0xf bound_ctrl:1\n"
"v_add_f32_dpp v19, v19, v19 row_shr:4 row_mask:0xf bank_mask:0xf bound_ctrl:1\n"
"v_add_f32_dpp v16, v16, v16 row_shr:8 row_mask:0xf bank_mask:0xf bound_ctrl:1\n"
"v_add_f32_dpp v17, v17, v17 row_shr:8 row_mask:0xf bank_mask:0xf bound_ctrl:1\n"
"s_add_u32 m0, s80, 81920\n"
"s_xor_b32 s73, s2, 640\n"
"buffer_load_dwordx4 v120, s[32:35], s73 offen offset:0 lds\n"
"v_add_f32_dpp v18, v18, v18 row_shr:8 row_mask:0xf bank_mask:0xf bound_ctrl:1\n"
"v_add_f32_dpp v19, v19, v19 row_shr:8 row_mask:0xf bank_mask:0xf bound_ctrl:1\n"
"v_add_f32_dpp v16, v16, v16 row_bcast:15 row_mask:0xa bank_mask:0xf\n"
"v_add_f32_dpp v17, v17, v17 row_bcast:15 row_mask:0xa bank_mask:0xf\n"
"v_add_f32_dpp v18, v18, v18 row_bcast:15 row_mask:0xa bank_mask:0xf\n"
"v_add_f32_dpp v19, v19, v19 row_bcast:15 row_mask:0xa bank_mask:0xf\n"
"v_add_f32_dpp v16, v16, v16 row_bcast:31 row_mask:0xc bank_mask:0xf\n"
"v_add_f32_dpp v17, v17, v17 row_bcast:31 row_mask:0xc bank_mask:0xf\n"
"v_add_f32_dpp v18, v18, v18 row_bcast:31 row_mask:0xc bank_mask:0xf\n"
"v_add_f32_dpp v19, v19, v19 row_bcast:31 row_mask:0xc bank_mask:0xf\n"
"v_readlane_b32 s96, v16, 63\n"
"v_readlane_b32 s97, v17, 63\n"
"buffer_load_dwordx4 v121, s[32:35], s73 offen offset:1024 lds\n"
"v_readlane_b32 s98, v18, 63\n"
"v_readlane_b32 s99, v19, 63\n"
"v_mul_f32 v16, s96, v20\n"
"v_mul_f32 v17, s97, v20\n"
"v_mul_f32 v18, s98, v20\n"
"v_mul_f32 v19, s99, v20\n"
"v_fma_f32 v17, -v16, v16, v17\n"
"v_fma_f32 v19, -v18, v18, v19\n"
"v_max_f32 v17, 0, v17\n"
"v_max_f32 v19, 0, v19\n"
"v_add_f32 v17, 0x3727c5ac, v17\n"
"v_add_f32 v19, 0x3727c5ac, v19\n"
"buffer_load_dwordx4 v122, s[32:35], s73 offen offset:2048 lds\n"
"v_rsq_f32 v17, v17\n"
"v_rsq_f32 v19, v19\n"
"v_sub_f32 v0, v0, v16\n"
"v_sub_f32 v1, v1, v16\n"
"v_sub_f32 v2, v2, v16\n"
"v_sub_f32 v3, v3, v16\n"
"v_sub_f32 v4, v4, v16\n"
"v_sub_f32 v5, v5, v16\n"
"v_sub_f32 v6, v6, v16\n"
"v_sub_f32 v7, v7, v16\n"
"v_sub_f32 v8, v8, v18\n"
"v_sub_f32 v9, v9, v18\n"
"buffer_load_dwordx4 v123, s[32:35], s73 offen offset:3072 lds\n"
"v_sub_f32 v10, v10, v18\n"
"v_sub_f32 v11, v11, v18\n"
"v_sub_f32 v12, v12, v18\n"
"v_sub_f32 v13, v13, v18\n"
"v_sub_f32 v14, v14, v18\n"
"v_sub_f32 v15, v15, v18\n"
"v_mul_f32 v0, v0, v17\n"
"v_mul_f32 v1, v1, v17\n"
"v_mul_f32 v2, v2, v17\n"
"v_mul_f32 v3, v3, v17\n"
"v_mul_f32 v4, v4, v17\n"
"v_mul_f32 v5, v5, v17\n"
"s_add_u32 m0, s80, 98304\n"
"s_xor_b32 s73, s2, 768\n"
"buffer_load_dwordx4 v120, s[32:35], s73 offen offset:0 lds\n"
"v_mul_f32 v6, v6, v17\n"
"v_mul_f32 v7, v7, v17\n"
"v_mul_f32 v8, v8, v19\n"
"v_mul_f32 v9, v9, v19\n"
"v_mul_f32 v10, v10, v19\n"
"v_mul_f32 v11, v11, v19\n"
"v_mul_f32 v12, v12, v19\n"
"v_mul_f32 v13, v13, v19\n"
"v_mul_f32 v14, v14, v19\n"
"v_mul_f32 v15, v15, v19\n"
"v_cvt_pk_bf16_f32 v32, v0, v1\n"
"v_cvt_pk_bf16_f32 v33, v2, v3\n"
"buffer_load_dwordx4 v121, s[32:35], s73 offen offset:1024 lds\n"
"v_cvt_pk_bf16_f32 v34, v4, v5\n"
"v_cvt_pk_bf16_f32 v35, v6, v7\n"
"v_cvt_pk_bf16_f32 v36, v8, v9\n"
"v_cvt_pk_bf16_f32 v37, v10, v11\n"
"v_cvt_pk_bf16_f32 v38, v12, v13\n"
"v_cvt_pk_bf16_f32 v39, v14, v15\n"
"ds_write_b128 v30, v[32:35]\n"
"ds_write_b128 v30, v[36:39] offset:1088\n"
"s_waitcnt lgkmcnt(0)\n"
"s_add_u32 s68, s68, 1\n"
"s_and_b32 s0, s68, 0xff\n"
"s_barrier\n"
"ds_read_b64_tr_b16 v[32:33], v28 offset:0\n"
"ds_read_b64_tr_b16 v[34:35], v28 offset:4352\n"
"ds_read_b64_tr_b16 v[36:37], v28 offset:128\n"
"ds_read_b64_tr_b16 v[38:39], v28 offset:4480\n"
"s_waitcnt lgkmcnt(2)\n"
"buffer_store_dwordx4 v[32:35], v126, s[36:39], s74 offen sc1\n"
"s_waitcnt lgkmcnt(0)\n"
"buffer_store_dwordx4 v[36:39], v126, s[36:39], s75 offen sc1\n"
"buffer_load_dwordx4 v122, s[32:35], s73 offen offset:2048 lds\n"
"buffer_load_dwordx4 v123, s[32:35], s73 offen offset:3072 lds\n"
"s_and_b32 s24, s2, 0xfff\n"
"s_lshl_b32 s24, s24, 6\n"
"s_add_u32 s24, s24, s79\n"
"s_lshl_b32 s25, s69, 3\n"
"s_add_u32 s25, s25, s70\n"
"s_lshl_b32 s75, s85, 14\n"
"s_add_u32 s75, s75, s80\n"
"s_add_u32 s75, s75, 0x20000\n"
"v_lshlrev_b32 v34, 6, v127\n"
"s_add_u32 m0, s80, 114688\n"
"s_xor_b32 s73, s2, 896\n"
"buffer_load_dwordx4 v120, s[32:35], s73 offen offset:0 lds\n"
"buffer_load_dwordx4 v121, s[32:35], s73 offen offset:1024 lds\n"
"buffer_load_dwordx4 v122, s[32:35], s73 offen offset:2048 lds\n"
"buffer_load_dwordx4 v123, s[32:35], s73 offen offset:3072 lds\n"
"v_lshrrev_b32 v64, 3, v127\n"
"v_lshlrev_b32 v64, 10, v64\n"
"v_and_b32 v65, 0x70, v126\n"
"v_add_u32 v64, v64, v65\n"
"s_lshl_b32 s96, s3, 14\n"
"s_lshl_b32 s99, s70, 7\n"
"s_add_u32 s96, s96, s99\n"
"s_add_u32 s97, s96, 7168\n"
"s_add_u32 m0, s80, 16384\n"
"s_nop 0\n"
"buffer_load_dwordx4 v64, s[28:31], s96 offen lds\n"
"buffer_load_dwordx4 v64, s[28:31], s97 offen offset:1024 lds\n"
"s_add_u32 m0, s80, 18432\n"
"s_nop 0\n"
"buffer_load_dwordx4 v126, s[64:67], 0 offen lds\n"
"s_add_u32 m0, s80, 131072\n"
"s_xor_b32 s73, s2, 0\n"
"buffer_load_dwordx4 v120, s[32:35], s73 offen offset:0 lds\n"
"buffer_load_dwordx4 v121, s[32:35], s73 offen offset:1024 lds\n"
"buffer_load_dwordx4 v122, s[32:35], s73 offen offset:2048 lds\n"
"buffer_load_dwordx4 v123, s[32:35], s73 offen offset:3072 lds\n"
"s_lshl_b32 s88, s72, 1\n"
"s_lshr_b32 s96, s71, 1\n"
"s_add_u32 s88, s88, s96\n"
"s_and_b32 s96, s88, 0x1c0\n"
"s_add_u32 s88, s88, s96\n"
"s_lshl_b32 s88, s88, 7\n"
"s_and_b32 s96, s71, 1\n"
"s_lshl_b32 s96, s96, 13\n"
"s_add_u32 s88, s88, s96\n"
"s_add_u32 s88, s88, 0xe0000\n"
"s_waitcnt lgkmcnt(0)\n"
"s_memtime s[90:91]\n"
"s_waitcnt lgkmcnt(0)\n"
"s_buffer_load_dword s96, s[36:39], s88\n"
"s_getreg_b32 s86, hwreg(HW_REG_XCC_ID, 2, 1)\n"
"s_lshl_b32 s97, s71, 2\n"
"v_mov_b32 v26, s97\n"
"s_xor_b32 s97, s86, s71\n"
"s_waitcnt lgkmcnt(0)\n"
"s_memtime s[26:27]\n"
"s_waitcnt lgkmcnt(0)\n"
"s_sub_u32 s87, s26, s90\n"
"s_min_u32 s87, s87, 2500\n"
"s_sub_u32 s96, 0, s87\n"
"s_bitcmp1_b32 s97, 0\n"
"s_cselect_b32 s87, s87, s96\n"
"v_mov_b32 v27, s87\n"
"ds_write_b32 v26, v27 offset:12288\n"
"s_waitcnt lgkmcnt(0)\n"
"s_add_u32 m0, s80, 147456\n"
"s_xor_b32 s73, s2, 128\n"
"buffer_load_dwordx4 v120, s[32:35], s73 offen offset:0 lds\n"
"buffer_load_dwordx4 v121, s[32:35], s73 offen offset:1024 lds\n"
"buffer_load_dwordx4 v122, s[32:35], s73 offen offset:2048 lds\n"
"buffer_load_dwordx4 v123, s[32:35], s73 offen offset:3072 lds\n"
"s_waitcnt vmcnt(36)\n"
"ds_read_b128 v[128:131], v44 offset:32768\n"
"ds_read_b128 v[132:135], v44 offset:34816\n"
"ds_read_b128 v[136:139], v45 offset:32768\n"
"ds_read_b128 v[140:143], v45 offset:34816\n"
"s_waitcnt lgkmcnt(0)\n"
"s_add_u32 m0, s80, 32768\n"
"s_xor_b32 s73, s2, 1280\n"
"buffer_load_dwordx4 v120, s[32:35], s73 offen offset:0 lds\n"
"buffer_load_dwordx4 v121, s[32:35], s73 offen offset:1024 lds\n"
"buffer_load_dwordx4 v122, s[32:35], s73 offen offset:2048 lds\n"
"buffer_load_dwordx4 v123, s[32:35], s73 offen offset:3072 lds\n"
"s_cmp_lg_u32 s71, 1\n"
"s_cbranch_scc1 R1_%=\n"
"s_sub_u32 s96, s68, 1\n"
"v_mov_b32 v20, s96\n"
"v_mov_b32 v21, s96\n"
"v_mov_b32 v22, s96\n"
"v_mov_b32 v23, s96\n"
"v_and_b32 v24, 48, v126\n"
"s_lshl_b32 s96, s85, 14\n"
"s_lshl_b32 s97, s84, 6\n"
"s_add_u32 s96, s96, s97\n"
"s_add_u32 s96, s96, 0xc0000\n"
"v_bfe_u32 v25, v127, 2, 1\n"
"v_lshl_add_u32 v24, v25, 11, v24\n"
"v_bfe_u32 v25, v127, 3, 1\n"
"v_lshl_add_u32 v24, v25, 13, v24\n"
"s_mov_b64 exec, 0xffff\n"
"buffer_store_dwordx4 v[20:23], v24, s[36:39], s96 offen sc1\n"
"s_mov_b64 exec, -1\n"
"R1_%=:\n"
"s_waitcnt vmcnt(36)\n"
"ds_read_b128 v[144:147], v44 offset:49152\n"
"ds_read_b128 v[148:151], v44 offset:51200\n"
"ds_read_b128 v[152:155], v45 offset:49152\n"
"ds_read_b128 v[156:159], v45 offset:51200\n"
"s_waitcnt lgkmcnt(0)\n"
"s_add_u32 m0, s80, 49152\n"
"s_xor_b32 s73, s2, 1408\n"
"buffer_load_dwordx4 v120, s[32:35], s73 offen offset:0 lds\n"
"buffer_load_dwordx4 v121, s[32:35], s73 offen offset:1024 lds\n"
"buffer_load_dwordx4 v122, s[32:35], s73 offen offset:2048 lds\n"
"buffer_load_dwordx4 v123, s[32:35], s73 offen offset:3072 lds\n"
"s_lshl_b32 s99, s25, 6\n"
"s_add_u32 s99, s99, 0x20000\n"
"s_cmp_eq_u32 s71, 0\n"
"s_cselect_b32 s99, s99, 0x2800000\n"
"v_lshlrev_b32 v59, 14, v127\n"
"v_mov_b32 v60, s68\n"
"s_waitcnt vmcnt(25)\n"
"v_mov_b32 v26, 0\n"
"s_barrier\n"
"ds_read_b128 v[24:27], v26 offset:12288\n"
"s_waitcnt lgkmcnt(0)\n"
"v_add_u32 v24, v24, v25\n"
"v_add3_u32 v24, v24, v26, v27\n"
"v_lshl_or_b32 v61, v24, 1, s86\n"
"s_mov_b64 exec, 0xff\n"
"buffer_store_dwordx2 v[60:61], v59, s[36:39], s99 offen sc1\n"
"s_mov_b64 exec, -1\n"
"ds_read_b128 v[160:163], v116 offset:0\n"
"ds_read_b128 v[164:167], v116 offset:2048\n"
"ds_read_b128 v[168:171], v117 offset:0\n"
"ds_read_b128 v[172:175], v117 offset:2048\n"
"s_waitcnt lgkmcnt(0)\n"
"s_add_u32 m0, s80, 65536\n"
"s_xor_b32 s73, s2, 1536\n"
"buffer_load_dwordx4 v120, s[32:35], s73 offen offset:0 lds\n"
"buffer_load_dwordx4 v121, s[32:35], s73 offen offset:1024 lds\n"
"buffer_load_dwordx4 v122, s[32:35], s73 offen offset:2048 lds\n"
"buffer_load_dwordx4 v123, s[32:35], s73 offen offset:3072 lds\n"
"v_and_b32 v64, 15, v127\n"
"v_lshlrev_b32 v64, 7, v64\n"
"v_and_b32 v65, 48, v127\n"
"v_add3_u32 v64, v64, v65, s80\n"
"s_lshl_b32 s96, s70, 7\n"
"s_add_u32 s96, s96, s80\n"
"v_add_u32 v65, s96, v65\n"
"v_add_u32 v66, s80, v126\n"
"s_waitcnt vmcnt(21)\n"
"ds_read_b128 v[48:51], v64 offset:16384\n"
"ds_read_b128 v[80:83], v65 offset:18432\n"
"ds_read_b128 v[52:55], v64 offset:16448\n"
"ds_read_b128 v[84:87], v65 offset:18496\n"
"ds_read_b128 v[56:59], v66 offset:18432\n"
"s_waitcnt lgkmcnt(0)\n"
"v_lshlrev_b32 v96, 16, v48\n"
"v_and_b32 v97, 0xffff0000, v48\n"
"v_lshlrev_b32 v98, 16, v49\n"
"v_and_b32 v99, 0xffff0000, v49\n"
"v_lshlrev_b32 v100, 16, v50\n"
"v_and_b32 v101, 0xffff0000, v50\n"
"v_lshlrev_b32 v102, 16, v51\n"
"v_and_b32 v103, 0xffff0000, v51\n"
"v_lshlrev_b32 v104, 16, v52\n"
"v_and_b32 v105, 0xffff0000, v52\n"
"v_lshlrev_b32 v106, 16, v53\n"
"v_and_b32 v107, 0xffff0000, v53\n"
"v_lshlrev_b32 v108, 16, v54\n"
"v_and_b32 v109, 0xffff0000, v54\n"
"v_lshlrev_b32 v110, 16, v55\n"
"v_and_b32 v111, 0xffff0000, v55\n"
"v_lshlrev_b32 v16, 16, v56\n"
"v_and_b32 v17, 0xffff0000, v56\n"
"v_lshlrev_b32 v18, 16, v57\n"
"v_and_b32 v19, 0xffff0000, v57\n"
"v_lshlrev_b32 v20, 16, v58\n"
"v_and_b32 v21, 0xffff0000, v58\n"
"v_lshlrev_b32 v22, 16, v59\n"
"v_and_b32 v23, 0xffff0000, v59\n"
"v_add_f32 v16, v16, v17\n"
"v_add_f32 v16, v16, v18\n"
"v_add_f32 v16, v16, v19\n"
"v_add_f32 v16, v16, v20\n"
"v_add_f32 v16, v16, v21\n"
"v_add_f32 v16, v16, v22\n"
"v_add_f32 v16, v16, v23\n"
"s_nop 1\n"
"v_add_f32_dpp v16, v16, v16 row_shr:1 row_mask:0xf bank_mask:0xf bound_ctrl:1\n"
"s_nop 1\n"
"v_add_f32_dpp v16, v16, v16 row_shr:2 row_mask:0xf bank_mask:0xf bound_ctrl:1\n"
"s_nop 1\n"
"v_add_f32_dpp v16, v16, v16 row_shr:4 row_mask:0xf bank_mask:0xf bound_ctrl:1\n"
"s_nop 1\n"
"v_add_f32_dpp v16, v16, v16 row_shr:8 row_mask:0xf bank_mask:0xf bound_ctrl:1\n"
"s_nop 1\n"
"v_add_f32_dpp v16, v16, v16 row_bcast:15 row_mask:0xa bank_mask:0xf\n"
"s_nop 1\n"
"v_add_f32_dpp v16, v16, v16 row_bcast:31 row_mask:0xc bank_mask:0xf\n"
"s_nop 0\n"
"v_readlane_b32 s82, v16, 63\n"
"ds_read_b128 v[176:179], v116 offset:16384\n"
"ds_read_b128 v[180:183], v116 offset:18432\n"
"ds_read_b128 v[184:187], v117 offset:16384\n"
"ds_read_b128 v[188:191], v117 offset:18432\n"
"s_waitcnt lgkmcnt(0)\n"
"s_add_u32 m0, s80, 81920\n"
"s_xor_b32 s73, s2, 1664\n"
"buffer_load_dwordx4 v120, s[32:35], s73 offen offset:0 lds\n"
"buffer_load_dwordx4 v121, s[32:35], s73 offen offset:1024 lds\n"
"buffer_load_dwordx4 v122, s[32:35], s73 offen offset:2048 lds\n"
"buffer_load_dwordx4 v123, s[32:35], s73 offen offset:3072 lds\n"
"ds_read_b128 v[192:195], v116 offset:32768\n"
"ds_read_b128 v[196:199], v116 offset:34816\n"
"ds_read_b128 v[200:203], v117 offset:32768\n"
"ds_read_b128 v[204:207], v117 offset:34816\n"
"s_waitcnt lgkmcnt(0)\n"
"s_add_u32 m0, s80, 98304\n"
"s_xor_b32 s73, s2, 1792\n"
"buffer_load_dwordx4 v120, s[32:35], s73 offen offset:0 lds\n"
"buffer_load_dwordx4 v121, s[32:35], s73 offen offset:1024 lds\n"
"buffer_load_dwordx4 v122, s[32:35], s73 offen offset:2048 lds\n"
"buffer_load_dwordx4 v123, s[32:35], s73 offen offset:3072 lds\n"
"ds_read_b128 v[208:211], v116 offset:49152\n"
"ds_read_b128 v[212:215], v116 offset:51200\n"
"ds_read_b128 v[216:219], v117 offset:49152\n"
"ds_read_b128 v[220:223], v117 offset:51200\n"
"s_waitcnt lgkmcnt(0)\n"
"s_add_u32 m0, s80, 114688\n"
"s_xor_b32 s73, s2, 1920\n"
"buffer_load_dwordx4 v120, s[32:35], s73 offen offset:0 lds\n"
"buffer_load_dwordx4 v121, s[32:35], s73 offen offset:1024 lds\n"
"buffer_load_dwordx4 v122, s[32:35], s73 offen offset:2048 lds\n"
"buffer_load_dwordx4 v123, s[32:35], s73 offen offset:3072 lds\n"
"s_waitcnt vmcnt(21)\n"
"ds_read_b128 v[224:227], v44 offset:32768\n"
"ds_read_b128 v[228:231], v44 offset:34816\n"
"ds_read_b128 v[232:235], v45 offset:32768\n"
"ds_read_b128 v[236:239], v45 offset:34816\n"
"s_waitcnt lgkmcnt(0)\n"
"s_add_u32 m0, s80, 32768\n"
"s_xor_b32 s73, s2, 2304\n"
"buffer_load_dwordx4 v120, s[32:35], s73 offen offset:0 lds\n"
"buffer_load_dwordx4 v121, s[32:35], s73 offen offset:1024 lds\n"
"buffer_load_dwordx4 v122, s[32:35], s73 offen offset:2048 lds\n"
"buffer_load_dwordx4 v123, s[32:35], s73 offen offset:3072 lds\n"
"s_waitcnt vmcnt(21)\n"
"ds_read_b128 v[240:243], v44 offset:49152\n"
"ds_read_b128 v[244:247], v44 offset:51200\n"
"ds_read_b128 v[248:251], v45 offset:49152\n"
"ds_read_b128 v[252:255], v45 offset:51200\n"
"s_waitcnt lgkmcnt(0)\n"
"s_add_u32 m0, s80, 49152\n"
"s_xor_b32 s73, s2, 2432\n"
"buffer_load_dwordx4 v120, s[32:35], s73 offen offset:0 lds\n"
"buffer_load_dwordx4 v121, s[32:35], s73 offen offset:1024 lds\n"
"buffer_load_dwordx4 v122, s[32:35], s73 offen offset:2048 lds\n"
"buffer_load_dwordx4 v123, s[32:35], s73 offen offset:3072 lds\n"
"s_waitcnt vmcnt(20)\n"
"ds_read_b128 a[0:3], v116 offset:0\n"
"ds_read_b128 a[4:7], v116 offset:2048\n"
"ds_read_b128 a[8:11], v117 offset:0\n"
"ds_read_b128 a[12:15], v117 offset:2048\n"
"s_waitcnt lgkmcnt(0)\n"
"s_add_u32 m0, s80, 65536\n"
"s_xor_b32 s73, s2, 2560\n"
"buffer_load_dwordx4 v120, s[32:35], s73 offen offset:0 lds\n"
"buffer_load_dwordx4 v121, s[32:35], s73 offen offset:1024 lds\n"
"buffer_load_dwordx4 v122, s[32:35], s73 offen offset:2048 lds\n"
"buffer_load_dwordx4 v123, s[32:35], s73 offen offset:3072 lds\n"
"s_waitcnt vmcnt(20)\n"
"ds_read_b128 a[16:19], v116 offset:16384\n"
"ds_read_b128 a[20:23], v116 offset:18432\n"
"ds_read_b128 a[24:27], v117 offset:16384\n"
"ds_read_b128 a[28:31], v117 offset:18432\n"
"s_waitcnt lgkmcnt(0)\n"
"s_add_u32 m0, s80, 81920\n"
"s_xor_b32 s73, s2, 2688\n"
"buffer_load_dwordx4 v120, s[32:35], s73 offen offset:0 lds\n"
"buffer_load_dwordx4 v121, s[32:35], s73 offen offset:1024 lds\n"
"buffer_load_dwordx4 v122, s[32:35], s73 offen offset:2048 lds\n"
"buffer_load_dwordx4 v123, s[32:35], s73 offen offset:3072 lds\n"
"s_waitcnt vmcnt(20)\n"
"ds_read_b128 a[32:35], v116 offset:32768\n"
"ds_read_b128 a[36:39], v116 offset:34816\n"
"ds_read_b128 a[40:43], v117 offset:32768\n"
"ds_read_b128 a[44:47], v117 offset:34816\n"
"s_waitcnt lgkmcnt(0)\n"
"s_add_u32 m0, s80, 98304\n"
"s_xor_b32 s73, s2, 2816\n"
"buffer_load_dwordx4 v120, s[32:35], s73 offen offset:0 lds\n"
"buffer_load_dwordx4 v121, s[32:35], s73 offen offset:1024 lds\n"
"buffer_load_dwordx4 v122, s[32:35], s73 offen offset:2048 lds\n"
"buffer_load_dwordx4 v123, s[32:35], s73 offen offset:3072 lds\n"
"s_waitcnt vmcnt(20)\n"
"ds_read_b128 a[48:51], v116 offset:49152\n"
"ds_read_b128 a[52:55], v116 offset:51200\n"
"ds_read_b128 a[56:59], v117 offset:49152\n"
"ds_read_b128 a[60:63], v117 offset:51200\n"
"s_waitcnt lgkmcnt(0)\n"
"s_add_u32 m0, s80, 114688\n"
"s_xor_b32 s73, s2, 2944\n"
"buffer_load_dwordx4 v120, s[32:35], s73 offen offset:0 lds\n"
"buffer_load_dwordx4 v121, s[32:35], s73 offen offset:1024 lds\n"
"buffer_load_dwordx4 v122, s[32:35], s73 offen offset:2048 lds\n"
"buffer_load_dwordx4 v123, s[32:35], s73 offen offset:3072 lds\n"
"s_waitcnt vmcnt(20)\n"
"ds_read_b128 a[64:67], v44 offset:32768\n"
"ds_read_b128 a[68:71], v44 offset:34816\n"
"ds_read_b128 a[72:75], v45 offset:32768\n"
"ds_read_b128 a[76:79], v45 offset:34816\n"
"s_waitcnt lgkmcnt(0)\n"
"s_add_u32 m0, s80, 32768\n"
"s_xor_b32 s73, s2, 3328\n"
"buffer_load_dwordx4 v120, s[32:35], s73 offen offset:0 lds\n"
"buffer_load_dwordx4 v121, s[32:35], s73 offen offset:1024 lds\n"
"buffer_load_dwordx4 v122, s[32:35], s73 offen offset:2048 lds\n"
"buffer_load_dwordx4 v123, s[32:35], s73 offen offset:3072 lds\n"
"s_waitcnt vmcnt(20)\n"
"ds_read_b128 a[80:83], v44 offset:49152\n"
"ds_read_b128 a[84:87], v44 offset:51200\n"
"ds_read_b128 a[88:91], v45 offset:49152\n"
"ds_read_b128 a[92:95], v45 offset:51200\n"
"s_waitcnt lgkmcnt(0)\n"
"s_add_u32 m0, s80, 49152\n"
"s_xor_b32 s73, s2, 3456\n"
"buffer_load_dwordx4 v120, s[32:35], s73 offen offset:0 lds\n"
"buffer_load_dwordx4 v121, s[32:35], s73 offen offset:1024 lds\n"
"buffer_load_dwordx4 v122, s[32:35], s73 offen offset:2048 lds\n"
"buffer_load_dwordx4 v123, s[32:35], s73 offen offset:3072 lds\n"
"buffer_load_dwordx2 v[62:63], v34, s[36:39], s75 offen sc1\n"
"s_waitcnt vmcnt(21)\n"
"ds_read_b128 a[96:99], v116 offset:0\n"
"ds_read_b128 a[100:103], v116 offset:2048\n"
"ds_read_b128 a[104:107], v117 offset:0\n"
"ds_read_b128 a[108:111], v117 offset:2048\n"
"s_waitcnt lgkmcnt(0)\n"
"s_add_u32 m0, s80, 65536\n"
"s_xor_b32 s73, s2, 3584\n"
"buffer_load_dwordx4 v120, s[32:35], s73 offen offset:0 lds\n"
"buffer_load_dwordx4 v121, s[32:35], s73 offen offset:1024 lds\n"
"buffer_load_dwordx4 v122, s[32:35], s73 offen offset:2048 lds\n"
"buffer_load_dwordx4 v123, s[32:35], s73 offen offset:3072 lds\n"
"s_waitcnt vmcnt(21)\n"
"ds_read_b128 a[112:115], v116 offset:16384\n"
"ds_read_b128 a[116:119], v116 offset:18432\n"
"ds_read_b128 a[120:123], v117 offset:16384\n"
"ds_read_b128 a[124:127], v117 offset:18432\n"
"s_waitcnt lgkmcnt(0)\n"
"s_add_u32 m0, s80, 81920\n"
"s_xor_b32 s73, s2, 3712\n"
"buffer_load_dwordx4 v120, s[32:35], s73 offen offset:0 lds\n"
"buffer_load_dwordx4 v121, s[32:35], s73 offen offset:1024 lds\n"
"buffer_load_dwordx4 v122, s[32:35], s73 offen offset:2048 lds\n"
"buffer_load_dwordx4 v123, s[32:35], s73 offen offset:3072 lds\n"
"s_waitcnt vmcnt(21)\n"
"ds_read_b128 a[128:131], v116 offset:32768\n"
"ds_read_b128 a[132:135], v116 offset:34816\n"
"ds_read_b128 a[136:139], v117 offset:32768\n"
"ds_read_b128 a[140:143], v117 offset:34816\n"
"s_waitcnt lgkmcnt(0)\n"
"s_add_u32 m0, s80, 98304\n"
"s_xor_b32 s73, s2, 3840\n"
"buffer_load_dwordx4 v120, s[32:35], s73 offen offset:0 lds\n"
"buffer_load_dwordx4 v121, s[32:35], s73 offen offset:1024 lds\n"
"buffer_load_dwordx4 v122, s[32:35], s73 offen offset:2048 lds\n"
"buffer_load_dwordx4 v123, s[32:35], s73 offen offset:3072 lds\n"
"s_waitcnt vmcnt(21)\n"
"ds_read_b128 a[144:147], v116 offset:49152\n"
"ds_read_b128 a[148:151], v116 offset:51200\n"
"ds_read_b128 a[152:155], v117 offset:49152\n"
"ds_read_b128 a[156:159], v117 offset:51200\n"
"s_waitcnt lgkmcnt(0)\n"
"s_add_u32 m0, s80, 114688\n"
"s_xor_b32 s73, s2, 3968\n"
"buffer_load_dwordx4 v120, s[32:35], s73 offen offset:0 lds\n"
"buffer_load_dwordx4 v121, s[32:35], s73 offen offset:1024 lds\n"
"buffer_load_dwordx4 v122, s[32:35], s73 offen offset:2048 lds\n"
"buffer_load_dwordx4 v123, s[32:35], s73 offen offset:3072 lds\n"
"s_waitcnt vmcnt(21)\n"
"ds_read_b128 a[160:163], v44 offset:32768\n"
"ds_read_b128 a[164:167], v44 offset:34816\n"
"ds_read_b128 a[168:171], v45 offset:32768\n"
"ds_read_b128 a[172:175], v45 offset:34816\n"
"s_waitcnt vmcnt(17) lgkmcnt(0)\n"
"ds_read_b128 a[176:179], v44 offset:49152\n"
"ds_read_b128 a[180:183], v44 offset:51200\n"
"ds_read_b128 a[184:187], v45 offset:49152\n"
"ds_read_b128 a[188:191], v45 offset:51200\n"
"s_waitcnt vmcnt(16)\n"
"v_cmp_ne_u32 vcc, s68, v62\n"
"s_cbranch_vccnz S3_%=\n"
"B4_%=:\n"
"v_readlane_b32 s96, v63, 0\n"
"v_readlane_b32 s97, v63, 32\n"
"v_ashrrev_i32 v63, 1, v63\n"
"s_and_b32 s96, s96, 1\n"
"s_and_b32 s97, s97, 1\n"
"v_add_u32_dpp v63, v63, v63 row_shr:1 row_mask:0xf bank_mask:0xf bound_ctrl:1\n"
"s_lshl_b32 s97, s97, 1\n"
"s_or_b32 s96, s96, s97\n"
"v_add_u32_dpp v63, v63, v63 row_shr:2 row_mask:0xf bank_mask:0xf bound_ctrl:1\n"
"s_lshl_b32 s97, s71, 1\n"
"s_lshl_b32 s96, s96, s97\n"
"v_add_u32_dpp v63, v63, v63 row_shr:4 row_mask:0xf bank_mask:0xf bound_ctrl:1\n"
"s_lshl_b32 s97, s71, 2\n"
"v_mov_b32 v62, s97\n"
"v_add_u32_dpp v63, v63, v63 row_shr:8 row_mask:0xf bank_mask:0xf bound_ctrl:1\n"
"s_nop 1\n"
"v_add_u32_dpp v63, v63, v63 row_bcast:15 row_mask:0xa bank_mask:0xf\n"
"s_nop 1\n"
"v_add_u32_dpp v63, v63, v63 row_bcast:31 row_mask:0xc bank_mask:0xf\n"
"s_nop 0\n"
"v_readlane_b32 s98, v63, 63\n"
"s_nop 0\n"
"s_lshl_b32 s98, s98, 8\n"
"s_add_u32 s96, s96, s98\n"
"v_mov_b32 v63, s96\n"
"ds_write_b32 v62, v63 offset:32752\n"
"v_mov_b32 v62, 0\n"
"s_waitcnt lgkmcnt(0)\n"
"s_barrier\n"
"ds_read_b128 v[88:91], v62 offset:32752\n"
"s_add_u32 m0, s81, 0\n"
"s_xor_b32 s74, s24, 0\n"
"buffer_load_dwordx4 v126, s[36:39], s74 offen offset:0 sc1 lds\n"
"buffer_load_dwordx4 v126, s[36:39], s74 offen offset:1024 sc1 lds\n"
"s_add_u32 m0, s81, 8192\n"
"s_xor_b32 s74, s24, 8192\n"
"buffer_load_dwordx4 v126, s[36:39], s74 offen offset:0 sc1 lds\n"
"buffer_load_dwordx4 v126, s[36:39], s74 offen offset:1024 sc1 lds\n"
"s_waitcnt vmcnt(16) lgkmcnt(0)\n"
"ds_read_b128 a[192:195], v116 offset:0\n"
"ds_read_b128 a[196:199], v116 offset:2048\n"
"ds_read_b128 a[200:203], v117 offset:0\n"
"ds_read_b128 a[204:207], v117 offset:2048\n"
"s_add_u32 m0, s81, 16384\n"
"s_xor_b32 s74, s24, 16384\n"
"buffer_load_dwordx4 v126, s[36:39], s74 offen offset:0 sc1 lds\n"
"buffer_load_dwordx4 v126, s[36:39], s74 offen offset:1024 sc1 lds\n"
"s_waitcnt vmcnt(14) lgkmcnt(0)\n"
"ds_read_b128 a[208:211], v116 offset:16384\n"
"ds_read_b128 a[212:215], v116 offset:18432\n"
"ds_read_b128 a[216:219], v117 offset:16384\n"
"ds_read_b128 a[220:223], v117 offset:18432\n"
"s_waitcnt vmcnt(2) lgkmcnt(0)\n"
"s_barrier\n"
"ds_read_b128 v[32:35], v124 offset:0\n"
"ds_read_b128 v[36:39], v124 offset:256\n"
"ds_read_b128 v[64:67], v118 offset:0\n"
"ds_read_b128 v[68:71], v118 offset:2048\n"
"ds_read_b128 v[40:43], v124 offset:512\n"
"ds_read_b128 v[44:47], v124 offset:768\n"
"s_add_u32 m0, s81, 24576\n"
"s_xor_b32 s74, s24, 24576\n"
"buffer_load_dwordx4 v126, s[36:39], s74 offen offset:0 sc1 lds\n"
"s_waitcnt lgkmcnt(2)\n"
"v_mfma_f32_16x16x32_bf16 v[0:3], v[32:35], v[64:67], 0\n"
"ds_read_b128 v[48:51], v124 offset:4096\n"
"v_add_u32 v88, v88, v89\n"
"v_add3_u32 v88, v88, v90, v91\n"
"v_mfma_f32_16x16x32_bf16 v[16:19], v[32:35], v[68:71], 0\n"
"ds_read_b128 v[52:55], v124 offset:4352\n"
"v_readfirstlane_b32 s99, v88\n"
"s_nop 0\n"
"s_cmp_lt_i32 s99, 0\n"
"s_cselect_b32 s64, 0xff, 0\n"
"v_mfma_f32_16x16x32_bf16 v[4:7], v[36:39], v[64:67], 0\n"
"ds_read_b128 v[72:75], v119 offset:0\n"
"buffer_load_dwordx4 v126, s[36:39], s74 offen offset:1024 sc1 lds\n"
"v_mfma_f32_16x16x32_bf16 v[20:23], v[36:39], v[68:71], 0\n"
"ds_read_b128 v[76:79], v119 offset:2048\n"
"s_xor_b32 s64, s64, s99\n"
"s_waitcnt lgkmcnt(4)\n"
"v_mfma_f32_16x16x32_bf16 v[8:11], v[40:43], v[64:67], 0\n"
"ds_read_b128 v[56:59], v124 offset:4608\n"
"s_add_u32 m0, s81, 32768\n"
"s_and_b32 s64, s64, 0xff\n"
"v_mfma_f32_16x16x32_bf16 v[24:27], v[40:43], v[68:71], 0\n"
"ds_read_b128 v[60:63], v124 offset:4864\n"
"s_xor_b32 s74, s24, 32768\n"
"s_lshr_b32 s65, s64, s85\n"
"v_mfma_f32_16x16x32_bf16 v[12:15], v[44:47], v[64:67], 0\n"
"buffer_load_dwordx4 v126, s[36:39], s74 offen offset:0 sc1 lds\n"
"v_mfma_f32_16x16x32_bf16 v[28:31], v[44:47], v[68:71], 0\n"
"s_waitcnt lgkmcnt(2)\n"
"v_mfma_f32_16x16x32_bf16 v[0:3], v[48:51], v[72:75], v[0:3]\n"
"ds_read_b128 v[32:35], v124 offset:8192\n"
"s_and_b32 s65, s65, 1\n"
"s_lshl_b32 s65, s65, 13\n"
"v_mfma_f32_16x16x32_bf16 v[16:19], v[48:51], v[76:79], v[16:19]\n"
"ds_read_b128 v[36:39], v124 offset:8448\n"
"v_mfma_f32_16x16x32_bf16 v[4:7], v[52:55], v[72:75], v[4:7]\n"
"ds_read_b128 v[64:67], v118 offset:16384\n"
"buffer_load_dwordx4 v126, s[36:39], s74 offen offset:1024 sc1 lds\n"
"v_mfma_f32_16x16x32_bf16 v[20:23], v[52:55], v[76:79], v[20:23]\n"
"ds_read_b128 v[68:71], v118 offset:18432\n"
"s_waitcnt lgkmcnt(4)\n"
"v_mfma_f32_16x16x32_bf16 v[8:11], v[56:59], v[72:75], v[8:11]\n"
"ds_read_b128 v[40:43], v124 offset:8704\n"
"s_add_u32 m0, s81, 40960\n"
"v_mfma_f32_16x16x32_bf16 v[24:27], v[56:59], v[76:79], v[24:27]\n"
"ds_read_b128 v[44:47], v124 offset:8960\n"
"s_xor_b32 s74, s24, 40960\n"
"v_mfma_f32_16x16x32_bf16 v[12:15], v[60:63], v[72:75], v[12:15]\n"
"buffer_load_dwordx4 v126, s[36:39], s74 offen offset:0 sc1 lds\n"
"v_mfma_f32_16x16x32_bf16 v[28:31], v[60:63], v[76:79], v[28:31]\n"
"s_waitcnt lgkmcnt(2)\n"
"v_mfma_f32_16x16x32_bf16 v[0:3], v[32:35], v[64:67], v[0:3]\n"
"ds_read_b128 v[48:51], v124 offset:12288\n"
"v_mov_b32 v90, s68\n"
"s_or_b32 s96, s72, s71\n"
"s_cselect_b32 s96, 0x2800000, 0\n"
"s_mov_b64 exec, 1\n"
"buffer_store_dword v90, off, s[36:39], s96 sc1\n"
"s_mov_b64 exec, -1\n"
"v_mfma_f32_16x16x32_bf16 v[16:19], v[32:35], v[68:71], v[16:19]\n"
"ds_read_b128 v[52:55], v124 offset:12544\n"
"v_mfma_f32_16x16x32_bf16 v[4:7], v[36:39], v[64:67], v[4:7]\n"
"ds_read_b128 v[72:75], v119 offset:16384\n"
"buffer_load_dwordx4 v126, s[36:39], s74 offen offset:1024 sc1 lds\n"
"v_mfma_f32_16x16x32_bf16 v[20:23], v[36:39], v[68:71], v[20:23]\n"
"ds_read_b128 v[76:79], v119 offset:18432\n"
"s_waitcnt lgkmcnt(4)\n"
"v_mfma_f32_16x16x32_bf16 v[8:11], v[40:43], v[64:67], v[8:11]\n"
"ds_read_b128 v[56:59], v124 offset:12800\n"
"s_add_u32 m0, s81, 49152\n"
"v_mfma_f32_16x16x32_bf16 v[24:27], v[40:43], v[68:71], v[24:27]\n"
"ds_read_b128 v[60:63], v124 offset:13056\n"
"s_xor_b32 s74, s24, 49152\n"
"v_mfma_f32_16x16x32_bf16 v[12:15], v[44:47], v[64:67], v[12:15]\n"
"buffer_load_dwordx4 v126, s[36:39], s74 offen offset:0 sc1 lds\n"
"v_mfma_f32_16x16x32_bf16 v[28:31], v[44:47], v[68:71], v[28:31]\n"
"s_waitcnt vmcnt(6) lgkmcnt(2)\n"
"s_barrier\n"
"v_mfma_f32_16x16x32_bf16 v[0:3], v[48:51], v[72:75], v[0:3]\n"
"ds_read_b128 v[32:35], v124 offset:16384\n"
"v_mfma_f32_16x16x32_bf16 v[16:19], v[48:51], v[76:79], v[16:19]\n"
"ds_read_b128 v[36:39], v124 offset:16640\n"
"v_mfma_f32_16x16x32_bf16 v[4:7], v[52:55], v[72:75], v[4:7]\n"
"ds_read_b128 a[224:227], v116 offset:32768\n"
"buffer_load_dwordx4 v126, s[36:39], s74 offen offset:1024 sc1 lds\n"
"v_mfma_f32_16x16x32_bf16 v[20:23], v[52:55], v[76:79], v[20:23]\n"
"ds_read_b128 a[228:231], v116 offset:34816\n"
"s_waitcnt lgkmcnt(4)\n"
"v_mfma_f32_16x16x32_bf16 v[8:11], v[56:59], v[72:75], v[8:11]\n"
"ds_read_b128 v[40:43], v124 offset:16896\n"
"s_add_u32 m0, s81, 57344\n"
"v_mfma_f32_16x16x32_bf16 v[24:27], v[56:59], v[76:79], v[24:27]\n"
"ds_read_b128 v[44:47], v124 offset:17152\n"
"s_xor_b32 s74, s24, 57344\n"
"v_mfma_f32_16x16x32_bf16 v[12:15], v[60:63], v[72:75], v[12:15]\n"
"buffer_load_dwordx4 v126, s[36:39], s74 offen offset:0 sc1 lds\n"
"v_mfma_f32_16x16x32_bf16 v[28:31], v[60:63], v[76:79], v[28:31]\n"
"s_waitcnt lgkmcnt(2)\n"
"v_mfma_f32_16x16x32_bf16 v[0:3], v[32:35], v[128:131], v[0:3]\n"
"ds_read_b128 v[48:51], v124 offset:20480\n"
"v_lshrrev_b32 v93, 3, v127\n"
"v_mul_u32_u24 v93, 3584, v93\n"
"v_mfma_f32_16x16x32_bf16 v[16:19], v[32:35], v[132:135], v[16:19]\n"
"ds_read_b128 v[52:55], v124 offset:20736\n"
"v_and_b32 v95, 0x70, v126\n"
"v_add_u32 v93, v93, v95\n"
"v_mfma_f32_16x16x32_bf16 v[4:7], v[36:39], v[128:131], v[4:7]\n"
"ds_read_b128 a[232:235], v117 offset:32768\n"
"buffer_load_dwordx4 v126, s[36:39], s74 offen offset:1024 sc1 lds\n"
"v_mfma_f32_16x16x32_bf16 v[20:23], v[36:39], v[132:135], v[20:23]\n"
"ds_read_b128 a[236:239], v117 offset:34816\n"
"s_lshl_b32 s96, s70, 2\n"
"s_waitcnt lgkmcnt(4)\n"
"v_mfma_f32_16x16x32_bf16 v[8:11], v[40:43], v[128:131], v[8:11]\n"
"ds_read_b128 v[56:59], v124 offset:20992\n"
"s_add_u32 m0, s81, 0\n"
"s_add_u32 s96, s96, s71\n"
"v_mfma_f32_16x16x32_bf16 v[24:27], v[40:43], v[132:135], v[24:27]\n"
"ds_read_b128 v[60:63], v124 offset:21248\n"
"s_xor_b32 s74, s24, 81920\n"
"s_mul_i32 s96, s96, 57344\n"
"v_mfma_f32_16x16x32_bf16 v[12:15], v[44:47], v[128:131], v[12:15]\n"
"buffer_load_dwordx4 v126, s[36:39], s74 offen offset:0 sc1 lds\n"
"v_mfma_f32_16x16x32_bf16 v[28:31], v[44:47], v[132:135], v[28:31]\n"
"s_waitcnt lgkmcnt(2)\n"
"v_mfma_f32_16x16x32_bf16 v[0:3], v[48:51], v[136:139], v[0:3]\n"
"ds_read_b128 v[32:35], v124 offset:24576\n"
"s_lshl_b32 s97, s69, 7\n"
"s_add_u32 s96, s96, s97\n"
"v_mfma_f32_16x16x32_bf16 v[16:19], v[48:51], v[140:143], v[16:19]\n"
"ds_read_b128 v[36:39], v124 offset:24832\n"
"s_cmp_ge_u32 s69, 28\n"
"s_cselect_b32 s96, 0x1000000, s96\n"
"v_mfma_f32_16x16x32_bf16 v[4:7], v[52:55], v[136:139], v[4:7]\n"
"ds_read_b128 a[240:243], v116 offset:49152\n"
"buffer_load_dwordx4 v126, s[36:39], s74 offen offset:1024 sc1 lds\n"
"v_mfma_f32_16x16x32_bf16 v[20:23], v[52:55], v[140:143], v[20:23]\n"
"ds_read_b128 a[244:247], v116 offset:51200\n"
"s_add_u32 s97, s96, 28672\n"
"s_waitcnt lgkmcnt(4)\n"
"v_mfma_f32_16x16x32_bf16 v[8:11], v[56:59], v[136:139], v[8:11]\n"
"ds_read_b128 v[40:43], v124 offset:25088\n"
"s_add_u32 m0, s80, 65536\n"
"s_lshl_b32 s98, s25, 13\n"
"v_mfma_f32_16x16x32_bf16 v[24:27], v[56:59], v[140:143], v[24:27]\n"
"ds_read_b128 v[44:47], v124 offset:25344\n"
"s_xor_b32 s73, s2, 1024\n"
"s_add_u32 s98, s98, s81\n"
"v_mfma_f32_16x16x32_bf16 v[12:15], v[60:63], v[136:139], v[12:15]\n"
"buffer_load_dwordx4 v120, s[32:35], s73 offen offset:0 lds\n"
"v_mfma_f32_16x16x32_bf16 v[28:31], v[60:63], v[140:143], v[28:31]\n"
"s_waitcnt lgkmcnt(2)\n"
"v_mfma_f32_16x16x32_bf16 v[0:3], v[32:35], v[144:147], v[0:3]\n"
"ds_read_b128 v[48:51], v124 offset:28672\n"
"s_lshl_b32 s1, s71, 6\n"
"v_xor_b32 v89, s1, v126\n"
"v_mfma_f32_16x16x32_bf16 v[16:19], v[32:35], v[148:151], v[16:19]\n"
"ds_read_b128 v[52:55], v124 offset:28928\n"
"v_xor_b32 v90, 16, v89\n"
"v_xor_b32 v91, 32, v89\n"
"v_mfma_f32_16x16x32_bf16 v[4:7], v[36:39], v[144:147], v[4:7]\n"
"ds_read_b128 a[248:251], v117 offset:49152\n"
"v_xor_b32 v92, 48, v89\n"
"s_lshl_b32 s1, s70, 3\n"
"v_mfma_f32_16x16x32_bf16 v[20:23], v[36:39], v[148:151], v[20:23]\n"
"ds_read_b128 a[252:255], v117 offset:51200\n"
"s_add_u32 s1, s1, s71\n"
"s_waitcnt lgkmcnt(4)\n"
"v_mfma_f32_16x16x32_bf16 v[8:11], v[40:43], v[144:147], v[8:11]\n"
"ds_read_b128 v[56:59], v124 offset:29184\n"
"s_lshl_b32 s1, s1, 13\n"
"s_lshl_b32 s29, s69, 10\n"
"v_mfma_f32_16x16x32_bf16 v[24:27], v[40:43], v[148:151], v[24:27]\n"
"ds_read_b128 v[60:63], v124 offset:29440\n"
"s_and_b32 s29, s29, 0x2000\n"
"s_xor_b32 s29, s29, s65\n"
"v_mfma_f32_16x16x32_bf16 v[12:15], v[44:47], v[144:147], v[12:15]\n"
"buffer_load_dwordx4 v121, s[32:35], s73 offen offset:1024 lds\n"
"v_mfma_f32_16x16x32_bf16 v[28:31], v[44:47], v[148:151], v[28:31]\n"
"s_waitcnt vmcnt(8) lgkmcnt(2)\n"
"s_barrier\n"
"v_mfma_f32_16x16x32_bf16 v[0:3], v[48:51], v[152:155], v[0:3]\n"
"ds_read_b128 v[32:35], v124 offset:32768\n"
"s_add_u32 m0, s81, 8192\n"
"s_lshl_b32 s28, s69, 18\n"
"v_mfma_f32_16x16x32_bf16 v[16:19], v[48:51], v[156:159], v[16:19]\n"
"ds_read_b128 v[36:39], v124 offset:33024\n"
"s_xor_b32 s74, s24, 90112\n"
"s_add_u32 s28, s28, s29\n"
"v_mfma_f32_16x16x32_bf16 v[4:7], v[52:55], v[152:155], v[4:7]\n"
"buffer_load_dwordx4 v126, s[36:39], s74 offen offset:0 sc1 lds\n"
"v_mfma_f32_16x16x32_bf16 v[20:23], v[52:55], v[156:159], v[20:23]\n"
"s_add_u32 s28, s28, 0x1800000\n"
"s_and_b32 s29, s71, 1\n"
"s_waitcnt lgkmcnt(2)\n"
"v_mfma_f32_16x16x32_bf16 v[8:11], v[56:59], v[152:155], v[8:11]\n"
"ds_read_b128 v[40:43], v124 offset:33280\n"
"s_lshl_b32 s29, s29, 17\n"
"s_add_u32 s28, s28, s29\n"
"v_mfma_f32_16x16x32_bf16 v[24:27], v[56:59], v[156:159], v[24:27]\n"
"ds_read_b128 v[44:47], v124 offset:33536\n"
"s_lshr_b32 s29, s71, 1\n"
"s_lshl_b32 s29, s29, 15\n"
"v_mfma_f32_16x16x32_bf16 v[12:15], v[60:63], v[152:155], v[12:15]\n"
"buffer_load_dwordx4 v126, s[36:39], s74 offen offset:1024 sc1 lds\n"
"v_mfma_f32_16x16x32_bf16 v[28:31], v[60:63], v[156:159], v[28:31]\n"
"s_add_u32 s28, s28, s29\n"
"s_and_b32 s29, s70, 4\n"
"s_waitcnt lgkmcnt(2)\n"
"v_mfma_f32_16x16x32_bf16 v[0:3], v[32:35], v[160:163], v[0:3]\n"
"ds_read_b128 v[48:51], v124 offset:36864\n"
"s_add_u32 m0, s81, 16384\n"
"s_add_u32 s29, s29, s70\n"
"v_mfma_f32_16x16x32_bf16 v[16:19], v[32:35], v[164:167], v[16:19]\n"
"ds_read_b128 v[52:55], v124 offset:37120\n"
"s_xor_b32 s74, s24, 98304\n"
"s_lshl_b32 s29, s29, 11\n"
"v_mfma_f32_16x16x32_bf16 v[4:7], v[36:39], v[160:163], v[4:7]\n"
"buffer_load_dwordx4 v126, s[36:39], s74 offen offset:0 sc1 lds\n"
"v_mfma_f32_16x16x32_bf16 v[20:23], v[36:39], v[164:167], v[20:23]\n"
"s_add_u32 s28, s28, s29\n"
"s_add_u32 s29, s28, 0x10000\n"
"s_waitcnt lgkmcnt(2)\n"
"v_mfma_f32_16x16x32_bf16 v[8:11], v[40:43], v[160:163], v[8:11]\n"
"ds_read_b128 v[56:59], v124 offset:37376\n"
"s_add_u32 m0, s80, 65536\n"
"v_mfma_f32_16x16x32_bf16 v[24:27], v[40:43], v[164:167], v[24:27]\n"
"ds_read_b128 v[60:63], v124 offset:37632\n"
"v_mfma_f32_16x16x32_bf16 v[12:15], v[44:47], v[160:163], v[12:15]\n"
"buffer_load_dwordx4 v122, s[32:35], s73 offen offset:2048 lds\n"
"v_mfma_f32_16x16x32_bf16 v[28:31], v[44:47], v[164:167], v[28:31]\n"
"s_waitcnt lgkmcnt(2)\n"
"v_mfma_f32_16x16x32_bf16 v[0:3], v[48:51], v[168:171], v[0:3]\n"
"ds_read_b128 v[32:35], v124 offset:40960\n"
"s_add_u32 m0, s81, 16384\n"
"v_mfma_f32_16x16x32_bf16 v[16:19], v[48:51], v[172:175], v[16:19]\n"
"ds_read_b128 v[36:39], v124 offset:41216\n"
"v_mfma_f32_16x16x32_bf16 v[4:7], v[52:55], v[168:171], v[4:7]\n"
"buffer_load_dwordx4 v126, s[36:39], s74 offen offset:1024 sc1 lds\n"
"v_mfma_f32_16x16x32_bf16 v[20:23], v[52:55], v[172:175], v[20:23]\n"
"s_waitcnt lgkmcnt(2)\n"
"v_mfma_f32_16x16x32_bf16 v[8:11], v[56:59], v[168:171], v[8:11]\n"
"ds_read_b128 v[40:43], v124 offset:41472\n"
"s_add_u32 m0, s80, 65536\n"
"v_mfma_f32_16x16x32_bf16 v[24:27], v[56:59], v[172:175], v[24:27]\n"
"ds_read_b128 v[44:47], v124 offset:41728\n"
"v_mfma_f32_16x16x32_bf16 v[12:15], v[60:63], v[168:171], v[12:15]\n"
"buffer_load_dwordx4 v123, s[32:35], s73 offen offset:3072 lds\n"
"v_mfma_f32_16x16x32_bf16 v[28:31], v[60:63], v[172:175], v[28:31]\n"
"s_waitcnt lgkmcnt(2)\n"
"v_mfma_f32_16x16x32_bf16 v[0:3], v[32:35], v[176:179], v[0:3]\n"
"ds_read_b128 v[48:51], v124 offset:45056\n"
"v_mfma_f32_16x16x32_bf16 v[16:19], v[32:35], v[180:183], v[16:19]\n"
"ds_read_b128 v[52:55], v124 offset:45312\n"
"v_mfma_f32_16x16x32_bf16 v[4:7], v[36:39], v[176:179], v[4:7]\n"
"v_mfma_f32_16x16x32_bf16 v[20:23], v[36:39], v[180:183], v[20:23]\n"
"s_waitcnt lgkmcnt(2)\n"
"v_mfma_f32_16x16x32_bf16 v[8:11], v[40:43], v[176:179], v[8:11]\n"
"ds_read_b128 v[56:59], v124 offset:45568\n"
"s_add_u32 m0, s80, 131072\n"
"v_mfma_f32_16x16x32_bf16 v[24:27], v[40:43], v[180:183], v[24:27]\n"
"ds_read_b128 v[60:63], v124 offset:45824\n"
"v_mfma_f32_16x16x32_bf16 v[12:15], v[44:47], v[176:179], v[12:15]\n"
"buffer_load_dwordx4 v93, s[52:55], s96 offen lds\n"
"v_mfma_f32_16x16x32_bf16 v[28:31], v[44:47], v[180:183], v[28:31]\n"
"s_waitcnt vmcnt(11) lgkmcnt(2)\n"
"s_barrier\n"
"v_mfma_f32_16x16x32_bf16 v[0:3], v[48:51], v[184:187], v[0:3]\n"
"ds_read_b128 v[32:35], v124 offset:49152\n"
"s_add_u32 m0, s81, 24576\n"
"v_mfma_f32_16x16x32_bf16 v[16:19], v[48:51], v[188:191], v[16:19]\n"
"ds_read_b128 v[36:39], v124 offset:49408\n"
"s_xor_b32 s74, s24, 106496\n"
"v_mfma_f32_16x16x32_bf16 v[4:7], v[52:55], v[184:187], v[4:7]\n"
"buffer_load_dwordx4 v126, s[36:39], s74 offen offset:0 sc1 lds\n"
"v_mfma_f32_16x16x32_bf16 v[20:23], v[52:55], v[188:191], v[20:23]\n"
"s_waitcnt lgkmcnt(2)\n"
"v_mfma_f32_16x16x32_bf16 v[8:11], v[56:59], v[184:187], v[8:11]\n"
"ds_read_b128 v[40:43], v124 offset:49664\n"
"s_add_u32 m0, s80, 132096\n"
"v_mfma_f32_16x16x32_bf16 v[24:27], v[56:59], v[188:191], v[24:27]\n"
"ds_read_b128 v[44:47], v124 offset:49920\n"
"v_mfma_f32_16x16x32_bf16 v[12:15], v[60:63], v[184:187], v[12:15]\n"
"buffer_load_dwordx4 v93, s[52:55], s97 offen lds\n"
"v_mfma_f32_16x16x32_bf16 v[28:31], v[60:63], v[188:191], v[28:31]\n"
"s_waitcnt lgkmcnt(2)\n"
"v_mfma_f32_16x16x32_bf16 v[0:3], v[32:35], v[192:195], v[0:3]\n"
"ds_read_b128 v[48:51], v124 offset:53248\n"
"s_add_u32 m0, s81, 24576\n"
"v_mfma_f32_16x16x32_bf16 v[16:19], v[32:35], v[196:199], v[16:19]\n"
"ds_read_b128 v[52:55], v124 offset:53504\n"
"v_mfma_f32_16x16x32_bf16 v[4:7], v[36:39], v[192:195], v[4:7]\n"
"buffer_load_dwordx4 v126, s[36:39], s74 offen offset:1024 sc1 lds\n"
"v_mfma_f32_16x16x32_bf16 v[20:23], v[36:39], v[196:199], v[20:23]\n"
"s_waitcnt lgkmcnt(2)\n"
"v_mfma_f32_16x16x32_bf16 v[8:11], v[40:43], v[192:195], v[8:11]\n"
"ds_read_b128 v[56:59], v124 offset:53760\n"
"s_add_u32 m0, s80, 133120\n"
"v_mfma_f32_16x16x32_bf16 v[24:27], v[40:43], v[196:199], v[24:27]\n"
"ds_read_b128 v[60:63], v124 offset:54016\n"
"v_mfma_f32_16x16x32_bf16 v[12:15], v[44:47], v[192:195], v[12:15]\n"
"buffer_load_dwordx4 v93, s[56:59], s96 offen lds\n"
"v_mfma_f32_16x16x32_bf16 v[28:31], v[44:47], v[196:199], v[28:31]\n"
"s_waitcnt lgkmcnt(2)\n"
"v_mfma_f32_16x16x32_bf16 v[0:3], v[48:51], v[200:203], v[0:3]\n"
"ds_read_b128 v[32:35], v124 offset:57344\n"
"s_add_u32 m0, s81, 32768\n"
"v_mfma_f32_16x16x32_bf16 v[16:19], v[48:51], v[204:207], v[16:19]\n"
"ds_read_b128 v[36:39], v124 offset:57600\n"
"s_xor_b32 s74, s24, 65536\n"
"v_mfma_f32_16x16x32_bf16 v[4:7], v[52:55], v[200:203], v[4:7]\n"
"buffer_load_dwordx4 v126, s[36:39], s74 offen offset:0 sc1 lds\n"
"v_mfma_f32_16x16x32_bf16 v[20:23], v[52:55], v[204:207], v[20:23]\n"
"s_waitcnt lgkmcnt(2)\n"
"v_mfma_f32_16x16x32_bf16 v[8:11], v[56:59], v[200:203], v[8:11]\n"
"ds_read_b128 v[40:43], v124 offset:57856\n"
"s_add_u32 m0, s80, 134144\n"
"v_mfma_f32_16x16x32_bf16 v[24:27], v[56:59], v[204:207], v[24:27]\n"
"ds_read_b128 v[44:47], v124 offset:58112\n"
"v_mfma_f32_16x16x32_bf16 v[12:15], v[60:63], v[200:203], v[12:15]\n"
"buffer_load_dwordx4 v93, s[56:59], s97 offen lds\n"
"v_mfma_f32_16x16x32_bf16 v[28:31], v[60:63], v[204:207], v[28:31]\n"
"s_waitcnt lgkmcnt(2)\n"
"v_mfma_f32_16x16x32_bf16 v[0:3], v[32:35], v[208:211], v[0:3]\n"
"ds_read_b128 v[48:51], v124 offset:61440\n"
"s_add_u32 m0, s81, 32768\n"
"v_mfma_f32_16x16x32_bf16 v[16:19], v[32:35], v[212:215], v[16:19]\n"
"ds_read_b128 v[52:55], v124 offset:61696\n"
"v_mfma_f32_16x16x32_bf16 v[4:7], v[36:39], v[208:211], v[4:7]\n"
"buffer_load_dwordx4 v126, s[36:39], s74 offen offset:1024 sc1 lds\n"
"v_mfma_f32_16x16x32_bf16 v[20:23], v[36:39], v[212:215], v[20:23]\n"
"s_waitcnt lgkmcnt(2)\n"
"v_mfma_f32_16x16x32_bf16 v[8:11], v[40:43], v[208:211], v[8:11]\n"
"ds_read_b128 v[56:59], v124 offset:61952\n"
"s_add_u32 m0, s81, 147456\n"
"v_mfma_f32_16x16x32_bf16 v[24:27], v[40:43], v[212:215], v[24:27]\n"
"ds_read_b128 v[60:63], v124 offset:62208\n"
"v_mfma_f32_16x16x32_bf16 v[12:15], v[44:47], v[208:211], v[12:15]\n"
"buffer_load_dwordx4 v126, s[60:63], s98 offen offset:0 lds\n"
"v_mfma_f32_16x16x32_bf16 v[28:31], v[44:47], v[212:215], v[28:31]\n"
"s_waitcnt vmcnt(13) lgkmcnt(2)\n"
"s_barrier\n"
"v_mfma_f32_16x16x32_bf16 v[0:3], v[48:51], v[216:219], v[0:3]\n"
"ds_read_b128 v[32:35], v124 offset:0\n"
"s_add_u32 m0, s81, 40960\n"
"v_mfma_f32_16x16x32_bf16 v[16:19], v[48:51], v[220:223], v[16:19]\n"
"ds_read_b128 v[36:39], v124 offset:256\n"
"s_xor_b32 s74, s24, 114688\n"
"v_mfma_f32_16x16x32_bf16 v[4:7], v[52:55], v[216:219], v[4:7]\n"
"buffer_load_dwordx4 v126, s[36:39], s74 offen offset:0 sc1 lds\n"
"v_mfma_f32_16x16x32_bf16 v[20:23], v[52:55], v[220:223], v[20:23]\n"
"s_waitcnt lgkmcnt(2)\n"
"v_mfma_f32_16x16x32_bf16 v[8:11], v[56:59], v[216:219], v[8:11]\n"
"ds_read_b128 v[40:43], v124 offset:512\n"
"s_add_u32 m0, s81, 147456\n"
"v_mfma_f32_16x16x32_bf16 v[24:27], v[56:59], v[220:223], v[24:27]\n"
"ds_read_b128 v[44:47], v124 offset:768\n"
"v_mfma_f32_16x16x32_bf16 v[12:15], v[60:63], v[216:219], v[12:15]\n"
"buffer_load_dwordx4 v126, s[60:63], s98 offen offset:1024 lds\n"
"v_mfma_f32_16x16x32_bf16 v[28:31], v[60:63], v[220:223], v[28:31]\n"
"s_waitcnt lgkmcnt(2)\n"
"v_mfma_f32_16x16x32_bf16 v[0:3], v[32:35], v[224:227], v[0:3]\n"
"ds_read_b128 v[48:51], v124 offset:4096\n"
"s_add_u32 m0, s81, 40960\n"
"v_mfma_f32_16x16x32_bf16 v[16:19], v[32:35], v[228:231], v[16:19]\n"
"ds_read_b128 v[52:55], v124 offset:4352\n"
"v_mfma_f32_16x16x32_bf16 v[4:7], v[36:39], v[224:227], v[4:7]\n"
"buffer_load_dwordx4 v126, s[36:39], s74 offen offset:1024 sc1 lds\n"
"v_mfma_f32_16x16x32_bf16 v[20:23], v[36:39], v[228:231], v[20:23]\n"
"s_waitcnt lgkmcnt(2)\n"
"v_mfma_f32_16x16x32_bf16 v[8:11], v[40:43], v[224:227], v[8:11]\n"
"ds_read_b128 v[56:59], v124 offset:4608\n"
"s_add_u32 m0, s80, 81920\n"
"v_mfma_f32_16x16x32_bf16 v[24:27], v[40:43], v[228:231], v[24:27]\n"
"ds_read_b128 v[60:63], v124 offset:4864\n"
"s_xor_b32 s73, s2, 1152\n"
"v_mfma_f32_16x16x32_bf16 v[12:15], v[44:47], v[224:227], v[12:15]\n"
"buffer_load_dwordx4 v120, s[32:35], s73 offen offset:0 lds\n"
"v_mfma_f32_16x16x32_bf16 v[28:31], v[44:47], v[228:231], v[28:31]\n"
"s_waitcnt lgkmcnt(2)\n"
"v_mfma_f32_16x16x32_bf16 v[0:3], v[48:51], v[232:235], v[0:3]\n"
"ds_read_b128 v[32:35], v124 offset:8192\n"
"s_add_u32 m0, s81, 49152\n"
"v_mfma_f32_16x16x32_bf16 v[16:19], v[48:51], v[236:239], v[16:19]\n"
"ds_read_b128 v[36:39], v124 offset:8448\n"
"s_xor_b32 s74, s24, 122880\n"
"v_mfma_f32_16x16x32_bf16 v[4:7], v[52:55], v[232:235], v[4:7]\n"
"buffer_load_dwordx4 v126, s[36:39], s74 offen offset:0 sc1 lds\n"
"v_mfma_f32_16x16x32_bf16 v[20:23], v[52:55], v[236:239], v[20:23]\n"
"s_waitcnt lgkmcnt(2)\n"
"v_mfma_f32_16x16x32_bf16 v[8:11], v[56:59], v[232:235], v[8:11]\n"
"ds_read_b128 v[40:43], v124 offset:8704\n"
"s_add_u32 m0, s80, 81920\n"
"v_mfma_f32_16x16x32_bf16 v[24:27], v[56:59], v[236:239], v[24:27]\n"
"ds_read_b128 v[44:47], v124 offset:8960\n"
"v_mfma_f32_16x16x32_bf16 v[12:15], v[60:63], v[232:235], v[12:15]\n"
"buffer_load_dwordx4 v121, s[32:35], s73 offen offset:1024 lds\n"
"v_mfma_f32_16x16x32_bf16 v[28:31], v[60:63], v[236:239], v[28:31]\n"
"s_waitcnt lgkmcnt(2)\n"
"v_mfma_f32_16x16x32_bf16 v[0:3], v[32:35], v[240:243], v[0:3]\n"
"ds_read_b128 v[48:51], v124 offset:12288\n"
"s_add_u32 m0, s81, 49152\n"
"v_mfma_f32_16x16x32_bf16 v[16:19], v[32:35], v[244:247], v[16:19]\n"
"ds_read_b128 v[52:55], v124 offset:12544\n"
"v_mfma_f32_16x16x32_bf16 v[4:7], v[36:39], v[240:243], v[4:7]\n"
"buffer_load_dwordx4 v126, s[36:39], s74 offen offset:1024 sc1 lds\n"
"v_mfma_f32_16x16x32_bf16 v[20:23], v[36:39], v[244:247], v[20:23]\n"
"s_waitcnt lgkmcnt(2)\n"
"v_mfma_f32_16x16x32_bf16 v[8:11], v[40:43], v[240:243], v[8:11]\n"
"ds_read_b128 v[56:59], v124 offset:12800\n"
"s_add_u32 m0, s80, 81920\n"
"v_mfma_f32_16x16x32_bf16 v[24:27], v[40:43], v[244:247], v[24:27]\n"
"ds_read_b128 v[60:63], v124 offset:13056\n"
"v_mfma_f32_16x16x32_bf16 v[12:15], v[44:47], v[240:243], v[12:15]\n"
"buffer_load_dwordx4 v122, s[32:35], s73 offen offset:2048 lds\n"
"v_mfma_f32_16x16x32_bf16 v[28:31], v[44:47], v[244:247], v[28:31]\n"
"s_waitcnt vmcnt(13) lgkmcnt(2)\n"
"s_barrier\n"
"v_mfma_f32_16x16x32_bf16 v[0:3], v[48:51], v[248:251], v[0:3]\n"
"ds_read_b128 v[32:35], v124 offset:16384\n"
"s_add_u32 m0, s81, 57344\n"
"v_mfma_f32_16x16x32_bf16 v[16:19], v[48:51], v[252:255], v[16:19]\n"
"ds_read_b128 v[36:39], v124 offset:16640\n"
"s_xor_b32 s74, s24, 73728\n"
"v_mfma_f32_16x16x32_bf16 v[4:7], v[52:55], v[248:251], v[4:7]\n"
"buffer_load_dwordx4 v126, s[36:39], s74 offen offset:0 sc1 lds\n"
"v_mfma_f32_16x16x32_bf16 v[20:23], v[52:55], v[252:255], v[20:23]\n"
"s_waitcnt lgkmcnt(2)\n"
"v_mfma_f32_16x16x32_bf16 v[8:11], v[56:59], v[248:251], v[8:11]\n"
"ds_read_b128 v[40:43], v124 offset:16896\n"
"s_add_u32 m0, s80, 81920\n"
"v_mfma_f32_16x16x32_bf16 v[24:27], v[56:59], v[252:255], v[24:27]\n"
"ds_read_b128 v[44:47], v124 offset:17152\n"
"v_mfma_f32_16x16x32_bf16 v[12:15], v[60:63], v[248:251], v[12:15]\n"
"buffer_load_dwordx4 v123, s[32:35], s73 offen offset:3072 lds\n"
"v_mfma_f32_16x16x32_bf16 v[28:31], v[60:63], v[252:255], v[28:31]\n"
"s_waitcnt lgkmcnt(2)\n"
"v_mfma_f32_16x16x32_bf16 v[0:3], v[32:35], a[0:3], v[0:3]\n"
"ds_read_b128 v[48:51], v124 offset:20480\n"
"s_add_u32 m0, s81, 57344\n"
"v_and_b32 v88, 15, v127\n"
"v_mfma_f32_16x16x32_bf16 v[16:19], v[32:35], a[4:7], v[16:19]\n"
"ds_read_b128 v[52:55], v124 offset:20736\n"
"v_lshlrev_b32 v93, 7, v88\n"
"v_and_or_b32 v93, v127, 48, v93\n"
"v_mfma_f32_16x16x32_bf16 v[4:7], v[36:39], a[0:3], v[4:7]\n"
"buffer_load_dwordx4 v126, s[36:39], s74 offen offset:1024 sc1 lds\n"
"v_mfma_f32_16x16x32_bf16 v[20:23], v[36:39], a[4:7], v[20:23]\n"
"s_add_u32 s96, s80, 131072\n"
"v_add_u32 v93, s96, v93\n"
"s_waitcnt lgkmcnt(2)\n"
"v_mfma_f32_16x16x32_bf16 v[8:11], v[40:43], a[0:3], v[8:11]\n"
"ds_read_b128 v[56:59], v124 offset:20992\n"
"s_add_u32 m0, s80, 98304\n"
"v_lshrrev_b32 v112, 4, v127\n"
"v_mfma_f32_16x16x32_bf16 v[24:27], v[40:43], a[4:7], v[24:27]\n"
"ds_read_b128 v[60:63], v124 offset:21248\n"
"s_xor_b32 s73, s2, 2048\n"
"v_lshlrev_b32 v112, 14, v112\n"
"v_mfma_f32_16x16x32_bf16 v[12:15], v[44:47], a[0:3], v[12:15]\n"
"buffer_load_dwordx4 v120, s[32:35], s73 offen offset:0 lds\n"
"v_mfma_f32_16x16x32_bf16 v[28:31], v[44:47], a[4:7], v[28:31]\n"
"s_waitcnt lgkmcnt(2)\n"
"v_mfma_f32_16x16x32_bf16 v[0:3], v[48:51], a[8:11], v[0:3]\n"
"ds_read_b128 v[32:35], v124 offset:24576\n"
"s_add_u32 m0, s81, 0\n"
"v_lshl_add_u32 v88, v88, 4, v112\n"
"v_mfma_f32_16x16x32_bf16 v[16:19], v[48:51], a[12:15], v[16:19]\n"
"ds_read_b128 v[36:39], v124 offset:24832\n"
"s_xor_b32 s74, s24, 147456\n"
"s_lshl_b32 s96, s69, 17\n"
"v_mfma_f32_16x16x32_bf16 v[4:7], v[52:55], a[8:11], v[4:7]\n"
"buffer_load_dwordx4 v126, s[36:39], s74 offen offset:0 sc1 lds\n"
"v_mfma_f32_16x16x32_bf16 v[20:23], v[52:55], a[12:15], v[20:23]\n"
"s_lshl_b32 s97, s70, 10\n"
"s_add_u32 s96, s96, s97\n"
"s_waitcnt lgkmcnt(2)\n"
"v_mfma_f32_16x16x32_bf16 v[8:11], v[56:59], a[8:11], v[8:11]\n"
"ds_read_b128 v[40:43], v124 offset:25088\n"
"s_add_u32 m0, s80, 98304\n"
"s_lshl_b32 s97, s71, 8\n"
"v_mfma_f32_16x16x32_bf16 v[24:27], v[56:59], a[12:15], v[24:27]\n"
"ds_read_b128 v[44:47], v124 offset:25344\n"
"s_add_u32 s96, s96, s97\n"
"s_waitcnt vmcnt(15)\n"
"v_mfma_f32_16x16x32_bf16 v[12:15], v[60:63], a[8:11], v[12:15]\n"
"buffer_load_dwordx4 v121, s[32:35], s73 offen offset:1024 lds\n"
"v_mfma_f32_16x16x32_bf16 v[28:31], v[60:63], a[12:15], v[28:31]\n"
"ds_read_b128 v[112:115], v93 offset:0\n"
"s_add_u32 s97, s96, 0x1400000\n"
"s_waitcnt lgkmcnt(3)\n"
"v_mfma_f32_16x16x32_bf16 v[0:3], v[32:35], a[16:19], v[0:3]\n"
"ds_read_b128 v[48:51], v124 offset:28672\n"
"s_add_u32 m0, s81, 0\n"
"v_mfma_f32_16x16x32_bf16 v[16:19], v[32:35], a[20:23], v[16:19]\n"
"ds_read_b128 v[52:55], v124 offset:28928\n"
"v_mfma_f32_16x16x32_bf16 v[4:7], v[36:39], a[16:19], v[4:7]\n"
"buffer_load_dwordx4 v126, s[36:39], s74 offen offset:1024 sc1 lds\n"
"v_mfma_f32_16x16x32_bf16 v[20:23], v[36:39], a[20:23], v[20:23]\n"
"s_waitcnt lgkmcnt(3)\n"
"v_mfma_f32_16x16x32_bf16 v[8:11], v[40:43], a[16:19], v[8:11]\n"
"ds_read_b128 v[56:59], v124 offset:29184\n"
"s_add_u32 m0, s80, 98304\n"
"v_mfma_f32_16x16x32_bf16 v[24:27], v[40:43], a[20:23], v[24:27]\n"
"ds_read_b128 v[60:63], v124 offset:29440\n"
"v_mfma_f32_16x16x32_bf16 v[12:15], v[44:47], a[16:19], v[12:15]\n"
"buffer_load_dwordx4 v122, s[32:35], s73 offen offset:2048 lds\n"
"v_mfma_f32_16x16x32_bf16 v[28:31], v[44:47], a[20:23], v[28:31]\n"
"s_waitcnt vmcnt(13) lgkmcnt(2)\n"
"s_barrier\n"
"v_mfma_f32_16x16x32_bf16 v[0:3], v[48:51], a[24:27], v[0:3]\n"
"ds_read_b128 v[32:35], v124 offset:32768\n"
"s_add_u32 m0, s81, 8192\n"
"v_mfma_f32_16x16x32_bf16 v[16:19], v[48:51], a[28:31], v[16:19]\n"
"ds_read_b128 v[36:39], v124 offset:33024\n"
"s_xor_b32 s74, s24, 155648\n"
"v_mfma_f32_16x16x32_bf16 v[4:7], v[52:55], a[24:27], v[4:7]\n"
"ds_read_b128 v[64:67], v116 offset:0\n"
"buffer_load_dwordx4 v126, s[36:39], s74 offen offset:0 sc1 lds\n"
"v_mfma_f32_16x16x32_bf16 v[20:23], v[52:55], a[28:31], v[20:23]\n"
"ds_read_b128 v[68:71], v116 offset:2048\n"
"s_waitcnt lgkmcnt(4)\n"
"v_mfma_f32_16x16x32_bf16 v[8:11], v[56:59], a[24:27], v[8:11]\n"
"ds_read_b128 v[40:43], v124 offset:33280\n"
"s_add_u32 m0, s80, 98304\n"
"v_mfma_f32_16x16x32_bf16 v[24:27], v[56:59], a[28:31], v[24:27]\n"
"ds_read_b128 v[44:47], v124 offset:33536\n"
"v_mfma_f32_16x16x32_bf16 v[12:15], v[60:63], a[24:27], v[12:15]\n"
"buffer_load_dwordx4 v123, s[32:35], s73 offen offset:3072 lds\n"
"v_mfma_f32_16x16x32_bf16 v[28:31], v[60:63], a[28:31], v[28:31]\n"
"s_waitcnt lgkmcnt(2)\n"
"v_mfma_f32_16x16x32_bf16 v[0:3], v[32:35], v[64:67], v[0:3]\n"
"ds_read_b128 v[48:51], v124 offset:36864\n"
"s_add_u32 m0, s81, 8192\n"
"v_mfma_f32_16x16x32_bf16 v[16:19], v[32:35], v[68:71], v[16:19]\n"
"ds_read_b128 v[52:55], v124 offset:37120\n"
"v_mfma_f32_16x16x32_bf16 v[4:7], v[36:39], v[64:67], v[4:7]\n"
"ds_read_b128 v[72:75], v117 offset:0\n"
"buffer_load_dwordx4 v126, s[36:39], s74 offen offset:1024 sc1 lds\n"
"v_mfma_f32_16x16x32_bf16 v[20:23], v[36:39], v[68:71], v[20:23]\n"
"ds_read_b128 v[76:79], v117 offset:2048\n"
"s_waitcnt lgkmcnt(4)\n"
"v_mfma_f32_16x16x32_bf16 v[8:11], v[40:43], v[64:67], v[8:11]\n"
"ds_read_b128 v[56:59], v124 offset:37376\n"
"v_mfma_f32_16x16x32_bf16 v[24:27], v[40:43], v[68:71], v[24:27]\n"
"ds_read_b128 v[60:63], v124 offset:37632\n"
"v_mfma_f32_16x16x32_bf16 v[12:15], v[44:47], v[64:67], v[12:15]\n"
"buffer_store_dwordx4 v[112:115], v88, s[36:39], s97 offen sc1\n"
"s_nop 1\n"
"ds_read_b128 v[112:115], v93 offset:64\n"
"s_add_u32 s97, s96, 0x1410000\n"
"v_mfma_f32_16x16x32_bf16 v[28:31], v[44:47], v[68:71], v[28:31]\n"
"s_waitcnt lgkmcnt(3)\n"
"v_mfma_f32_16x16x32_bf16 v[0:3], v[48:51], v[72:75], v[0:3]\n"
"ds_read_b128 v[32:35], v124 offset:40960\n"
"s_add_u32 m0, s81, 16384\n"
"v_mfma_f32_16x16x32_bf16 v[16:19], v[48:51], v[76:79], v[16:19]\n"
"ds_read_b128 v[36:39], v124 offset:41216\n"
"s_xor_b32 s74, s24, 131072\n"
"v_mfma_f32_16x16x32_bf16 v[4:7], v[52:55], v[72:75], v[4:7]\n"
"buffer_load_dwordx4 v126, s[36:39], s74 offen offset:0 sc1 lds\n"
"v_mfma_f32_16x16x32_bf16 v[20:23], v[52:55], v[76:79], v[20:23]\n"
"s_waitcnt lgkmcnt(3)\n"
"v_mfma_f32_16x16x32_bf16 v[8:11], v[56:59], v[72:75], v[8:11]\n"
"ds_read_b128 v[40:43], v124 offset:41472\n"
"s_add_u32 m0, s80, 114688\n"
"v_mfma_f32_16x16x32_bf16 v[24:27], v[56:59], v[76:79], v[24:27]\n"
"ds_read_b128 v[44:47], v124 offset:41728\n"
"s_xor_b32 s73, s2, 2176\n"
"v_mfma_f32_16x16x32_bf16 v[12:15], v[60:63], v[72:75], v[12:15]\n"
"buffer_load_dwordx4 v120, s[32:35], s73 offen offset:0 lds\n"
"v_mfma_f32_16x16x32_bf16 v[28:31], v[60:63], v[76:79], v[28:31]\n"
"s_waitcnt lgkmcnt(2)\n"
"v_mfma_f32_16x16x32_bf16 v[0:3], v[32:35], a[32:35], v[0:3]\n"
"ds_read_b128 v[48:51], v124 offset:45056\n"
"s_add_u32 m0, s81, 16384\n"
"v_mfma_f32_16x16x32_bf16 v[16:19], v[32:35], a[36:39], v[16:19]\n"
"ds_read_b128 v[52:55], v124 offset:45312\n"
"v_mfma_f32_16x16x32_bf16 v[4:7], v[36:39], a[32:35], v[4:7]\n"
"buffer_load_dwordx4 v126, s[36:39], s74 offen offset:1024 sc1 lds\n"
"v_mfma_f32_16x16x32_bf16 v[20:23], v[36:39], a[36:39], v[20:23]\n"
"s_waitcnt lgkmcnt(2)\n"
"v_mfma_f32_16x16x32_bf16 v[8:11], v[40:43], a[32:35], v[8:11]\n"
"ds_read_b128 v[56:59], v124 offset:45568\n"
"v_mfma_f32_16x16x32_bf16 v[24:27], v[40:43], a[36:39], v[24:27]\n"
"ds_read_b128 v[60:63], v124 offset:45824\n"
"v_mfma_f32_16x16x32_bf16 v[12:15], v[44:47], a[32:35], v[12:15]\n"
"buffer_store_dwordx4 v[112:115], v88, s[36:39], s97 offen sc1\n"
"s_nop 1\n"
"ds_read_b128 v[112:115], v93 offset:2048\n"
"s_add_u32 s97, s96, 0x1402000\n"
"v_mfma_f32_16x16x32_bf16 v[28:31], v[44:47], a[36:39], v[28:31]\n"
"s_waitcnt vmcnt(13) lgkmcnt(3)\n"
"s_barrier\n"
"v_mfma_f32_16x16x32_bf16 v[0:3], v[48:51], a[40:43], v[0:3]\n"
"ds_read_b128 v[32:35], v124 offset:49152\n"
"s_add_u32 m0, s81, 24576\n"
"v_mfma_f32_16x16x32_bf16 v[16:19], v[48:51], a[44:47], v[16:19]\n"
"ds_read_b128 v[36:39], v124 offset:49408\n"
"s_xor_b32 s74, s24, 163840\n"
"v_mfma_f32_16x16x32_bf16 v[4:7], v[52:55], a[40:43], v[4:7]\n"
"buffer_load_dwordx4 v126, s[36:39], s74 offen offset:0 sc1 lds\n"
"v_mfma_f32_16x16x32_bf16 v[20:23], v[52:55], a[44:47], v[20:23]\n"
"s_waitcnt lgkmcnt(3)\n"
"v_mfma_f32_16x16x32_bf16 v[8:11], v[56:59], a[40:43], v[8:11]\n"
"ds_read_b128 v[40:43], v124 offset:49664\n"
"s_add_u32 m0, s80, 114688\n"
"v_mfma_f32_16x16x32_bf16 v[24:27], v[56:59], a[44:47], v[24:27]\n"
"ds_read_b128 v[44:47], v124 offset:49920\n"
"v_mfma_f32_16x16x32_bf16 v[12:15], v[60:63], a[40:43], v[12:15]\n"
"buffer_load_dwordx4 v121, s[32:35], s73 offen offset:1024 lds\n"
"v_mfma_f32_16x16x32_bf16 v[28:31], v[60:63], a[44:47], v[28:31]\n"
"s_waitcnt lgkmcnt(2)\n"
"v_mfma_f32_16x16x32_bf16 v[0:3], v[32:35], a[48:51], v[0:3]\n"
"ds_read_b128 v[48:51], v124 offset:53248\n"
"s_add_u32 m0, s81, 24576\n"
"v_mfma_f32_16x16x32_bf16 v[16:19], v[32:35], a[52:55], v[16:19]\n"
"ds_read_b128 v[52:55], v124 offset:53504\n"
"v_mfma_f32_16x16x32_bf16 v[4:7], v[36:39], a[48:51], v[4:7]\n"
"buffer_load_dwordx4 v126, s[36:39], s74 offen offset:1024 sc1 lds\n"
"v_mfma_f32_16x16x32_bf16 v[20:23], v[36:39], a[52:55], v[20:23]\n"
"s_waitcnt lgkmcnt(2)\n"
"v_mfma_f32_16x16x32_bf16 v[8:11], v[40:43], a[48:51], v[8:11]\n"
"ds_read_b128 v[56:59], v124 offset:53760\n"
"v_mfma_f32_16x16x32_bf16 v[24:27], v[40:43], a[52:55], v[24:27]\n"
"ds_read_b128 v[60:63], v124 offset:54016\n"
"v_mfma_f32_16x16x32_bf16 v[12:15], v[44:47], a[48:51], v[12:15]\n"
"buffer_store_dwordx4 v[112:115], v88, s[36:39], s97 offen sc1\n"
"s_nop 1\n"
"ds_read_b128 v[112:115], v93 offset:2112\n"
"s_add_u32 s97, s96, 0x1412000\n"
"v_mfma_f32_16x16x32_bf16 v[28:31], v[44:47], a[52:55], v[28:31]\n"
"s_waitcnt lgkmcnt(3)\n"
"v_mfma_f32_16x16x32_bf16 v[0:3], v[48:51], a[56:59], v[0:3]\n"
"ds_read_b128 v[32:35], v124 offset:57344\n"
"s_add_u32 m0, s81, 32768\n"
"v_mfma_f32_16x16x32_bf16 v[16:19], v[48:51], a[60:63], v[16:19]\n"
"ds_read_b128 v[36:39], v124 offset:57600\n"
"s_xor_b32 s74, s24, 172032\n"
"v_mfma_f32_16x16x32_bf16 v[4:7], v[52:55], a[56:59], v[4:7]\n"
"ds_read_b128 v[64:67], v116 offset:16384\n"
"buffer_load_dwordx4 v126, s[36:39], s74 offen offset:0 sc1 lds\n"
"v_mfma_f32_16x16x32_bf16 v[20:23], v[52:55], a[60:63], v[20:23]\n"
"ds_read_b128 v[68:71], v116 offset:18432\n"
"s_waitcnt lgkmcnt(5)\n"
"v_mfma_f32_16x16x32_bf16 v[8:11], v[56:59], a[56:59], v[8:11]\n"
"ds_read_b128 v[40:43], v124 offset:57856\n"
"s_add_u32 m0, s80, 114688\n"
"v_mfma_f32_16x16x32_bf16 v[24:27], v[56:59], a[60:63], v[24:27]\n"
"ds_read_b128 v[44:47], v124 offset:58112\n"
"v_mfma_f32_16x16x32_bf16 v[12:15], v[60:63], a[56:59], v[12:15]\n"
"buffer_load_dwordx4 v122, s[32:35], s73 offen offset:2048 lds\n"
"v_mfma_f32_16x16x32_bf16 v[28:31], v[60:63], a[60:63], v[28:31]\n"
"s_waitcnt lgkmcnt(2)\n"
"v_mfma_f32_16x16x32_bf16 v[0:3], v[32:35], v[64:67], v[0:3]\n"
"ds_read_b128 v[48:51], v124 offset:61440\n"
"s_add_u32 m0, s81, 32768\n"
"v_mfma_f32_16x16x32_bf16 v[16:19], v[32:35], v[68:71], v[16:19]\n"
"ds_read_b128 v[52:55], v124 offset:61696\n"
"v_mfma_f32_16x16x32_bf16 v[4:7], v[36:39], v[64:67], v[4:7]\n"
"ds_read_b128 v[72:75], v117 offset:16384\n"
"buffer_load_dwordx4 v126, s[36:39], s74 offen offset:1024 sc1 lds\n"
"v_mfma_f32_16x16x32_bf16 v[20:23], v[36:39], v[68:71], v[20:23]\n"
"ds_read_b128 v[76:79], v117 offset:18432\n"
"s_waitcnt lgkmcnt(4)\n"
"v_mfma_f32_16x16x32_bf16 v[8:11], v[40:43], v[64:67], v[8:11]\n"
"ds_read_b128 v[56:59], v124 offset:61952\n"
"v_mfma_f32_16x16x32_bf16 v[24:27], v[40:43], v[68:71], v[24:27]\n"
"ds_read_b128 v[60:63], v124 offset:62208\n"
"v_mfma_f32_16x16x32_bf16 v[12:15], v[44:47], v[64:67], v[12:15]\n"
"buffer_store_dwordx4 v[112:115], v88, s[36:39], s97 offen sc1\n"
"v_mfma_f32_16x16x32_bf16 v[28:31], v[44:47], v[68:71], v[28:31]\n"
"s_waitcnt vmcnt(13) lgkmcnt(2)\n"
"s_barrier\n"
"v_mfma_f32_16x16x32_bf16 v[0:3], v[48:51], v[72:75], v[0:3]\n"
"ds_read_b128 v[32:35], v124 offset:0\n"
"s_add_u32 m0, s81, 40960\n"
"v_mfma_f32_16x16x32_bf16 v[16:19], v[48:51], v[76:79], v[16:19]\n"
"ds_read_b128 v[36:39], v124 offset:256\n"
"s_xor_b32 s74, s24, 139264\n"
"v_mfma_f32_16x16x32_bf16 v[4:7], v[52:55], v[72:75], v[4:7]\n"
"buffer_load_dwordx4 v126, s[36:39], s74 offen offset:0 sc1 lds\n"
"v_mfma_f32_16x16x32_bf16 v[20:23], v[52:55], v[76:79], v[20:23]\n"
"s_waitcnt lgkmcnt(2)\n"
"v_mfma_f32_16x16x32_bf16 v[8:11], v[56:59], v[72:75], v[8:11]\n"
"ds_read_b128 v[40:43], v124 offset:512\n"
"s_add_u32 m0, s80, 114688\n"
"v_mfma_f32_16x16x32_bf16 v[24:27], v[56:59], v[76:79], v[24:27]\n"
"ds_read_b128 v[44:47], v124 offset:768\n"
"v_mfma_f32_16x16x32_bf16 v[12:15], v[60:63], v[72:75], v[12:15]\n"
"buffer_load_dwordx4 v123, s[32:35], s73 offen offset:3072 lds\n"
"v_mfma_f32_16x16x32_bf16 v[28:31], v[60:63], v[76:79], v[28:31]\n"
"s_waitcnt lgkmcnt(2)\n"
"v_mfma_f32_16x16x32_bf16 v[0:3], v[32:35], a[64:67], v[0:3]\n"
"ds_read_b128 v[48:51], v124 offset:4096\n"
"s_add_u32 m0, s81, 40960\n"
"v_mfma_f32_16x16x32_bf16 v[16:19], v[32:35], a[68:71], v[16:19]\n"
"ds_read_b128 v[52:55], v124 offset:4352\n"
"v_mfma_f32_16x16x32_bf16 v[4:7], v[36:39], a[64:67], v[4:7]\n"
"buffer_load_dwordx4 v126, s[36:39], s74 offen offset:1024 sc1 lds\n"
"v_mfma_f32_16x16x32_bf16 v[20:23], v[36:39], a[68:71], v[20:23]\n"
"s_waitcnt lgkmcnt(2)\n"
"v_mfma_f32_16x16x32_bf16 v[8:11], v[40:43], a[64:67], v[8:11]\n"
"ds_read_b128 v[56:59], v124 offset:4608\n"
"s_add_u32 m0, s80, 65536\n"
"v_mfma_f32_16x16x32_bf16 v[24:27], v[40:43], a[68:71], v[24:27]\n"
"ds_read_b128 v[60:63], v124 offset:4864\n"
"s_xor_b32 s73, s2, 3072\n"
"v_mfma_f32_16x16x32_bf16 v[12:15], v[44:47], a[64:67], v[12:15]\n"
"buffer_load_dwordx4 v120, s[32:35], s73 offen offset:0 lds\n"
"v_mfma_f32_16x16x32_bf16 v[28:31], v[44:47], a[68:71], v[28:31]\n"
"s_waitcnt lgkmcnt(2)\n"
"v_mfma_f32_16x16x32_bf16 v[0:3], v[48:51], a[72:75], v[0:3]\n"
"ds_read_b128 v[32:35], v124 offset:8192\n"
"s_add_u32 m0, s81, 49152\n"
"v_mfma_f32_16x16x32_bf16 v[16:19], v[48:51], a[76:79], v[16:19]\n"
"ds_read_b128 v[36:39], v124 offset:8448\n"
"s_xor_b32 s74, s24, 180224\n"
"v_mfma_f32_16x16x32_bf16 v[4:7], v[52:55], a[72:75], v[4:7]\n"
"buffer_load_dwordx4 v126, s[36:39], s74 offen offset:0 sc1 lds\n"
"v_mfma_f32_16x16x32_bf16 v[20:23], v[52:55], a[76:79], v[20:23]\n"
"s_waitcnt lgkmcnt(2)\n"
"v_mfma_f32_16x16x32_bf16 v[8:11], v[56:59], a[72:75], v[8:11]\n"
"ds_read_b128 v[40:43], v124 offset:8704\n"
"s_add_u32 m0, s80, 65536\n"
"v_mfma_f32_16x16x32_bf16 v[24:27], v[56:59], a[76:79], v[24:27]\n"
"ds_read_b128 v[44:47], v124 offset:8960\n"
"v_mfma_f32_16x16x32_bf16 v[12:15], v[60:63], a[72:75], v[12:15]\n"
"buffer_load_dwordx4 v121, s[32:35], s73 offen offset:1024 lds\n"
"v_mfma_f32_16x16x32_bf16 v[28:31], v[60:63], a[76:79], v[28:31]\n"
"s_waitcnt lgkmcnt(2)\n"
"v_mfma_f32_16x16x32_bf16 v[0:3], v[32:35], a[80:83], v[0:3]\n"
"ds_read_b128 v[48:51], v124 offset:12288\n"
"s_add_u32 m0, s81, 49152\n"
"v_mfma_f32_16x16x32_bf16 v[16:19], v[32:35], a[84:87], v[16:19]\n"
"ds_read_b128 v[52:55], v124 offset:12544\n"
"v_mfma_f32_16x16x32_bf16 v[4:7], v[36:39], a[80:83], v[4:7]\n"
"buffer_load_dwordx4 v126, s[36:39], s74 offen offset:1024 sc1 lds\n"
"v_mfma_f32_16x16x32_bf16 v[20:23], v[36:39], a[84:87], v[20:23]\n"
"s_waitcnt lgkmcnt(2)\n"
"v_mfma_f32_16x16x32_bf16 v[8:11], v[40:43], a[80:83], v[8:11]\n"
"ds_read_b128 v[56:59], v124 offset:12800\n"
"s_add_u32 m0, s80, 65536\n"
"v_mfma_f32_16x16x32_bf16 v[24:27], v[40:43], a[84:87], v[24:27]\n"
"ds_read_b128 v[60:63], v124 offset:13056\n"
"v_mfma_f32_16x16x32_bf16 v[12:15], v[44:47], a[80:83], v[12:15]\n"
"buffer_load_dwordx4 v122, s[32:35], s73 offen offset:2048 lds\n"
"v_mfma_f32_16x16x32_bf16 v[28:31], v[44:47], a[84:87], v[28:31]\n"
"s_waitcnt vmcnt(13) lgkmcnt(2)\n"
"s_barrier\n"
"v_mfma_f32_16x16x32_bf16 v[0:3], v[48:51], a[88:91], v[0:3]\n"
"ds_read_b128 v[32:35], v124 offset:16384\n"
"s_add_u32 m0, s81, 57344\n"
"v_mfma_f32_16x16x32_bf16 v[16:19], v[48:51], a[92:95], v[16:19]\n"
"ds_read_b128 v[36:39], v124 offset:16640\n"
"s_xor_b32 s74, s24, 188416\n"
"v_mfma_f32_16x16x32_bf16 v[4:7], v[52:55], a[88:91], v[4:7]\n"
"ds_read_b128 v[64:67], v116 offset:32768\n"
"buffer_load_dwordx4 v126, s[36:39], s74 offen offset:0 sc1 lds\n"
"v_mfma_f32_16x16x32_bf16 v[20:23], v[52:55], a[92:95], v[20:23]\n"
"ds_read_b128 v[68:71], v116 offset:34816\n"
"s_waitcnt lgkmcnt(4)\n"
"v_mfma_f32_16x16x32_bf16 v[8:11], v[56:59], a[88:91], v[8:11]\n"
"ds_read_b128 v[40:43], v124 offset:16896\n"
"s_add_u32 m0, s80, 65536\n"
"v_mfma_f32_16x16x32_bf16 v[24:27], v[56:59], a[92:95], v[24:27]\n"
"ds_read_b128 v[44:47], v124 offset:17152\n"
"v_mfma_f32_16x16x32_bf16 v[12:15], v[60:63], a[88:91], v[12:15]\n"
"buffer_load_dwordx4 v123, s[32:35], s73 offen offset:3072 lds\n"
"v_mfma_f32_16x16x32_bf16 v[28:31], v[60:63], a[92:95], v[28:31]\n"
"s_waitcnt lgkmcnt(2)\n"
"v_mfma_f32_16x16x32_bf16 v[0:3], v[32:35], v[64:67], v[0:3]\n"
"ds_read_b128 v[48:51], v124 offset:20480\n"
"s_add_u32 m0, s81, 57344\n"
"v_mfma_f32_16x16x32_bf16 v[16:19], v[32:35], v[68:71], v[16:19]\n"
"ds_read_b128 v[52:55], v124 offset:20736\n"
"v_mfma_f32_16x16x32_bf16 v[4:7], v[36:39], v[64:67], v[4:7]\n"
"ds_read_b128 v[72:75], v117 offset:32768\n"
"buffer_load_dwordx4 v126, s[36:39], s74 offen offset:1024 sc1 lds\n"
"v_mfma_f32_16x16x32_bf16 v[20:23], v[36:39], v[68:71], v[20:23]\n"
"ds_read_b128 v[76:79], v117 offset:34816\n"
"s_waitcnt lgkmcnt(4)\n"
"v_mfma_f32_16x16x32_bf16 v[8:11], v[40:43], v[64:67], v[8:11]\n"
"ds_read_b128 v[56:59], v124 offset:20992\n"
"s_add_u32 m0, s80, 81920\n"
"v_mfma_f32_16x16x32_bf16 v[24:27], v[40:43], v[68:71], v[24:27]\n"
"ds_read_b128 v[60:63], v124 offset:21248\n"
"s_xor_b32 s73, s2, 3200\n"
"v_mfma_f32_16x16x32_bf16 v[12:15], v[44:47], v[64:67], v[12:15]\n"
"buffer_load_dwordx4 v120, s[32:35], s73 offen offset:0 lds\n"
"v_mfma_f32_16x16x32_bf16 v[28:31], v[44:47], v[68:71], v[28:31]\n"
"s_waitcnt lgkmcnt(2)\n"
"v_mfma_f32_16x16x32_bf16 v[0:3], v[48:51], v[72:75], v[0:3]\n"
"ds_read_b128 v[32:35], v124 offset:24576\n"
"s_add_u32 m0, s81, 0\n"
"v_mfma_f32_16x16x32_bf16 v[16:19], v[48:51], v[76:79], v[16:19]\n"
"ds_read_b128 v[36:39], v124 offset:24832\n"
"s_xor_b32 s74, s24, 196608\n"
"v_mfma_f32_16x16x32_bf16 v[4:7], v[52:55], v[72:75], v[4:7]\n"
"buffer_load_dwordx4 v126, s[36:39], s74 offen offset:0 sc1 lds\n"
"v_mfma_f32_16x16x32_bf16 v[20:23], v[52:55], v[76:79], v[20:23]\n"
"s_waitcnt lgkmcnt(2)\n"
"v_mfma_f32_16x16x32_bf16 v[8:11], v[56:59], v[72:75], v[8:11]\n"
"ds_read_b128 v[40:43], v124 offset:25088\n"
"s_add_u32 m0, s80, 81920\n"
"v_mfma_f32_16x16x32_bf16 v[24:27], v[56:59], v[76:79], v[24:27]\n"
"ds_read_b128 v[44:47], v124 offset:25344\n"
"v_mfma_f32_16x16x32_bf16 v[12:15], v[60:63], v[72:75], v[12:15]\n"
"buffer_load_dwordx4 v121, s[32:35], s73 offen offset:1024 lds\n"
"v_mfma_f32_16x16x32_bf16 v[28:31], v[60:63], v[76:79], v[28:31]\n"
"s_waitcnt lgkmcnt(2)\n"
"v_mfma_f32_16x16x32_bf16 v[0:3], v[32:35], a[96:99], v[0:3]\n"
"ds_read_b128 v[48:51], v124 offset:28672\n"
"s_add_u32 m0, s81, 0\n"
"v_mfma_f32_16x16x32_bf16 v[16:19], v[32:35], a[100:103], v[16:19]\n"
"ds_read_b128 v[52:55], v124 offset:28928\n"
"v_mfma_f32_16x16x32_bf16 v[4:7], v[36:39], a[96:99], v[4:7]\n"
"buffer_load_dwordx4 v126, s[36:39], s74 offen offset:1024 sc1 lds\n"
"v_mfma_f32_16x16x32_bf16 v[20:23], v[36:39], a[100:103], v[20:23]\n"
"s_waitcnt lgkmcnt(2)\n"
"v_mfma_f32_16x16x32_bf16 v[8:11], v[40:43], a[96:99], v[8:11]\n"
"ds_read_b128 v[56:59], v124 offset:29184\n"
"s_add_u32 m0, s80, 81920\n"
"v_mfma_f32_16x16x32_bf16 v[24:27], v[40:43], a[100:103], v[24:27]\n"
"ds_read_b128 v[60:63], v124 offset:29440\n"
"v_mfma_f32_16x16x32_bf16 v[12:15], v[44:47], a[96:99], v[12:15]\n"
"buffer_load_dwordx4 v122, s[32:35], s73 offen offset:2048 lds\n"
"v_mfma_f32_16x16x32_bf16 v[28:31], v[44:47], a[100:103], v[28:31]\n"
"s_waitcnt vmcnt(13) lgkmcnt(2)\n"
"s_barrier\n"
"v_mfma_f32_16x16x32_bf16 v[0:3], v[48:51], a[104:107], v[0:3]\n"
"ds_read_b128 v[32:35], v124 offset:32768\n"
"s_add_u32 m0, s81, 8192\n"
"v_mfma_f32_16x16x32_bf16 v[16:19], v[48:51], a[108:111], v[16:19]\n"
"ds_read_b128 v[36:39], v124 offset:33024\n"
"s_xor_b32 s74, s24, 212992\n"
"v_mfma_f32_16x16x32_bf16 v[4:7], v[52:55], a[104:107], v[4:7]\n"
"buffer_load_dwordx4 v126, s[36:39], s74 offen offset:0 sc1 lds\n"
"v_mfma_f32_16x16x32_bf16 v[20:23], v[52:55], a[108:111], v[20:23]\n"
"s_waitcnt lgkmcnt(2)\n"
"v_mfma_f32_16x16x32_bf16 v[8:11], v[56:59], a[104:107], v[8:11]\n"
"ds_read_b128 v[40:43], v124 offset:33280\n"
"s_add_u32 m0, s80, 81920\n"
"v_mfma_f32_16x16x32_bf16 v[24:27], v[56:59], a[108:111], v[24:27]\n"
"ds_read_b128 v[44:47], v124 offset:33536\n"
"v_mfma_f32_16x16x32_bf16 v[12:15], v[60:63], a[104:107], v[12:15]\n"
"buffer_load_dwordx4 v123, s[32:35], s73 offen offset:3072 lds\n"
"v_mfma_f32_16x16x32_bf16 v[28:31], v[60:63], a[108:111], v[28:31]\n"
"s_waitcnt lgkmcnt(2)\n"
"v_mfma_f32_16x16x32_bf16 v[0:3], v[32:35], a[112:115], v[0:3]\n"
"ds_read_b128 v[48:51], v124 offset:36864\n"
"s_add_u32 m0, s81, 8192\n"
"v_mfma_f32_16x16x32_bf16 v[16:19], v[32:35], a[116:119], v[16:19]\n"
"ds_read_b128 v[52:55], v124 offset:37120\n"
"v_mfma_f32_16x16x32_bf16 v[4:7], v[36:39], a[112:115], v[4:7]\n"
"buffer_load_dwordx4 v126, s[36:39], s74 offen offset:1024 sc1 lds\n"
"v_mfma_f32_16x16x32_bf16 v[20:23], v[36:39], a[116:119], v[20:23]\n"
"s_waitcnt lgkmcnt(2)\n"
"v_mfma_f32_16x16x32_bf16 v[8:11], v[40:43], a[112:115], v[8:11]\n"
"ds_read_b128 v[56:59], v124 offset:37376\n"
"s_add_u32 m0, s80, 98304\n"
"v_mfma_f32_16x16x32_bf16 v[24:27], v[40:43], a[116:119], v[24:27]\n"
"ds_read_b128 v[60:63], v124 offset:37632\n"
"s_add_u32 s73, s1, 32768\n"
"v_mfma_f32_16x16x32_bf16 v[12:15], v[44:47], a[112:115], v[12:15]\n"
"buffer_load_dwordx4 v89, s[44:47], s73 offen offset:0 lds\n"
"v_mfma_f32_16x16x32_bf16 v[28:31], v[44:47], a[116:119], v[28:31]\n"
"s_waitcnt lgkmcnt(2)\n"
"v_mfma_f32_16x16x32_bf16 v[0:3], v[48:51], a[120:123], v[0:3]\n"
"ds_read_b128 v[32:35], v124 offset:40960\n"
"s_add_u32 m0, s81, 16384\n"
"v_mfma_f32_16x16x32_bf16 v[16:19], v[48:51], a[124:127], v[16:19]\n"
"ds_read_b128 v[36:39], v124 offset:41216\n"
"s_xor_b32 s74, s24, 221184\n"
"v_mfma_f32_16x16x32_bf16 v[4:7], v[52:55], a[120:123], v[4:7]\n"
"ds_read_b128 v[64:67], v116 offset:49152\n"
"buffer_load_dwordx4 v126, s[36:39], s74 offen offset:0 sc1 lds\n"
"v_mfma_f32_16x16x32_bf16 v[20:23], v[52:55], a[124:127], v[20:23]\n"
"ds_read_b128 v[68:71], v116 offset:51200\n"
"s_waitcnt lgkmcnt(4)\n"
"v_mfma_f32_16x16x32_bf16 v[8:11], v[56:59], a[120:123], v[8:11]\n"
"ds_read_b128 v[40:43], v124 offset:41472\n"
"s_add_u32 m0, s80, 98304\n"
"v_mfma_f32_16x16x32_bf16 v[24:27], v[56:59], a[124:127], v[24:27]\n"
"ds_read_b128 v[44:47], v124 offset:41728\n"
"v_mfma_f32_16x16x32_bf16 v[12:15], v[60:63], a[120:123], v[12:15]\n"
"buffer_load_dwordx4 v90, s[44:47], s73 offen offset:1024 lds\n"
"v_mfma_f32_16x16x32_bf16 v[28:31], v[60:63], a[124:127], v[28:31]\n"
"s_waitcnt lgkmcnt(2)\n"
"v_mfma_f32_16x16x32_bf16 v[0:3], v[32:35], v[64:67], v[0:3]\n"
"ds_read_b128 v[48:51], v124 offset:45056\n"
"s_add_u32 m0, s81, 16384\n"
"v_mfma_f32_16x16x32_bf16 v[16:19], v[32:35], v[68:71], v[16:19]\n"
"ds_read_b128 v[52:55], v124 offset:45312\n"
"v_mfma_f32_16x16x32_bf16 v[4:7], v[36:39], v[64:67], v[4:7]\n"
"ds_read_b128 v[72:75], v117 offset:49152\n"
"buffer_load_dwordx4 v126, s[36:39], s74 offen offset:1024 sc1 lds\n"
"v_mfma_f32_16x16x32_bf16 v[20:23], v[36:39], v[68:71], v[20:23]\n"
"ds_read_b128 v[76:79], v117 offset:51200\n"
"s_waitcnt lgkmcnt(4)\n"
"v_mfma_f32_16x16x32_bf16 v[8:11], v[40:43], v[64:67], v[8:11]\n"
"ds_read_b128 v[56:59], v124 offset:45568\n"
"s_add_u32 m0, s80, 98304\n"
"v_mfma_f32_16x16x32_bf16 v[24:27], v[40:43], v[68:71], v[24:27]\n"
"ds_read_b128 v[60:63], v124 offset:45824\n"
"v_mfma_f32_16x16x32_bf16 v[12:15], v[44:47], v[64:67], v[12:15]\n"
"buffer_load_dwordx4 v91, s[44:47], s73 offen offset:2048 lds\n"
"v_mfma_f32_16x16x32_bf16 v[28:31], v[44:47], v[68:71], v[28:31]\n"
"s_waitcnt vmcnt(13) lgkmcnt(2)\n"
"s_barrier\n"
"v_mfma_f32_16x16x32_bf16 v[0:3], v[48:51], v[72:75], v[0:3]\n"
"ds_read_b128 v[32:35], v124 offset:49152\n"
"s_add_u32 m0, s81, 24576\n"
"v_mfma_f32_16x16x32_bf16 v[16:19], v[48:51], v[76:79], v[16:19]\n"
"ds_read_b128 v[36:39], v124 offset:49408\n"
"s_xor_b32 s74, s24, 204800\n"
"v_mfma_f32_16x16x32_bf16 v[4:7], v[52:55], v[72:75], v[4:7]\n"
"buffer_load_dwordx4 v126, s[36:39], s74 offen offset:0 sc1 lds\n"
"v_mfma_f32_16x16x32_bf16 v[20:23], v[52:55], v[76:79], v[20:23]\n"
"s_waitcnt lgkmcnt(2)\n"
"v_mfma_f32_16x16x32_bf16 v[8:11], v[56:59], v[72:75], v[8:11]\n"
"ds_read_b128 v[40:43], v124 offset:49664\n"
"s_add_u32 m0, s80, 98304\n"
"v_mfma_f32_16x16x32_bf16 v[24:27], v[56:59], v[76:79], v[24:27]\n"
"ds_read_b128 v[44:47], v124 offset:49920\n"
"v_mfma_f32_16x16x32_bf16 v[12:15], v[60:63], v[72:75], v[12:15]\n"
"buffer_load_dwordx4 v92, s[44:47], s73 offen offset:3072 lds\n"
"v_mfma_f32_16x16x32_bf16 v[28:31], v[60:63], v[76:79], v[28:31]\n"
"s_waitcnt lgkmcnt(2)\n"
"v_mfma_f32_16x16x32_bf16 v[0:3], v[32:35], a[128:131], v[0:3]\n"
"ds_read_b128 v[48:51], v124 offset:53248\n"
"s_add_u32 m0, s81, 24576\n"
"v_mfma_f32_16x16x32_bf16 v[16:19], v[32:35], a[132:135], v[16:19]\n"
"ds_read_b128 v[52:55], v124 offset:53504\n"
"v_mfma_f32_16x16x32_bf16 v[4:7], v[36:39], a[128:131], v[4:7]\n"
"buffer_load_dwordx4 v126, s[36:39], s74 offen offset:1024 sc1 lds\n"
"v_mfma_f32_16x16x32_bf16 v[20:23], v[36:39], a[132:135], v[20:23]\n"
"s_waitcnt lgkmcnt(2)\n"
"v_mfma_f32_16x16x32_bf16 v[8:11], v[40:43], a[128:131], v[8:11]\n"
"ds_read_b128 v[56:59], v124 offset:53760\n"
"v_mfma_f32_16x16x32_bf16 v[24:27], v[40:43], a[132:135], v[24:27]\n"
"ds_read_b128 v[60:63], v124 offset:54016\n"
"v_mfma_f32_16x16x32_bf16 v[12:15], v[44:47], a[128:131], v[12:15]\n"
"v_mfma_f32_16x16x32_bf16 v[28:31], v[44:47], a[132:135], v[28:31]\n"
"s_waitcnt lgkmcnt(2)\n"
"v_mfma_f32_16x16x32_bf16 v[0:3], v[48:51], a[136:139], v[0:3]\n"
"ds_read_b128 v[32:35], v124 offset:57344\n"
"s_add_u32 m0, s81, 32768\n"
"v_mfma_f32_16x16x32_bf16 v[16:19], v[48:51], a[140:143], v[16:19]\n"
"ds_read_b128 v[36:39], v124 offset:57600\n"
"s_xor_b32 s74, s24, 229376\n"
"v_mfma_f32_16x16x32_bf16 v[4:7], v[52:55], a[136:139], v[4:7]\n"
"buffer_load_dwordx4 v126, s[36:39], s74 offen offset:0 sc1 lds\n"
"v_mfma_f32_16x16x32_bf16 v[20:23], v[52:55], a[140:143], v[20:23]\n"
"s_waitcnt lgkmcnt(2)\n"
"v_mfma_f32_16x16x32_bf16 v[8:11], v[56:59], a[136:139], v[8:11]\n"
"ds_read_b128 v[40:43], v124 offset:57856\n"
"s_add_u32 m0, s80, 114688\n"
"v_mfma_f32_16x16x32_bf16 v[24:27], v[56:59], a[140:143], v[24:27]\n"
"ds_read_b128 v[44:47], v124 offset:58112\n"
"s_add_u32 s73, s1, 36864\n"
"v_mfma_f32_16x16x32_bf16 v[12:15], v[60:63], a[136:139], v[12:15]\n"
"buffer_load_dwordx4 v89, s[44:47], s73 offen offset:0 lds\n"
"v_mfma_f32_16x16x32_bf16 v[28:31], v[60:63], a[140:143], v[28:31]\n"
"s_waitcnt lgkmcnt(2)\n"
"v_mfma_f32_16x16x32_bf16 v[0:3], v[32:35], a[144:147], v[0:3]\n"
"ds_read_b128 v[48:51], v124 offset:61440\n"
"s_add_u32 m0, s81, 32768\n"
"v_mfma_f32_16x16x32_bf16 v[16:19], v[32:35], a[148:151], v[16:19]\n"
"ds_read_b128 v[52:55], v124 offset:61696\n"
"v_mfma_f32_16x16x32_bf16 v[4:7], v[36:39], a[144:147], v[4:7]\n"
"buffer_load_dwordx4 v126, s[36:39], s74 offen offset:1024 sc1 lds\n"
"v_mfma_f32_16x16x32_bf16 v[20:23], v[36:39], a[148:151], v[20:23]\n"
"s_waitcnt lgkmcnt(2)\n"
"v_mfma_f32_16x16x32_bf16 v[8:11], v[40:43], a[144:147], v[8:11]\n"
"ds_read_b128 v[56:59], v124 offset:61952\n"
"s_add_u32 m0, s80, 114688\n"
"v_mfma_f32_16x16x32_bf16 v[24:27], v[40:43], a[148:151], v[24:27]\n"
"ds_read_b128 v[60:63], v124 offset:62208\n"
"v_mfma_f32_16x16x32_bf16 v[12:15], v[44:47], a[144:147], v[12:15]\n"
"buffer_load_dwordx4 v90, s[44:47], s73 offen offset:1024 lds\n"
"v_mfma_f32_16x16x32_bf16 v[28:31], v[44:47], a[148:151], v[28:31]\n"
"s_waitcnt vmcnt(12) lgkmcnt(2)\n"
"s_barrier\n"
"v_mfma_f32_16x16x32_bf16 v[0:3], v[48:51], a[152:155], v[0:3]\n"
"ds_read_b128 v[32:35], v124 offset:0\n"
"s_add_u32 m0, s81, 40960\n"
"v_mfma_f32_16x16x32_bf16 v[16:19], v[48:51], a[156:159], v[16:19]\n"
"ds_read_b128 v[36:39], v124 offset:256\n"
"s_xor_b32 s74, s24, 237568\n"
"v_mfma_f32_16x16x32_bf16 v[4:7], v[52:55], a[152:155], v[4:7]\n"
"ds_read_b128 v[64:67], v116 offset:0\n"
"buffer_load_dwordx4 v126, s[36:39], s74 offen offset:0 sc1 lds\n"
"v_mfma_f32_16x16x32_bf16 v[20:23], v[52:55], a[156:159], v[20:23]\n"
"ds_read_b128 v[68:71], v116 offset:2048\n"
"s_waitcnt lgkmcnt(4)\n"
"v_mfma_f32_16x16x32_bf16 v[8:11], v[56:59], a[152:155], v[8:11]\n"
"ds_read_b128 v[40:43], v124 offset:512\n"
"s_add_u32 m0, s80, 114688\n"
"v_mfma_f32_16x16x32_bf16 v[24:27], v[56:59], a[156:159], v[24:27]\n"
"ds_read_b128 v[44:47], v124 offset:768\n"
"v_mfma_f32_16x16x32_bf16 v[12:15], v[60:63], a[152:155], v[12:15]\n"
"buffer_load_dwordx4 v91, s[44:47], s73 offen offset:2048 lds\n"
"v_mfma_f32_16x16x32_bf16 v[28:31], v[60:63], a[156:159], v[28:31]\n"
"s_waitcnt lgkmcnt(2)\n"
"v_mfma_f32_16x16x32_bf16 v[0:3], v[32:35], v[64:67], v[0:3]\n"
"ds_read_b128 v[48:51], v124 offset:4096\n"
"s_add_u32 m0, s81, 40960\n"
"v_mfma_f32_16x16x32_bf16 v[16:19], v[32:35], v[68:71], v[16:19]\n"
"ds_read_b128 v[52:55], v124 offset:4352\n"
"v_mfma_f32_16x16x32_bf16 v[4:7], v[36:39], v[64:67], v[4:7]\n"
"ds_read_b128 v[72:75], v117 offset:0\n"
"buffer_load_dwordx4 v126, s[36:39], s74 offen offset:1024 sc1 lds\n"
"v_mfma_f32_16x16x32_bf16 v[20:23], v[36:39], v[68:71], v[20:23]\n"
"ds_read_b128 v[76:79], v117 offset:2048\n"
"s_waitcnt lgkmcnt(4)\n"
"v_mfma_f32_16x16x32_bf16 v[8:11], v[40:43], v[64:67], v[8:11]\n"
"ds_read_b128 v[56:59], v124 offset:4608\n"
"s_add_u32 m0, s80, 114688\n"
"v_mfma_f32_16x16x32_bf16 v[24:27], v[40:43], v[68:71], v[24:27]\n"
"ds_read_b128 v[60:63], v124 offset:4864\n"
"v_mfma_f32_16x16x32_bf16 v[12:15], v[44:47], v[64:67], v[12:15]\n"
"buffer_load_dwordx4 v92, s[44:47], s73 offen offset:3072 lds\n"
"v_mfma_f32_16x16x32_bf16 v[28:31], v[44:47], v[68:71], v[28:31]\n"
"s_waitcnt lgkmcnt(2)\n"
"s_barrier\n"
"v_mfma_f32_16x16x32_bf16 v[0:3], v[48:51], v[72:75], v[0:3]\n"
"ds_read_b128 v[32:35], v124 offset:8192\n"
"s_add_u32 m0, s81, 49152\n"
"v_mfma_f32_16x16x32_bf16 v[16:19], v[48:51], v[76:79], v[16:19]\n"
"ds_read_b128 v[36:39], v124 offset:8448\n"
"s_xor_b32 s74, s24, 245760\n"
"v_mfma_f32_16x16x32_bf16 v[4:7], v[52:55], v[72:75], v[4:7]\n"
"buffer_load_dwordx4 v126, s[36:39], s74 offen offset:0 sc1 lds\n"
"v_mfma_f32_16x16x32_bf16 v[20:23], v[52:55], v[76:79], v[20:23]\n"
"s_waitcnt lgkmcnt(2)\n"
"v_mfma_f32_16x16x32_bf16 v[8:11], v[56:59], v[72:75], v[8:11]\n"
"ds_read_b128 v[40:43], v124 offset:8704\n"
"v_mfma_f32_16x16x32_bf16 v[24:27], v[56:59], v[76:79], v[24:27]\n"
"ds_read_b128 v[44:47], v124 offset:8960\n"
"v_mfma_f32_16x16x32_bf16 v[12:15], v[60:63], v[72:75], v[12:15]\n"
"v_mfma_f32_16x16x32_bf16 v[28:31], v[60:63], v[76:79], v[28:31]\n"
"s_waitcnt lgkmcnt(2)\n"
"v_mfma_f32_16x16x32_bf16 v[0:3], v[32:35], a[160:163], v[0:3]\n"
"ds_read_b128 v[48:51], v124 offset:12288\n"
"s_lshl_b32 s99, s84, 2\n"
"s_add_u32 s99, s99, s71\n"
"v_mfma_f32_16x16x32_bf16 v[16:19], v[32:35], a[164:167], v[16:19]\n"
"ds_read_b128 v[52:55], v124 offset:12544\n"
"s_mul_i32 s99, s99, 256\n"
"v_lshl_add_u32 v94, v127, 7, s99\n"
"v_mfma_f32_16x16x32_bf16 v[4:7], v[36:39], a[160:163], v[4:7]\n"
"buffer_load_dwordx4 v126, s[36:39], s74 offen offset:1024 sc1 lds\n"
"v_mfma_f32_16x16x32_bf16 v[20:23], v[36:39], a[164:167], v[20:23]\n"
"v_min_u32 v94, END_%=-PC2_%=, v94\n"
"s_waitcnt lgkmcnt(2)\n"
"v_mfma_f32_16x16x32_bf16 v[8:11], v[40:43], a[160:163], v[8:11]\n"
"ds_read_b128 v[56:59], v124 offset:12800\n"
"v_mfma_f32_16x16x32_bf16 v[24:27], v[40:43], a[164:167], v[24:27]\n"
"ds_read_b128 v[60:63], v124 offset:13056\n"
"v_mfma_f32_16x16x32_bf16 v[12:15], v[44:47], a[160:163], v[12:15]\n"
"s_getpc_b64 s[26:27]\n"
"PC2_%=:\n"
"s_mov_b64 exec, 3\n"
"global_load_dword v95, v94, s[26:27]\n"
"s_mov_b64 exec, -1\n"
"v_mfma_f32_16x16x32_bf16 v[28:31], v[44:47], a[164:167], v[28:31]\n"
"s_waitcnt vmcnt(15) lgkmcnt(2)\n"
"s_barrier\n"
"v_mfma_f32_16x16x32_bf16 v[0:3], v[48:51], a[168:171], v[0:3]\n"
"ds_read_b128 v[32:35], v124 offset:16384\n"
"s_add_u32 m0, s81, 57344\n"
"v_mfma_f32_16x16x32_bf16 v[16:19], v[48:51], a[172:175], v[16:19]\n"
"ds_read_b128 v[36:39], v124 offset:16640\n"
"s_xor_b32 s74, s24, 253952\n"
"v_mfma_f32_16x16x32_bf16 v[4:7], v[52:55], a[168:171], v[4:7]\n"
"buffer_load_dwordx4 v126, s[36:39], s74 offen offset:0 sc1 lds\n"
"v_mfma_f32_16x16x32_bf16 v[20:23], v[52:55], a[172:175], v[20:23]\n"
"s_waitcnt lgkmcnt(2)\n"
"v_mfma_f32_16x16x32_bf16 v[8:11], v[56:59], a[168:171], v[8:11]\n"
"ds_read_b128 v[40:43], v124 offset:16896\n"
"s_add_u32 m0, s80, 65536\n"
"v_mfma_f32_16x16x32_bf16 v[24:27], v[56:59], a[172:175], v[24:27]\n"
"ds_read_b128 v[44:47], v124 offset:17152\n"
"s_add_u32 s73, s1, 0\n"
"v_mfma_f32_16x16x32_bf16 v[12:15], v[60:63], a[168:171], v[12:15]\n"
"buffer_load_dwordx4 v89, s[44:47], s73 offen offset:0 lds\n"
"v_mfma_f32_16x16x32_bf16 v[28:31], v[60:63], a[172:175], v[28:31]\n"
"s_waitcnt lgkmcnt(2)\n"
"v_mfma_f32_16x16x32_bf16 v[0:3], v[32:35], a[176:179], v[0:3]\n"
"ds_read_b128 v[48:51], v124 offset:20480\n"
"s_add_u32 m0, s81, 57344\n"
"v_mfma_f32_16x16x32_bf16 v[16:19], v[32:35], a[180:183], v[16:19]\n"
"ds_read_b128 v[52:55], v124 offset:20736\n"
"v_mfma_f32_16x16x32_bf16 v[4:7], v[36:39], a[176:179], v[4:7]\n"
"buffer_load_dwordx4 v126, s[36:39], s74 offen offset:1024 sc1 lds\n"
"v_mfma_f32_16x16x32_bf16 v[20:23], v[36:39], a[180:183], v[20:23]\n"
"s_waitcnt lgkmcnt(2)\n"
"v_mfma_f32_16x16x32_bf16 v[8:11], v[40:43], a[176:179], v[8:11]\n"
"ds_read_b128 v[56:59], v124 offset:20992\n"
"s_add_u32 m0, s80, 65536\n"
"v_mfma_f32_16x16x32_bf16 v[24:27], v[40:43], a[180:183], v[24:27]\n"
"ds_read_b128 v[60:63], v124 offset:21248\n"
"v_mfma_f32_16x16x32_bf16 v[12:15], v[44:47], a[176:179], v[12:15]\n"
"buffer_load_dwordx4 v90, s[44:47], s73 offen offset:1024 lds\n"
"v_mfma_f32_16x16x32_bf16 v[28:31], v[44:47], a[180:183], v[28:31]\n"
"s_waitcnt vmcnt(15) lgkmcnt(2)\n"
"s_barrier\n"
"v_mfma_f32_16x16x32_bf16 v[0:3], v[48:51], a[184:187], v[0:3]\n"
"ds_read_b128 v[32:35], v124 offset:24576\n"
"v_mfma_f32_16x16x32_bf16 v[16:19], v[48:51], a[188:191], v[16:19]\n"
"ds_read_b128 v[36:39], v124 offset:24832\n"
"v_mfma_f32_16x16x32_bf16 v[4:7], v[52:55], a[184:187], v[4:7]\n"
"ds_read_b128 v[64:67], v116 offset:16384\n"
"buffer_load_dwordx4 v91, s[44:47], s73 offen offset:2048 lds\n"
"v_mfma_f32_16x16x32_bf16 v[20:23], v[52:55], a[188:191], v[20:23]\n"
"ds_read_b128 v[68:71], v116 offset:18432\n"
"s_waitcnt lgkmcnt(4)\n"
"v_mfma_f32_16x16x32_bf16 v[8:11], v[56:59], a[184:187], v[8:11]\n"
"ds_read_b128 v[40:43], v124 offset:25088\n"
"v_mfma_f32_16x16x32_bf16 v[24:27], v[56:59], a[188:191], v[24:27]\n"
"ds_read_b128 v[44:47], v124 offset:25344\n"
"v_mfma_f32_16x16x32_bf16 v[12:15], v[60:63], a[184:187], v[12:15]\n"
"buffer_load_dwordx4 v92, s[44:47], s73 offen offset:3072 lds\n"
"v_mfma_f32_16x16x32_bf16 v[28:31], v[60:63], a[188:191], v[28:31]\n"
"s_waitcnt lgkmcnt(2)\n"
"v_mfma_f32_16x16x32_bf16 v[0:3], v[32:35], v[64:67], v[0:3]\n"
"ds_read_b128 v[48:51], v124 offset:28672\n"
"s_add_u32 m0, s80, 0\n"
"v_mfma_f32_16x16x32_bf16 v[16:19], v[32:35], v[68:71], v[16:19]\n"
"ds_read_b128 v[52:55], v124 offset:28928\n"
"s_add_u32 s73, s1, 0\n"
"v_mfma_f32_16x16x32_bf16 v[4:7], v[36:39], v[64:67], v[4:7]\n"
"ds_read_b128 v[72:75], v117 offset:16384\n"
"buffer_load_dwordx4 v89, s[40:43], s73 offen offset:0 lds\n"
"v_mfma_f32_16x16x32_bf16 v[20:23], v[36:39], v[68:71], v[20:23]\n"
"ds_read_b128 v[76:79], v117 offset:18432\n"
"s_waitcnt lgkmcnt(4)\n"
"v_mfma_f32_16x16x32_bf16 v[8:11], v[40:43], v[64:67], v[8:11]\n"
"ds_read_b128 v[56:59], v124 offset:29184\n"
"v_mfma_f32_16x16x32_bf16 v[24:27], v[40:43], v[68:71], v[24:27]\n"
"ds_read_b128 v[60:63], v124 offset:29440\n"
"v_mfma_f32_16x16x32_bf16 v[12:15], v[44:47], v[64:67], v[12:15]\n"
"buffer_load_dwordx4 v90, s[40:43], s73 offen offset:1024 lds\n"
"v_mfma_f32_16x16x32_bf16 v[28:31], v[44:47], v[68:71], v[28:31]\n"
"s_waitcnt vmcnt(16) lgkmcnt(2)\n"
"s_barrier\n"
"v_mfma_f32_16x16x32_bf16 v[0:3], v[48:51], v[72:75], v[0:3]\n"
"ds_read_b128 v[32:35], v124 offset:32768\n"
"v_mfma_f32_16x16x32_bf16 v[16:19], v[48:51], v[76:79], v[16:19]\n"
"ds_read_b128 v[36:39], v124 offset:33024\n"
"v_mfma_f32_16x16x32_bf16 v[4:7], v[52:55], v[72:75], v[4:7]\n"
"buffer_load_dwordx4 v91, s[40:43], s73 offen offset:2048 lds\n"
"v_mfma_f32_16x16x32_bf16 v[20:23], v[52:55], v[76:79], v[20:23]\n"
"s_waitcnt lgkmcnt(2)\n"
"v_mfma_f32_16x16x32_bf16 v[8:11], v[56:59], v[72:75], v[8:11]\n"
"ds_read_b128 v[40:43], v124 offset:33280\n"
"v_mfma_f32_16x16x32_bf16 v[24:27], v[56:59], v[76:79], v[24:27]\n"
"ds_read_b128 v[44:47], v124 offset:33536\n"
"v_mfma_f32_16x16x32_bf16 v[12:15], v[60:63], v[72:75], v[12:15]\n"
"buffer_load_dwordx4 v92, s[40:43], s73 offen offset:3072 lds\n"
"v_mfma_f32_16x16x32_bf16 v[28:31], v[60:63], v[76:79], v[28:31]\n"
"s_waitcnt lgkmcnt(2)\n"
"v_mfma_f32_16x16x32_bf16 v[0:3], v[32:35], a[192:195], v[0:3]\n"
"ds_read_b128 v[48:51], v124 offset:36864\n"
"v_mfma_f32_16x16x32_bf16 v[16:19], v[32:35], a[196:199], v[16:19]\n"
"ds_read_b128 v[52:55], v124 offset:37120\n"
"v_mfma_f32_16x16x32_bf16 v[4:7], v[36:39], a[192:195], v[4:7]\n"
"v_mfma_f32_16x16x32_bf16 v[20:23], v[36:39], a[196:199], v[20:23]\n"
"s_waitcnt lgkmcnt(2)\n"
"v_mfma_f32_16x16x32_bf16 v[8:11], v[40:43], a[192:195], v[8:11]\n"
"ds_read_b128 v[56:59], v124 offset:37376\n"
"v_mfma_f32_16x16x32_bf16 v[24:27], v[40:43], a[196:199], v[24:27]\n"
"ds_read_b128 v[60:63], v124 offset:37632\n"
"v_mfma_f32_16x16x32_bf16 v[12:15], v[44:47], a[192:195], v[12:15]\n"
"v_mfma_f32_16x16x32_bf16 v[28:31], v[44:47], a[196:199], v[28:31]\n"
"s_waitcnt vmcnt(14) lgkmcnt(2)\n"
"s_barrier\n"
"v_mfma_f32_16x16x32_bf16 v[0:3], v[48:51], a[200:203], v[0:3]\n"
"ds_read_b128 v[32:35], v124 offset:40960\n"
"s_add_u32 m0, s80, 81920\n"
"v_mfma_f32_16x16x32_bf16 v[16:19], v[48:51], a[204:207], v[16:19]\n"
"ds_read_b128 v[36:39], v124 offset:41216\n"
"s_add_u32 s73, s1, 4096\n"
"v_mfma_f32_16x16x32_bf16 v[4:7], v[52:55], a[200:203], v[4:7]\n"
"buffer_load_dwordx4 v89, s[44:47], s73 offen offset:0 lds\n"
"v_mfma_f32_16x16x32_bf16 v[20:23], v[52:55], a[204:207], v[20:23]\n"
"s_waitcnt lgkmcnt(2)\n"
"v_mfma_f32_16x16x32_bf16 v[8:11], v[56:59], a[200:203], v[8:11]\n"
"ds_read_b128 v[40:43], v124 offset:41472\n"
"v_mfma_f32_16x16x32_bf16 v[24:27], v[56:59], a[204:207], v[24:27]\n"
"ds_read_b128 v[44:47], v124 offset:41728\n"
"v_mfma_f32_16x16x32_bf16 v[12:15], v[60:63], a[200:203], v[12:15]\n"
"buffer_load_dwordx4 v90, s[44:47], s73 offen offset:1024 lds\n"
"v_mfma_f32_16x16x32_bf16 v[28:31], v[60:63], a[204:207], v[28:31]\n"
"s_waitcnt lgkmcnt(2)\n"
"v_mfma_f32_16x16x32_bf16 v[0:3], v[32:35], a[208:211], v[0:3]\n"
"ds_read_b128 v[48:51], v124 offset:45056\n"
"v_mfma_f32_16x16x32_bf16 v[16:19], v[32:35], a[212:215], v[16:19]\n"
"ds_read_b128 v[52:55], v124 offset:45312\n"
"v_mfma_f32_16x16x32_bf16 v[4:7], v[36:39], a[208:211], v[4:7]\n"
"buffer_load_dwordx4 v91, s[44:47], s73 offen offset:2048 lds\n"
"v_mfma_f32_16x16x32_bf16 v[20:23], v[36:39], a[212:215], v[20:23]\n"
"s_waitcnt lgkmcnt(2)\n"
"v_mfma_f32_16x16x32_bf16 v[8:11], v[40:43], a[208:211], v[8:11]\n"
"ds_read_b128 v[56:59], v124 offset:45568\n"
"v_mfma_f32_16x16x32_bf16 v[24:27], v[40:43], a[212:215], v[24:27]\n"
"ds_read_b128 v[60:63], v124 offset:45824\n"
"v_mfma_f32_16x16x32_bf16 v[12:15], v[44:47], a[208:211], v[12:15]\n"
"buffer_load_dwordx4 v92, s[44:47], s73 offen offset:3072 lds\n"
"v_mfma_f32_16x16x32_bf16 v[28:31], v[44:47], a[212:215], v[28:31]\n"
"s_waitcnt vmcnt(15) lgkmcnt(2)\n"
"s_barrier\n"
"v_mfma_f32_16x16x32_bf16 v[0:3], v[48:51], a[216:219], v[0:3]\n"
"ds_read_b128 v[32:35], v124 offset:49152\n"
"s_add_u32 m0, s80, 16384\n"
"v_mfma_f32_16x16x32_bf16 v[16:19], v[48:51], a[220:223], v[16:19]\n"
"ds_read_b128 v[36:39], v124 offset:49408\n"
"s_add_u32 s73, s1, 4096\n"
"v_mfma_f32_16x16x32_bf16 v[4:7], v[52:55], a[216:219], v[4:7]\n"
"buffer_load_dwordx4 v89, s[40:43], s73 offen offset:0 lds\n"
"v_mfma_f32_16x16x32_bf16 v[20:23], v[52:55], a[220:223], v[20:23]\n"
"s_waitcnt lgkmcnt(2)\n"
"v_mfma_f32_16x16x32_bf16 v[8:11], v[56:59], a[216:219], v[8:11]\n"
"ds_read_b128 v[40:43], v124 offset:49664\n"
"v_mfma_f32_16x16x32_bf16 v[24:27], v[56:59], a[220:223], v[24:27]\n"
"ds_read_b128 v[44:47], v124 offset:49920\n"
"v_mfma_f32_16x16x32_bf16 v[12:15], v[60:63], a[216:219], v[12:15]\n"
"buffer_load_dwordx4 v90, s[40:43], s73 offen offset:1024 lds\n"
"v_mfma_f32_16x16x32_bf16 v[28:31], v[60:63], a[220:223], v[28:31]\n"
"s_waitcnt lgkmcnt(2)\n"
"v_mfma_f32_16x16x32_bf16 v[0:3], v[32:35], a[224:227], v[0:3]\n"
"ds_read_b128 v[48:51], v124 offset:53248\n"
"v_mfma_f32_16x16x32_bf16 v[16:19], v[32:35], a[228:231], v[16:19]\n"
"ds_read_b128 v[52:55], v124 offset:53504\n"
"v_mfma_f32_16x16x32_bf16 v[4:7], v[36:39], a[224:227], v[4:7]\n"
"buffer_load_dwordx4 v91, s[40:43], s73 offen offset:2048 lds\n"
"v_mfma_f32_16x16x32_bf16 v[20:23], v[36:39], a[228:231], v[20:23]\n"
"s_waitcnt lgkmcnt(2)\n"
"v_mfma_f32_16x16x32_bf16 v[8:11], v[40:43], a[224:227], v[8:11]\n"
"ds_read_b128 v[56:59], v124 offset:53760\n"
"v_mfma_f32_16x16x32_bf16 v[24:27], v[40:43], a[228:231], v[24:27]\n"
"ds_read_b128 v[60:63], v124 offset:54016\n"
"v_mfma_f32_16x16x32_bf16 v[12:15], v[44:47], a[224:227], v[12:15]\n"
"buffer_load_dwordx4 v92, s[40:43], s73 offen offset:3072 lds\n"
"v_mfma_f32_16x16x32_bf16 v[28:31], v[44:47], a[228:231], v[28:31]\n"
"s_waitcnt vmcnt(15) lgkmcnt(2)\n"
"s_barrier\n"
"v_mfma_f32_16x16x32_bf16 v[0:3], v[48:51], a[232:235], v[0:3]\n"
"ds_read_b128 v[32:35], v124 offset:57344\n"
"s_add_u32 m0, s80, 32768\n"
"v_mfma_f32_16x16x32_bf16 v[16:19], v[48:51], a[236:239], v[16:19]\n"
"ds_read_b128 v[36:39], v124 offset:57600\n"
"s_add_u32 s73, s1, 32768\n"
"v_mfma_f32_16x16x32_bf16 v[4:7], v[52:55], a[232:235], v[4:7]\n"
"buffer_load_dwordx4 v89, s[40:43], s73 offen offset:0 lds\n"
"v_mfma_f32_16x16x32_bf16 v[20:23], v[52:55], a[236:239], v[20:23]\n"
"s_waitcnt lgkmcnt(2)\n"
"v_mfma_f32_16x16x32_bf16 v[8:11], v[56:59], a[232:235], v[8:11]\n"
"ds_read_b128 v[40:43], v124 offset:57856\n"
"v_mfma_f32_16x16x32_bf16 v[24:27], v[56:59], a[236:239], v[24:27]\n"
"ds_read_b128 v[44:47], v124 offset:58112\n"
"v_mfma_f32_16x16x32_bf16 v[12:15], v[60:63], a[232:235], v[12:15]\n"
"buffer_load_dwordx4 v90, s[40:43], s73 offen offset:1024 lds\n"
"v_mfma_f32_16x16x32_bf16 v[28:31], v[60:63], a[236:239], v[28:31]\n"
"s_waitcnt lgkmcnt(2)\n"
"v_mfma_f32_16x16x32_bf16 v[0:3], v[32:35], a[240:243], v[0:3]\n"
"ds_read_b128 v[48:51], v124 offset:61440\n"
"v_mfma_f32_16x16x32_bf16 v[16:19], v[32:35], a[244:247], v[16:19]\n"
"ds_read_b128 v[52:55], v124 offset:61696\n"
"v_mfma_f32_16x16x32_bf16 v[4:7], v[36:39], a[240:243], v[4:7]\n"
"buffer_load_dwordx4 v91, s[40:43], s73 offen offset:2048 lds\n"
"v_mfma_f32_16x16x32_bf16 v[20:23], v[36:39], a[244:247], v[20:23]\n"
"s_waitcnt lgkmcnt(2)\n"
"v_mfma_f32_16x16x32_bf16 v[8:11], v[40:43], a[240:243], v[8:11]\n"
"ds_read_b128 v[56:59], v124 offset:61952\n"
"v_mfma_f32_16x16x32_bf16 v[24:27], v[40:43], a[244:247], v[24:27]\n"
"ds_read_b128 v[60:63], v124 offset:62208\n"
"v_mfma_f32_16x16x32_bf16 v[12:15], v[44:47], a[240:243], v[12:15]\n"
"buffer_load_dwordx4 v92, s[40:43], s73 offen offset:3072 lds\n"
"v_mfma_f32_16x16x32_bf16 v[28:31], v[44:47], a[244:247], v[28:31]\n"
"s_waitcnt lgkmcnt(2)\n"
"v_mfma_f32_16x16x32_bf16 v[0:3], v[48:51], a[248:251], v[0:3]\n"
"v_mfma_f32_16x16x32_bf16 v[16:19], v[48:51], a[252:255], v[16:19]\n"
"v_mfma_f32_16x16x32_bf16 v[4:7], v[52:55], a[248:251], v[4:7]\n"
"v_mfma_f32_16x16x32_bf16 v[20:23], v[52:55], a[252:255], v[20:23]\n"
"s_waitcnt lgkmcnt(0)\n"
"v_mfma_f32_16x16x32_bf16 v[8:11], v[56:59], a[248:251], v[8:11]\n"
"v_mfma_f32_16x16x32_bf16 v[24:27], v[56:59], a[252:255], v[24:27]\n"
"v_mfma_f32_16x16x32_bf16 v[12:15], v[60:63], a[248:251], v[12:15]\n"
"v_mfma_f32_16x16x32_bf16 v[28:31], v[60:63], a[252:255], v[28:31]\n"
"v_cvt_pk_bf16_f32 v64, v0, v1\n"
"v_cvt_pk_bf16_f32 v65, v2, v3\n"
"v_cvt_pk_bf16_f32 v66, v4, v5\n"
"v_cvt_pk_bf16_f32 v67, v6, v7\n"
"v_xor_b32 v64, s83, v64\n"
"v_xor_b32 v65, s83, v65\n"
"v_xor_b32 v66, s83, v66\n"
"v_xor_b32 v67, s83, v67\n"
"buffer_store_dwordx4 v[64:67], v126, s[36:39], s28 offen offset:0 sc1\n"
"v_cvt_pk_bf16_f32 v72, v16, v17\n"
"v_cvt_pk_bf16_f32 v73, v18, v19\n"
"v_cvt_pk_bf16_f32 v74, v20, v21\n"
"v_cvt_pk_bf16_f32 v75, v22, v23\n"
"v_xor_b32 v72, s83, v72\n"
"v_xor_b32 v73, s83, v73\n"
"v_xor_b32 v74, s83, v74\n"
"v_xor_b32 v75, s83, v75\n"
"buffer_store_dwordx4 v[72:75], v126, s[36:39], s29 offen offset:0 sc1\n"
"v_cvt_pk_bf16_f32 v68, v8, v9\n"
"v_cvt_pk_bf16_f32 v69, v10, v11\n"
"v_cvt_pk_bf16_f32 v70, v12, v13\n"
"v_cvt_pk_bf16_f32 v71, v14, v15\n"
"v_xor_b32 v68, s83, v68\n"
"v_xor_b32 v69, s83, v69\n"
"v_xor_b32 v70, s83, v70\n"
"v_xor_b32 v71, s83, v71\n"
"buffer_store_dwordx4 v[68:71], v126, s[36:39], s28 offen offset:1024 sc1\n"
"v_cvt_pk_bf16_f32 v76, v24, v25\n"
"v_cvt_pk_bf16_f32 v77, v26, v27\n"
"v_cvt_pk_bf16_f32 v78, v28, v29\n"
"v_cvt_pk_bf16_f32 v79, v30, v31\n"
"v_xor_b32 v76, s83, v76\n"
"v_xor_b32 v77, s83, v77\n"
"v_xor_b32 v78, s83, v78\n"
"v_xor_b32 v79, s83, v79\n"
"buffer_store_dwordx4 v[76:79], v126, s[36:39], s29 offen offset:1024 sc1\n"
"s_barrier\n"
"s_add_u32 m0, s80, 49152\n"
"s_add_u32 s73, s1, 36864\n"
"buffer_load_dwordx4 v89, s[40:43], s73 offen offset:0 lds\n"
"buffer_load_dwordx4 v90, s[40:43], s73 offen offset:1024 lds\n"
"buffer_load_dwordx4 v91, s[40:43], s73 offen offset:2048 lds\n"
"buffer_load_dwordx4 v92, s[40:43], s73 offen offset:3072 lds\n"
"v_and_b32 v64, 15, v127\n"
"v_lshrrev_b32 v65, 4, v127\n"
"v_xor_b32 v65, v65, v64\n"
"v_lshlrev_b32 v65, 4, v65\n"
"v_lshl_add_u32 v112, v64, 10, v65\n"
"v_xor_b32 v113, 64, v112\n"
"v_xor_b32 v114, 128, v112\n"
"v_xor_b32 v115, 192, v112\n"
"s_lshl_b32 s76, s69, 10\n"
"s_and_b32 s76, s76, 0x2000\n"
"s_xor_b32 s76, s76, s65\n"
"s_lshl_b32 s75, s69, 18\n"
"s_add_u32 s75, s75, s76\n"
"s_add_u32 s75, s75, 0x1800000\n"
"s_lshl_b32 s76, s71, 16\n"
"s_add_u32 s75, s75, s76\n"
"s_lshl_b32 s73, s85, 14\n"
"s_lshr_b32 s76, s84, 3\n"
"s_lshl_b32 s76, s76, 9\n"
"s_add_u32 s73, s73, s76\n"
"s_add_u32 s73, s73, s65\n"
"s_add_u32 s73, s73, 0xc0000\n"
"v_lshlrev_b32 v38, 6, v127\n"
"v_mov_b32 v39, s68\n"
"s_lshl_b32 s99, s70, 6\n"
"s_add_u32 s99, s99, s73\n"
"s_cmp_eq_u32 s71, 1\n"
"s_cselect_b32 s99, s99, 0x2800000\n"
"v_lshlrev_b32 v36, 14, v127\n"
"v_mov_b32 v37, s68\n"
"s_sub_u32 s96, s68, 1\n"
"v_mov_b32 v92, s96\n"
"v_mov_b32 v93, s96\n"
"v_mov_b32 v94, s96\n"
"v_mov_b32 v95, s96\n"
"v_and_b32 v24, 48, v126\n"
"s_lshl_b32 s96, s69, 14\n"
"s_lshl_b32 s97, s70, 8\n"
"s_add_u32 s96, s96, s97\n"
"s_add_u32 s96, s96, s81\n"
"s_add_u32 s96, s96, s65\n"
"s_add_u32 s96, s96, 0x100000\n"
"v_and_b32 v89, 0xf0, v126\n"
"v_bfe_u32 v19, v127, 4, 1\n"
"v_lshl_add_u32 v89, v19, 19, v89\n"
"v_add_u32 v89, s96, v89\n"
"s_lshr_b32 s96, s64, s70\n"
"s_and_b32 s96, s96, 1\n"
"s_lshl_b32 s97, s70, 1\n"
"s_add_u32 s96, s96, s97\n"
"s_lshl_b32 s96, s96, 7\n"
"s_add_u32 s96, s96, s3\n"
"s_lshl_b32 s96, s96, 6\n"
"s_add_u32 s96, s96, 0x80000\n"
"v_bfe_u32 v19, v127, 2, 1\n"
"v_lshl_add_u32 v19, v19, 17, v24\n"
"s_mov_b32 exec_lo, 0\n"
"v_add_u32 v89, s96, v19\n"
"s_mov_b32 exec_lo, -1\n"
"s_and_b32 s96, s70, 4\n"
"s_add_u32 s96, s96, s70\n"
"s_lshl_b32 s96, s96, 11\n"
"s_lshl_b32 s97, s69, 6\n"
"s_add_u32 s96, s96, s97\n"
"s_add_u32 s96, s96, 0x40000\n"
"v_lshrrev_b32 v19, 2, v127\n"
"v_lshl_add_u32 v90, v19, 15, v24\n"
"v_bfe_u32 v19, s64, v19, 1\n"
"v_lshl_add_u32 v90, v19, 13, v90\n"
"s_cmp_eq_u32 s71, 0\n"
"s_cselect_b32 s98, s96, 0x2800000\n"
"s_lshl_b32 s97, s25, 4\n"
"s_add_u32 s97, s97, 0x10000\n"
"s_cmp_eq_u32 s71, 3\n"
"s_cselect_b32 s97, s97, 0x2800000\n"
"s_waitcnt vmcnt(4)\n"
"s_barrier\n"
"s_mov_b64 exec, 0x1\n"
"buffer_store_dword v37, v36, s[36:39], s99 offen sc1\n"
"s_mov_b64 exec, -1\n"
"s_waitcnt vmcnt(0)\n"
"s_cmp_lg_u32 s71, 1\n"
"s_cbranch_scc1 R5_%=\n"
"s_mov_b64 exec, 0xff\n"
"buffer_load_dword v39, v38, s[36:39], s73 offen sc1\n"
"s_mov_b64 exec, -1\n"
"R5_%=:\n"
"s_mov_b32 exec_hi, 0xff\n"
"buffer_store_dwordx4 v[92:95], v89, s[36:39], 0 offen sc1\n"
"s_mov_b64 exec, -1\n"
"s_mov_b32 exec_hi, 0\n"
"buffer_store_dwordx4 v[92:95], v90, s[36:39], s98 offen sc1\n"
"s_mov_b64 exec, -1\n"
"s_mov_b64 exec, 1\n"
"buffer_store_dwordx4 v[92:95], off, s[36:39], s97 sc1\n"
"s_mov_b64 exec, -1\n"
"s_cmp_lg_u32 s71, 1\n"
"s_cbranch_scc1 R7_%=\n"
"s_waitcnt vmcnt(3)\n"
"v_cmp_ne_u32 vcc, s68, v39\n"
"s_cbranch_vccnz S9_%=\n"
"B10_%=:\n"
"R7_%=:\n"
"s_barrier\n"
"s_lshl_b32 s73, s69, 14\n"
"s_add_u32 s73, s73, s81\n"
"s_add_u32 s73, s73, s65\n"
"s_add_u32 s73, s73, 0x100000\n"
"s_lshl_b32 s74, s70, 8\n"
"s_add_u32 s74, s74, s73\n"
"buffer_load_dwordx4 v[48:51], v126, s[36:39], s75 offen offset:0 sc1\n"
"buffer_load_dwordx4 v[52:55], v126, s[36:39], s75 offen offset:1024 sc1\n"
"buffer_load_dwordx4 v[56:59], v126, s[36:39], s75 offen offset:2048 sc1\n"
"buffer_load_dwordx4 v[60:63], v126, s[36:39], s75 offen offset:3072 sc1\n"
"s_add_u32 s75, s75, 4096\n"
"buffer_load_dwordx4 v[64:67], v126, s[36:39], s75 offen offset:0 sc1\n"
"buffer_load_dwordx4 v[68:71], v126, s[36:39], s75 offen offset:1024 sc1\n"
"buffer_load_dwordx4 v[72:75], v126, s[36:39], s75 offen offset:2048 sc1\n"
"buffer_load_dwordx4 v[76:79], v126, s[36:39], s75 offen offset:3072 sc1\n"
"ds_read_b128 v[16:19], v112 offset:0\n"
"ds_read_b128 v[20:23], v112 offset:16384\n"
"ds_read_b128 v[24:27], v112 offset:32768\n"
"ds_read_b128 v[28:31], v112 offset:49152\n"
"ds_read_b128 v[32:35], v113 offset:0\n"
"ds_read_b128 v[36:39], v113 offset:16384\n"
"ds_read_b128 v[40:43], v113 offset:32768\n"
"s_waitcnt vmcnt(7) lgkmcnt(3)\n"
"v_mfma_f32_16x16x32_bf16 v[0:3], v[16:19], v[48:51], v[96:99]\n"
"ds_read_b128 v[44:47], v113 offset:49152\n"
"v_mfma_f32_16x16x32_bf16 v[4:7], v[20:23], v[48:51], v[100:103]\n"
"ds_read_b128 v[16:19], v114 offset:0\n"
"s_add_u32 s75, s75, 12288\n"
"buffer_load_dwordx4 v[96:99], v126, s[36:39], s75 offen offset:0 sc1\n"
"v_mfma_f32_16x16x32_bf16 v[8:11], v[24:27], v[48:51], v[104:107]\n"
"ds_read_b128 v[20:23], v114 offset:16384\n"
"buffer_load_dwordx4 v[100:103], v126, s[36:39], s75 offen offset:1024 sc1\n"
"v_mfma_f32_16x16x32_bf16 v[12:15], v[28:31], v[48:51], v[108:111]\n"
"ds_read_b128 v[24:27], v114 offset:32768\n"
"s_waitcnt vmcnt(8) lgkmcnt(3)\n"
"v_mfma_f32_16x16x32_bf16 v[0:3], v[32:35], v[52:55], v[0:3]\n"
"ds_read_b128 v[28:31], v114 offset:49152\n"
"v_mfma_f32_16x16x32_bf16 v[4:7], v[36:39], v[52:55], v[4:7]\n"
"ds_read_b128 v[32:35], v115 offset:0\n"
"buffer_load_dwordx4 v[104:107], v126, s[36:39], s75 offen offset:2048 sc1\n"
"v_mfma_f32_16x16x32_bf16 v[8:11], v[40:43], v[52:55], v[8:11]\n"
"ds_read_b128 v[36:39], v115 offset:16384\n"
"buffer_load_dwordx4 v[108:111], v126, s[36:39], s75 offen offset:3072 sc1\n"
"v_mfma_f32_16x16x32_bf16 v[12:15], v[44:47], v[52:55], v[12:15]\n"
"ds_read_b128 v[40:43], v115 offset:32768\n"
"s_waitcnt vmcnt(9) lgkmcnt(3)\n"
"v_mfma_f32_16x16x32_bf16 v[0:3], v[16:19], v[56:59], v[0:3]\n"
"ds_read_b128 v[44:47], v115 offset:49152\n"
"v_mfma_f32_16x16x32_bf16 v[4:7], v[20:23], v[56:59], v[4:7]\n"
"ds_read_b128 v[16:19], v112 offset:256\n"
"s_add_u32 s75, s75, 4096\n"
"buffer_load_dwordx4 v[48:51], v126, s[36:39], s75 offen offset:0 sc1\n"
"v_mfma_f32_16x16x32_bf16 v[8:11], v[24:27], v[56:59], v[8:11]\n"
"ds_read_b128 v[20:23], v112 offset:16640\n"
"buffer_load_dwordx4 v[52:55], v126, s[36:39], s75 offen offset:1024 sc1\n"
"v_mfma_f32_16x16x32_bf16 v[12:15], v[28:31], v[56:59], v[12:15]\n"
"ds_read_b128 v[24:27], v112 offset:33024\n"
"s_waitcnt vmcnt(10) lgkmcnt(3)\n"
"v_mfma_f32_16x16x32_bf16 v[0:3], v[32:35], v[60:63], v[0:3]\n"
"ds_read_b128 v[28:31], v112 offset:49408\n"
"v_mfma_f32_16x16x32_bf16 v[4:7], v[36:39], v[60:63], v[4:7]\n"
"ds_read_b128 v[32:35], v113 offset:256\n"
"buffer_load_dwordx4 v[56:59], v126, s[36:39], s75 offen offset:2048 sc1\n"
"v_mfma_f32_16x16x32_bf16 v[8:11], v[40:43], v[60:63], v[8:11]\n"
"ds_read_b128 v[36:39], v113 offset:16640\n"
"v_mfma_f32_16x16x32_bf16 v[12:15], v[44:47], v[60:63], v[12:15]\n"
"ds_read_b128 v[40:43], v113 offset:33024\n"
"s_waitcnt vmcnt(10) lgkmcnt(3)\n"
"v_mfma_f32_16x16x32_bf16 v[0:3], v[16:19], v[64:67], v[0:3]\n"
"ds_read_b128 v[44:47], v113 offset:49408\n"
"v_mfma_f32_16x16x32_bf16 v[4:7], v[20:23], v[64:67], v[4:7]\n"
"ds_read_b128 v[16:19], v114 offset:256\n"
"buffer_load_dwordx4 v[60:63], v126, s[36:39], s75 offen offset:3072 sc1\n"
"v_mfma_f32_16x16x32_bf16 v[8:11], v[24:27], v[64:67], v[8:11]\n"
"ds_read_b128 v[20:23], v114 offset:16640\n"
"v_mfma_f32_16x16x32_bf16 v[12:15], v[28:31], v[64:67], v[12:15]\n"
"ds_read_b128 v[24:27], v114 offset:33024\n"
"s_waitcnt vmcnt(10) lgkmcnt(3)\n"
"v_mfma_f32_16x16x32_bf16 v[0:3], v[32:35], v[68:71], v[0:3]\n"
"ds_read_b128 v[28:31], v114 offset:49408\n"
"v_mfma_f32_16x16x32_bf16 v[4:7], v[36:39], v[68:71], v[4:7]\n"
"ds_read_b128 v[32:35], v115 offset:256\n"
"s_add_u32 s75, s75, 12288\n"
"buffer_load_dwordx4 v[64:67], v126, s[36:39], s75 offen offset:0 sc1\n"
"v_mfma_f32_16x16x32_bf16 v[8:11], v[40:43], v[68:71], v[8:11]\n"
"ds_read_b128 v[36:39], v115 offset:16640\n"
"v_mfma_f32_16x16x32_bf16 v[12:15], v[44:47], v[68:71], v[12:15]\n"
"ds_read_b128 v[40:43], v115 offset:33024\n"
"s_waitcnt vmcnt(10) lgkmcnt(3)\n"
"v_mfma_f32_16x16x32_bf16 v[0:3], v[16:19], v[72:75], v[0:3]\n"
"ds_read_b128 v[44:47], v115 offset:49408\n"
"v_mfma_f32_16x16x32_bf16 v[4:7], v[20:23], v[72:75], v[4:7]\n"
"ds_read_b128 v[16:19], v112 offset:512\n"
"buffer_load_dwordx4 v[68:71], v126, s[36:39], s75 offen offset:1024 sc1\n"
"v_mfma_f32_16x16x32_bf16 v[8:11], v[24:27], v[72:75], v[8:11]\n"
"ds_read_b128 v[20:23], v112 offset:16896\n"
"v_mfma_f32_16x16x32_bf16 v[12:15], v[28:31], v[72:75], v[12:15]\n"
"ds_read_b128 v[24:27], v112 offset:33280\n"
"s_waitcnt vmcnt(10) lgkmcnt(3)\n"
"v_mfma_f32_16x16x32_bf16 v[0:3], v[32:35], v[76:79], v[0:3]\n"
"ds_read_b128 v[28:31], v112 offset:49664\n"
"v_mfma_f32_16x16x32_bf16 v[4:7], v[36:39], v[76:79], v[4:7]\n"
"ds_read_b128 v[32:35], v113 offset:512\n"
"buffer_load_dwordx4 v[72:75], v126, s[36:39], s75 offen offset:2048 sc1\n"
"v_mfma_f32_16x16x32_bf16 v[8:11], v[40:43], v[76:79], v[8:11]\n"
"ds_read_b128 v[36:39], v113 offset:16896\n"
"v_mfma_f32_16x16x32_bf16 v[12:15], v[44:47], v[76:79], v[12:15]\n"
"ds_read_b128 v[40:43], v113 offset:33280\n"
"s_waitcnt vmcnt(10) lgkmcnt(3)\n"
"v_mfma_f32_16x16x32_bf16 v[0:3], v[16:19], v[96:99], v[0:3]\n"
"ds_read_b128 v[44:47], v113 offset:49664\n"
"v_mfma_f32_16x16x32_bf16 v[4:7], v[20:23], v[96:99], v[4:7]\n"
"ds_read_b128 v[16:19], v114 offset:512\n"
"buffer_load_dwordx4 v[76:79], v126, s[36:39], s75 offen offset:3072 sc1\n"
"v_mfma_f32_16x16x32_bf16 v[8:11], v[24:27], v[96:99], v[8:11]\n"
"ds_read_b128 v[20:23], v114 offset:16896\n"
"v_mfma_f32_16x16x32_bf16 v[12:15], v[28:31], v[96:99], v[12:15]\n"
"ds_read_b128 v[24:27], v114 offset:33280\n"
"s_waitcnt vmcnt(10) lgkmcnt(3)\n"
"v_mfma_f32_16x16x32_bf16 v[0:3], v[32:35], v[100:103], v[0:3]\n"
"ds_read_b128 v[28:31], v114 offset:49664\n"
"v_mfma_f32_16x16x32_bf16 v[4:7], v[36:39], v[100:103], v[4:7]\n"
"ds_read_b128 v[32:35], v115 offset:512\n"
"s_add_u32 s75, s75, 4096\n"
"buffer_load_dwordx4 v[96:99], v126, s[36:39], s75 offen offset:0 sc1\n"
"v_mfma_f32_16x16x32_bf16 v[8:11], v[40:43], v[100:103], v[8:11]\n"
"ds_read_b128 v[36:39], v115 offset:16896\n"
"v_mfma_f32_16x16x32_bf16 v[12:15], v[44:47], v[100:103], v[12:15]\n"
"ds_read_b128 v[40:43], v115 offset:33280\n"
"s_waitcnt vmcnt(10) lgkmcnt(3)\n"
"v_mfma_f32_16x16x32_bf16 v[0:3], v[16:19], v[104:107], v[0:3]\n"
"ds_read_b128 v[44:47], v115 offset:49664\n"
"v_mfma_f32_16x16x32_bf16 v[4:7], v[20:23], v[104:107], v[4:7]\n"
"ds_read_b128 v[16:19], v112 offset:768\n"
"buffer_load_dwordx4 v[100:103], v126, s[36:39], s75 offen offset:1024 sc1\n"
"v_mfma_f32_16x16x32_bf16 v[8:11], v[24:27], v[104:107], v[8:11]\n"
"ds_read_b128 v[20:23], v112 offset:17152\n"
"v_mfma_f32_16x16x32_bf16 v[12:15], v[28:31], v[104:107], v[12:15]\n"
"ds_read_b128 v[24:27], v112 offset:33536\n"
"s_waitcnt vmcnt(10) lgkmcnt(3)\n"
"v_mfma_f32_16x16x32_bf16 v[0:3], v[32:35], v[108:111], v[0:3]\n"
"ds_read_b128 v[28:31], v112 offset:49920\n"
"v_mfma_f32_16x16x32_bf16 v[4:7], v[36:39], v[108:111], v[4:7]\n"
"ds_read_b128 v[32:35], v113 offset:768\n"
"buffer_load_dwordx4 v[104:107], v126, s[36:39], s75 offen offset:2048 sc1\n"
"v_mfma_f32_16x16x32_bf16 v[8:11], v[40:43], v[108:111], v[8:11]\n"
"ds_read_b128 v[36:39], v113 offset:17152\n"
"v_mfma_f32_16x16x32_bf16 v[12:15], v[44:47], v[108:111], v[12:15]\n"
"ds_read_b128 v[40:43], v113 offset:33536\n"
"s_waitcnt vmcnt(10) lgkmcnt(3)\n"
"v_mfma_f32_16x16x32_bf16 v[0:3], v[16:19], v[48:51], v[0:3]\n"
"ds_read_b128 v[44:47], v113 offset:49920\n"
"v_mfma_f32_16x16x32_bf16 v[4:7], v[20:23], v[48:51], v[4:7]\n"
"ds_read_b128 v[16:19], v114 offset:768\n"
"buffer_load_dwordx4 v[108:111], v126, s[36:39], s75 offen offset:3072 sc1\n"
"v_mfma_f32_16x16x32_bf16 v[8:11], v[24:27], v[48:51], v[8:11]\n"
"ds_read_b128 v[20:23], v114 offset:17152\n"
"v_mfma_f32_16x16x32_bf16 v[12:15], v[28:31], v[48:51], v[12:15]\n"
"ds_read_b128 v[24:27], v114 offset:33536\n"
"s_waitcnt vmcnt(10) lgkmcnt(3)\n"
"v_mfma_f32_16x16x32_bf16 v[0:3], v[32:35], v[52:55], v[0:3]\n"
"ds_read_b128 v[28:31], v114 offset:49920\n"
"v_mfma_f32_16x16x32_bf16 v[4:7], v[36:39], v[52:55], v[4:7]\n"
"ds_read_b128 v[32:35], v115 offset:768\n"
"s_add_u32 s75, s75, 12288\n"
"buffer_load_dwordx4 v[48:51], v126, s[36:39], s75 offen offset:0 sc1\n"
"v_mfma_f32_16x16x32_bf16 v[8:11], v[40:43], v[52:55], v[8:11]\n"
"ds_read_b128 v[36:39], v115 offset:17152\n"
"v_mfma_f32_16x16x32_bf16 v[12:15], v[44:47], v[52:55], v[12:15]\n"
"ds_read_b128 v[40:43], v115 offset:33536\n"
"s_waitcnt vmcnt(10) lgkmcnt(3)\n"
"v_mfma_f32_16x16x32_bf16 v[0:3], v[16:19], v[56:59], v[0:3]\n"
"ds_read_b128 v[44:47], v115 offset:49920\n"
"v_mfma_f32_16x16x32_bf16 v[4:7], v[20:23], v[56:59], v[4:7]\n"
"v_add_u32 v112, 0x10000, v112\n"
"ds_read_b128 v[16:19], v112 offset:0\n"
"buffer_load_dwordx4 v[52:55], v126, s[36:39], s75 offen offset:1024 sc1\n"
"v_mfma_f32_16x16x32_bf16 v[8:11], v[24:27], v[56:59], v[8:11]\n"
"ds_read_b128 v[20:23], v112 offset:16384\n"
"v_mfma_f32_16x16x32_bf16 v[12:15], v[28:31], v[56:59], v[12:15]\n"
"ds_read_b128 v[24:27], v112 offset:32768\n"
"s_waitcnt vmcnt(10) lgkmcnt(3)\n"
"v_mfma_f32_16x16x32_bf16 v[0:3], v[32:35], v[60:63], v[0:3]\n"
"ds_read_b128 v[28:31], v112 offset:49152\n"
"v_mfma_f32_16x16x32_bf16 v[4:7], v[36:39], v[60:63], v[4:7]\n"
"v_add_u32 v113, 0x10000, v113\n"
"ds_read_b128 v[32:35], v113 offset:0\n"
"buffer_load_dwordx4 v[56:59], v126, s[36:39], s75 offen offset:2048 sc1\n"
"v_mfma_f32_16x16x32_bf16 v[8:11], v[40:43], v[60:63], v[8:11]\n"
"ds_read_b128 v[36:39], v113 offset:16384\n"
"v_mfma_f32_16x16x32_bf16 v[12:15], v[44:47], v[60:63], v[12:15]\n"
"ds_read_b128 v[40:43], v113 offset:32768\n"
"s_waitcnt vmcnt(10) lgkmcnt(3)\n"
"v_mfma_f32_16x16x32_bf16 v[0:3], v[16:19], v[64:67], v[0:3]\n"
"ds_read_b128 v[44:47], v113 offset:49152\n"
"v_mfma_f32_16x16x32_bf16 v[4:7], v[20:23], v[64:67], v[4:7]\n"
"v_add_u32 v114, 0x10000, v114\n"
"ds_read_b128 v[16:19], v114 offset:0\n"
"buffer_load_dwordx4 v[60:63], v126, s[36:39], s75 offen offset:3072 sc1\n"
"v_mfma_f32_16x16x32_bf16 v[8:11], v[24:27], v[64:67], v[8:11]\n"
"ds_read_b128 v[20:23], v114 offset:16384\n"
"v_mfma_f32_16x16x32_bf16 v[12:15], v[28:31], v[64:67], v[12:15]\n"
"ds_read_b128 v[24:27], v114 offset:32768\n"
"s_waitcnt vmcnt(10) lgkmcnt(3)\n"
"v_mfma_f32_16x16x32_bf16 v[0:3], v[32:35], v[68:71], v[0:3]\n"
"ds_read_b128 v[28:31], v114 offset:49152\n"
"v_mfma_f32_16x16x32_bf16 v[4:7], v[36:39], v[68:71], v[4:7]\n"
"v_add_u32 v115, 0x10000, v115\n"
"ds_read_b128 v[32:35], v115 offset:0\n"
"s_add_u32 s75, s75, 4096\n"
"buffer_load_dwordx4 v[64:67], v126, s[36:39], s75 offen offset:0 sc1\n"
"v_mfma_f32_16x16x32_bf16 v[8:11], v[40:43], v[68:71], v[8:11]\n"
"ds_read_b128 v[36:39], v115 offset:16384\n"
"v_mfma_f32_16x16x32_bf16 v[12:15], v[44:47], v[68:71], v[12:15]\n"
"ds_read_b128 v[40:43], v115 offset:32768\n"
"s_waitcnt vmcnt(10) lgkmcnt(3)\n"
"v_mfma_f32_16x16x32_bf16 v[0:3], v[16:19], v[72:75], v[0:3]\n"
"ds_read_b128 v[44:47], v115 offset:49152\n"
"v_mfma_f32_16x16x32_bf16 v[4:7], v[20:23], v[72:75], v[4:7]\n"
"ds_read_b128 v[16:19], v112 offset:256\n"
"buffer_load_dwordx4 v[68:71], v126, s[36:39], s75 offen offset:1024 sc1\n"
"v_mfma_f32_16x16x32_bf16 v[8:11], v[24:27], v[72:75], v[8:11]\n"
"ds_read_b128 v[20:23], v112 offset:16640\n"
"v_mfma_f32_16x16x32_bf16 v[12:15], v[28:31], v[72:75], v[12:15]\n"
"ds_read_b128 v[24:27], v112 offset:33024\n"
"s_waitcnt vmcnt(10) lgkmcnt(3)\n"
"v_mfma_f32_16x16x32_bf16 v[0:3], v[32:35], v[76:79], v[0:3]\n"
"ds_read_b128 v[28:31], v112 offset:49408\n"
"v_mfma_f32_16x16x32_bf16 v[4:7], v[36:39], v[76:79], v[4:7]\n"
"ds_read_b128 v[32:35], v113 offset:256\n"
"buffer_load_dwordx4 v[72:75], v126, s[36:39], s75 offen offset:2048 sc1\n"
"v_mfma_f32_16x16x32_bf16 v[8:11], v[40:43], v[76:79], v[8:11]\n"
"ds_read_b128 v[36:39], v113 offset:16640\n"
"v_mfma_f32_16x16x32_bf16 v[12:15], v[44:47], v[76:79], v[12:15]\n"
"ds_read_b128 v[40:43], v113 offset:33024\n"
"s_waitcnt vmcnt(10) lgkmcnt(3)\n"
"v_mfma_f32_16x16x32_bf16 v[0:3], v[16:19], v[96:99], v[0:3]\n"
"ds_read_b128 v[44:47], v113 offset:49408\n"
"v_mfma_f32_16x16x32_bf16 v[4:7], v[20:23], v[96:99], v[4:7]\n"
"ds_read_b128 v[16:19], v114 offset:256\n"
"buffer_load_dwordx4 v[76:79], v126, s[36:39], s75 offen offset:3072 sc1\n"
"v_mfma_f32_16x16x32_bf16 v[8:11], v[24:27], v[96:99], v[8:11]\n"
"ds_read_b128 v[20:23], v114 offset:16640\n"
"v_mfma_f32_16x16x32_bf16 v[12:15], v[28:31], v[96:99], v[12:15]\n"
"ds_read_b128 v[24:27], v114 offset:33024\n"
"s_waitcnt vmcnt(10) lgkmcnt(3)\n"
"v_mfma_f32_16x16x32_bf16 v[0:3], v[32:35], v[100:103], v[0:3]\n"
"ds_read_b128 v[28:31], v114 offset:49408\n"
"v_mfma_f32_16x16x32_bf16 v[4:7], v[36:39], v[100:103], v[4:7]\n"
"ds_read_b128 v[32:35], v115 offset:256\n"
"s_lshl_b32 s96, s25, 14\n"
"s_add_u32 s96, s96, s81\n"
"s_add_u32 s96, s96, 0x1400000\n"
"s_add_u32 m0, s81, 131072\n"
"s_nop 0\n"
"buffer_load_dwordx4 v126, s[36:39], s96 offen offset:0 sc1 lds\n"
"v_mfma_f32_16x16x32_bf16 v[8:11], v[40:43], v[100:103], v[8:11]\n"
"ds_read_b128 v[36:39], v115 offset:16640\n"
"v_mfma_f32_16x16x32_bf16 v[12:15], v[44:47], v[100:103], v[12:15]\n"
"ds_read_b128 v[40:43], v115 offset:33024\n"
"s_waitcnt vmcnt(10) lgkmcnt(3)\n"
"v_mfma_f32_16x16x32_bf16 v[0:3], v[16:19], v[104:107], v[0:3]\n"
"ds_read_b128 v[44:47], v115 offset:49408\n"
"v_mfma_f32_16x16x32_bf16 v[4:7], v[20:23], v[104:107], v[4:7]\n"
"ds_read_b128 v[16:19], v112 offset:512\n"
"buffer_load_dwordx4 v126, s[36:39], s96 offen offset:1024 sc1 lds\n"
"v_mfma_f32_16x16x32_bf16 v[8:11], v[24:27], v[104:107], v[8:11]\n"
"ds_read_b128 v[20:23], v112 offset:16896\n"
"v_mfma_f32_16x16x32_bf16 v[12:15], v[28:31], v[104:107], v[12:15]\n"
"ds_read_b128 v[24:27], v112 offset:33280\n"
"s_waitcnt vmcnt(10) lgkmcnt(3)\n"
"v_mfma_f32_16x16x32_bf16 v[0:3], v[32:35], v[108:111], v[0:3]\n"
"ds_read_b128 v[28:31], v112 offset:49664\n"
"v_mfma_f32_16x16x32_bf16 v[4:7], v[36:39], v[108:111], v[4:7]\n"
"ds_read_b128 v[32:35], v113 offset:512\n"
"s_add_u32 s96, s96, 8192\n"
"s_add_u32 m0, s81, 139264\n"
"s_nop 0\n"
"buffer_load_dwordx4 v126, s[36:39], s96 offen offset:0 sc1 lds\n"
"v_mfma_f32_16x16x32_bf16 v[8:11], v[40:43], v[108:111], v[8:11]\n"
"ds_read_b128 v[36:39], v113 offset:16896\n"
"v_mfma_f32_16x16x32_bf16 v[12:15], v[44:47], v[108:111], v[12:15]\n"
"ds_read_b128 v[40:43], v113 offset:33280\n"
"s_waitcnt vmcnt(10) lgkmcnt(3)\n"
"v_mfma_f32_16x16x32_bf16 v[0:3], v[16:19], v[48:51], v[0:3]\n"
"ds_read_b128 v[44:47], v113 offset:49664\n"
"v_mfma_f32_16x16x32_bf16 v[4:7], v[20:23], v[48:51], v[4:7]\n"
"ds_read_b128 v[16:19], v114 offset:512\n"
"buffer_load_dwordx4 v126, s[36:39], s96 offen offset:1024 sc1 lds\n"
"v_mfma_f32_16x16x32_bf16 v[8:11], v[24:27], v[48:51], v[8:11]\n"
"ds_read_b128 v[20:23], v114 offset:16896\n"
"v_mfma_f32_16x16x32_bf16 v[12:15], v[28:31], v[48:51], v[12:15]\n"
"ds_read_b128 v[24:27], v114 offset:33280\n"
"s_waitcnt vmcnt(10) lgkmcnt(3)\n"
"v_mfma_f32_16x16x32_bf16 v[0:3], v[32:35], v[52:55], v[0:3]\n"
"ds_read_b128 v[28:31], v114 offset:49664\n"
"v_mfma_f32_16x16x32_bf16 v[4:7], v[36:39], v[52:55], v[4:7]\n"
"ds_read_b128 v[32:35], v115 offset:512\n"
"v_mfma_f32_16x16x32_bf16 v[8:11], v[40:43], v[52:55], v[8:11]\n"
"ds_read_b128 v[36:39], v115 offset:16896\n"
"v_mfma_f32_16x16x32_bf16 v[12:15], v[44:47], v[52:55], v[12:15]\n"
"ds_read_b128 v[40:43], v115 offset:33280\n"
"s_waitcnt vmcnt(9) lgkmcnt(3)\n"
"v_mfma_f32_16x16x32_bf16 v[0:3], v[16:19], v[56:59], v[0:3]\n"
"ds_read_b128 v[44:47], v115 offset:49664\n"
"v_mfma_f32_16x16x32_bf16 v[4:7], v[20:23], v[56:59], v[4:7]\n"
"ds_read_b128 v[16:19], v112 offset:768\n"
"v_mfma_f32_16x16x32_bf16 v[8:11], v[24:27], v[56:59], v[8:11]\n"
"ds_read_b128 v[20:23], v112 offset:17152\n"
"v_mfma_f32_16x16x32_bf16 v[12:15], v[28:31], v[56:59], v[12:15]\n"
"ds_read_b128 v[24:27], v112 offset:33536\n"
"s_waitcnt vmcnt(8) lgkmcnt(3)\n"
"v_mfma_f32_16x16x32_bf16 v[0:3], v[32:35], v[60:63], v[0:3]\n"
"ds_read_b128 v[28:31], v112 offset:49920\n"
"v_mfma_f32_16x16x32_bf16 v[4:7], v[36:39], v[60:63], v[4:7]\n"
"ds_read_b128 v[32:35], v113 offset:768\n"
"v_mfma_f32_16x16x32_bf16 v[8:11], v[40:43], v[60:63], v[8:11]\n"
"ds_read_b128 v[36:39], v113 offset:17152\n"
"v_mfma_f32_16x16x32_bf16 v[12:15], v[44:47], v[60:63], v[12:15]\n"
"ds_read_b128 v[40:43], v113 offset:33536\n"
"s_waitcnt vmcnt(7) lgkmcnt(3)\n"
"v_mfma_f32_16x16x32_bf16 v[0:3], v[16:19], v[64:67], v[0:3]\n"
"ds_read_b128 v[44:47], v113 offset:49920\n"
"v_mfma_f32_16x16x32_bf16 v[4:7], v[20:23], v[64:67], v[4:7]\n"
"ds_read_b128 v[16:19], v114 offset:768\n"
"v_mfma_f32_16x16x32_bf16 v[8:11], v[24:27], v[64:67], v[8:11]\n"
"ds_read_b128 v[20:23], v114 offset:17152\n"
"v_mfma_f32_16x16x32_bf16 v[12:15], v[28:31], v[64:67], v[12:15]\n"
"ds_read_b128 v[24:27], v114 offset:33536\n"
"s_waitcnt vmcnt(6) lgkmcnt(3)\n"
"v_mfma_f32_16x16x32_bf16 v[0:3], v[32:35], v[68:71], v[0:3]\n"
"ds_read_b128 v[28:31], v114 offset:49920\n"
"v_mfma_f32_16x16x32_bf16 v[4:7], v[36:39], v[68:71], v[4:7]\n"
"ds_read_b128 v[32:35], v115 offset:768\n"
"v_mfma_f32_16x16x32_bf16 v[8:11], v[40:43], v[68:71], v[8:11]\n"
"ds_read_b128 v[36:39], v115 offset:17152\n"
"v_mfma_f32_16x16x32_bf16 v[12:15], v[44:47], v[68:71], v[12:15]\n"
"ds_read_b128 v[40:43], v115 offset:33536\n"
"s_waitcnt vmcnt(5) lgkmcnt(3)\n"
"v_mfma_f32_16x16x32_bf16 v[0:3], v[16:19], v[72:75], v[0:3]\n"
"ds_read_b128 v[44:47], v115 offset:49920\n"
"v_mfma_f32_16x16x32_bf16 v[4:7], v[20:23], v[72:75], v[4:7]\n"
"v_mfma_f32_16x16x32_bf16 v[8:11], v[24:27], v[72:75], v[8:11]\n"
"v_mfma_f32_16x16x32_bf16 v[12:15], v[28:31], v[72:75], v[12:15]\n"
"s_waitcnt vmcnt(4) lgkmcnt(0)\n"
"v_mfma_f32_16x16x32_bf16 v[0:3], v[32:35], v[76:79], v[0:3]\n"
"v_mfma_f32_16x16x32_bf16 v[4:7], v[36:39], v[76:79], v[4:7]\n"
"v_mfma_f32_16x16x32_bf16 v[8:11], v[40:43], v[76:79], v[8:11]\n"
"v_mfma_f32_16x16x32_bf16 v[12:15], v[44:47], v[76:79], v[12:15]\n"
"v_lshlrev_b32 v48, 16, v80\n"
"v_and_b32 v49, 0xffff0000, v80\n"
"v_lshlrev_b32 v50, 16, v81\n"
"v_and_b32 v51, 0xffff0000, v81\n"
"v_lshlrev_b32 v52, 16, v82\n"
"v_and_b32 v53, 0xffff0000, v82\n"
"v_lshlrev_b32 v54, 16, v83\n"
"v_and_b32 v55, 0xffff0000, v83\n"
"v_lshlrev_b32 v56, 16, v84\n"
"v_and_b32 v57, 0xffff0000, v84\n"
"v_lshlrev_b32 v58, 16, v85\n"
"v_and_b32 v59, 0xffff0000, v85\n"
"v_lshlrev_b32 v60, 16, v86\n"
"v_and_b32 v61, 0xffff0000, v86\n"
"v_lshlrev_b32 v62, 16, v87\n"
"v_and_b32 v63, 0xffff0000, v87\n"
"v_pk_add_f32 v[20:21], v[0:1], v[2:3]\n"
"v_pk_mul_f32 v[22:23], v[0:1], v[0:1]\n"
"v_pk_mul_f32 v[24:25], v[0:1], v[48:49]\n"
"v_pk_fma_f32 v[22:23], v[2:3], v[2:3], v[22:23]\n"
"v_pk_fma_f32 v[24:25], v[2:3], v[50:51], v[24:25]\n"
"v_pk_add_f32 v[20:21], v[20:21], v[4:5]\n"
"v_pk_fma_f32 v[22:23], v[4:5], v[4:5], v[22:23]\n"
"v_pk_fma_f32 v[24:25], v[4:5], v[52:53], v[24:25]\n"
"v_pk_add_f32 v[20:21], v[20:21], v[6:7]\n"
"v_pk_fma_f32 v[22:23], v[6:7], v[6:7], v[22:23]\n"
"v_pk_fma_f32 v[24:25], v[6:7], v[54:55], v[24:25]\n"
"v_pk_add_f32 v[20:21], v[20:21], v[8:9]\n"
"v_pk_fma_f32 v[22:23], v[8:9], v[8:9], v[22:23]\n"
"v_pk_fma_f32 v[24:25], v[8:9], v[56:57], v[24:25]\n"
"v_pk_add_f32 v[20:21], v[20:21], v[10:11]\n"
"v_pk_fma_f32 v[22:23], v[10:11], v[10:11], v[22:23]\n"
"v_pk_fma_f32 v[24:25], v[10:11], v[58:59], v[24:25]\n"
"v_pk_add_f32 v[20:21], v[20:21], v[12:13]\n"
"v_pk_fma_f32 v[22:23], v[12:13], v[12:13], v[22:23]\n"
"v_pk_fma_f32 v[24:25], v[12:13], v[60:61], v[24:25]\n"
"v_pk_add_f32 v[20:21], v[20:21], v[14:15]\n"
"v_pk_fma_f32 v[22:23], v[14:15], v[14:15], v[22:23]\n"
"v_pk_fma_f32 v[24:25], v[14:15], v[62:63], v[24:25]\n"
"v_add_f32 v16, v20, v21\n"
"v_add_f32 v17, v22, v23\n"
"v_add_f32 v18, v24, v25\n"
"v_mov_b32 v20, v16\n"
"v_mov_b32 v21, v17\n"
"v_mov_b32 v22, v18\n"
"v_permlane32_swap_b32 v16, v20\n"
"v_permlane32_swap_b32 v17, v21\n"
"v_permlane32_swap_b32 v18, v22\n"
"v_add_f32 v16, v16, v20\n"
"v_add_f32 v17, v17, v21\n"
"v_add_f32 v18, v18, v22\n"
"v_mov_b32 v20, v16\n"
"v_mov_b32 v21, v17\n"
"v_mov_b32 v22, v18\n"
"v_permlane16_swap_b32 v16, v20\n"
"v_permlane16_swap_b32 v17, v21\n"
"v_permlane16_swap_b32 v18, v22\n"
"v_add_f32 v16, v16, v20\n"
"v_add_f32 v17, v17, v21\n"
"v_add_f32 v18, v18, v22\n"
"v_mov_b32 v19, s68\n"
"s_mov_b64 exec, 0xffff\n"
"buffer_store_dwordx4 v[16:19], v126, s[36:39], s74 offen sc1\n"
"s_mov_b64 exec, -1\n"
"s_barrier\n"
"s_add_u32 m0, s80, 65536\n"
"s_xor_b32 s99, s2, 0\n"
"buffer_load_dwordx4 v120, s[32:35], s99 offen offset:0 lds\n"
"buffer_load_dwordx4 v121, s[32:35], s99 offen offset:1024 lds\n"
"buffer_load_dwordx4 v122, s[32:35], s99 offen offset:2048 lds\n"
"buffer_load_dwordx4 v123, s[32:35], s99 offen offset:3072 lds\n"
"s_lshl_b32 s96, s3, 4\n"
"v_and_b32 v28, 15, v127\n"
"v_add_u32 v28, s96, v28\n"
"v_xor_b32 v28, 0x7ff, v28\n"
"s_add_u32 s96, s81, 155648\n"
"v_and_b32 v29, 48, v127\n"
"v_add_u32 v29, s96, v29\n"
"v_add_u32 v30, 128, v29\n"
"v_lshl_add_u32 v89, v127, 2, s96\n"
"v_and_b32 v95, 32, v127\n"
"v_mul_u32_u24 v95, 112, v95\n"
"v_add_u32 v95, v95, v126\n"
"s_lshr_b32 s96, s64, s70\n"
"s_and_b32 s96, s96, 1\n"
"s_lshl_b32 s97, s70, 1\n"
"s_add_u32 s96, s96, s97\n"
"s_lshl_b32 s96, s96, 7\n"
"s_add_u32 s96, s96, s3\n"
"s_lshl_b32 s96, s96, 6\n"
"s_add_u32 s96, s96, 0x80000\n"
"s_lshl_b32 s97, s3, 14\n"
"s_lshl_b32 s98, s70, 9\n"
"s_add_u32 s97, s97, s98\n"
"s_add_u32 s97, s97, 0x400000\n"
"s_mov_b32 s98, 0xfffff800\n"
"s_add_u32 s75, s81, 155648\n"
"v_and_b32 v24, 15, v127\n"
"v_lshrrev_b32 v25, 4, v127\n"
"v_and_b32 v26, 7, v127\n"
"v_xor_b32 v25, v25, v26\n"
"v_lshlrev_b32 v25, 4, v25\n"
"v_lshl_add_u32 v24, v24, 7, s75\n"
"v_add_u32 v90, v24, v25\n"
"v_xor_b32 v25, 64, v25\n"
"v_add_u32 v91, v24, v25\n"
"v_bfe_u32 v24, v127, 2, 2\n"
"v_lshrrev_b32 v25, 5, v127\n"
"v_and_b32 v26, 3, v127\n"
"v_lshl_add_u32 v25, v25, 2, v26\n"
"v_xor_b32 v25, v25, v24\n"
"v_lshlrev_b32 v25, 4, v25\n"
"v_bfe_u32 v26, v127, 4, 1\n"
"v_lshl_add_u32 v25, v26, 3, v25\n"
"v_lshl_add_u32 v24, v24, 7, s75\n"
"v_add_u32 v92, v24, v25\n"
"v_xor_b32 v25, 64, v25\n"
"v_add_u32 v93, v24, v25\n"
"s_add_u32 s79, s79, 0x400000\n"
"s_and_b32 s24, s2, 0xfff\n"
"s_lshl_b32 s24, s24, 6\n"
"s_add_u32 s24, s24, s79\n"
"s_lshl_b32 s75, s69, 13\n"
"s_add_u32 s75, s75, s79\n"
"v_mov_b32 v31, 0x3727c5ac\n"
"s_waitcnt vmcnt(0)\n"
"s_mov_b32 s77, 200000\n"
"P11_%=:\n"
"buffer_load_dwordx4 v[16:19], v126, s[36:39], s73 offen offset:0 sc1\n"
"buffer_load_dwordx4 v[20:23], v126, s[36:39], s73 offen offset:1024 sc1\n"
"s_sub_u32 s77, s77, 1\n"
"s_cselect_b64 s[26:27], 0, -1\n"
"s_waitcnt vmcnt(0)\n"
"v_xor_b32 v24, s68, v19\n"
"v_bitop3_b32 v24, v24, v23, s68 bitop3:0xf6\n"
"v_cmp_ne_u32 vcc, 0, v24\n"
"s_nop 0\n"
"s_and_b64 vcc, vcc, s[26:27]\n"
"s_cbranch_vccnz P11_%=\n"
"v_add_f32 v16, v16, v20\n"
"v_add_f32 v17, v17, v21\n"
"v_add_f32 v18, v18, v22\n"
"v_mov_b32 v20, v16\n"
"v_mov_b32 v21, v17\n"
"v_mov_b32 v22, v18\n"
"v_permlane32_swap_b32 v16, v20\n"
"v_permlane32_swap_b32 v17, v21\n"
"v_permlane32_swap_b32 v18, v22\n"
"v_add_f32 v16, v16, v20\n"
"v_add_f32 v17, v17, v21\n"
"v_add_f32 v18, v18, v22\n"
"v_mov_b32 v20, v16\n"
"v_mov_b32 v21, v17\n"
"v_mov_b32 v22, v18\n"
"v_permlane16_swap_b32 v16, v20\n"
"v_permlane16_swap_b32 v17, v21\n"
"v_permlane16_swap_b32 v18, v22\n"
"v_add_f32 v16, v16, v20\n"
"v_add_f32 v17, v17, v21\n"
"v_add_f32 v18, v18, v22\n"
"v_mul_f32 v16, 0x3b000000, v16\n"
"v_mul_f32 v17, 0x3b000000, v17\n"
"v_fma_f32 v19, -v16, s82, v18\n"
"v_fma_f32 v17, -v16, v16, v17\n"
"v_max_f32 v17, 0, v17\n"
"v_add_f32 v18, 0x3727c5ac, v17\n"
"v_rsq_f32 v18, v18\n"
"s_nop 0\n"
"v_mul_f32 v19, v19, v18\n"
"v_add_f32 v20, 1.0, v18\n"
"v_mul_f32 v22, v20, v20\n"
"v_fma_f32 v22, v22, v17, v31\n"
"v_rsq_f32 v22, v22\n"
"s_nop 0\n"
"v_mul_f32 v22, v22, v20\n"
"v_ashrrev_i32 v21, 31, v19\n"
"v_or_b32 v21, 0x80000000, v21\n"
"v_xor_b32 v21, v19, v21\n"
"v_and_or_b32 v88, v21, s98, v28\n"
"v_sub_f32 v32, v0, v16\n"
"v_sub_f32 v33, v1, v16\n"
"v_max_u32_dpp v24, v88, v88 row_ror:8 row_mask:0xf bank_mask:0xf\n"
"v_sub_f32 v34, v2, v16\n"
"v_sub_f32 v35, v3, v16\n"
"v_max_u32_dpp v24, v24, v24 row_ror:4 row_mask:0xf bank_mask:0xf\n"
"v_sub_f32 v36, v4, v16\n"
"v_sub_f32 v37, v5, v16\n"
"v_max_u32_dpp v24, v24, v24 row_ror:2 row_mask:0xf bank_mask:0xf\n"
"v_sub_f32 v38, v6, v16\n"
"v_sub_f32 v39, v7, v16\n"
"v_max_u32_dpp v24, v24, v24 row_ror:1 row_mask:0xf bank_mask:0xf\n"
"v_cmp_ne_u32 vcc, v24, v88\n"
"v_mov_b32 v25, s68\n"
"v_mov_b32 v27, s68\n"
"v_cndmask_b32 v26, 0, v88, vcc\n"
"v_cndmask_b32 v29, v29, v30, vcc\n"
"v_sub_f32 v40, v8, v16\n"
"v_sub_f32 v41, v9, v16\n"
"v_max_u32_dpp v26, v26, v26 row_ror:8 row_mask:0xf bank_mask:0xf\n"
"v_sub_f32 v42, v10, v16\n"
"v_sub_f32 v43, v11, v16\n"
"v_max_u32_dpp v26, v26, v26 row_ror:4 row_mask:0xf bank_mask:0xf\n"
"v_sub_f32 v44, v12, v16\n"
"v_sub_f32 v45, v13, v16\n"
"v_max_u32_dpp v26, v26, v26 row_ror:2 row_mask:0xf bank_mask:0xf\n"
"v_sub_f32 v46, v14, v16\n"
"v_sub_f32 v47, v15, v16\n"
"v_max_u32_dpp v26, v26, v26 row_ror:1 row_mask:0xf bank_mask:0xf\n"
"s_mov_b64 exec, 1\n"
"buffer_store_dwordx4 v[24:27], off, s[36:39], s96 sc1\n"
"s_mov_b64 exec, -1\n"
"v_cmp_ge_u32 s[94:95], v88, v26\n"
"v_mul_f32 v48, v32, v22\n"
"v_mul_f32 v49, v33, v22\n"
"v_mul_f32 v50, v34, v22\n"
"v_mul_f32 v51, v35, v22\n"
"v_mul_f32 v52, v36, v22\n"
"v_mul_f32 v53, v37, v22\n"
"v_mul_f32 v54, v38, v22\n"
"v_mul_f32 v55, v39, v22\n"
"v_mul_f32 v56, v40, v22\n"
"v_mul_f32 v57, v41, v22\n"
"v_mul_f32 v58, v42, v22\n"
"v_mul_f32 v59, v43, v22\n"
"v_mul_f32 v60, v44, v22\n"
"v_mul_f32 v61, v45, v22\n"
"v_mul_f32 v62, v46, v22\n"
"v_mul_f32 v63, v47, v22\n"
"v_cvt_pk_bf16_f32 v64, v48, v49\n"
"v_cvt_pk_bf16_f32 v65, v50, v51\n"
"v_cvt_pk_bf16_f32 v66, v52, v53\n"
"v_cvt_pk_bf16_f32 v67, v54, v55\n"
"v_cvt_pk_bf16_f32 v68, v56, v57\n"
"v_cvt_pk_bf16_f32 v69, v58, v59\n"
"v_cvt_pk_bf16_f32 v70, v60, v61\n"
"v_cvt_pk_bf16_f32 v71, v62, v63\n"
"ds_write_b128 v90, v[64:67]\n"
"ds_write_b128 v91, v[68:71]\n"
"ds_read_b64_tr_b16 v[72:73], v92 offset:0\n"
"ds_read_b64_tr_b16 v[74:75], v93 offset:512\n"
"ds_read_b64_tr_b16 v[76:77], v92 offset:1024\n"
"ds_read_b64_tr_b16 v[78:79], v93 offset:1536\n"
"v_mul_f32 v32, v32, v18\n"
"v_mul_f32 v33, v33, v18\n"
"v_mul_f32 v34, v34, v18\n"
"v_mul_f32 v35, v35, v18\n"
"v_mul_f32 v36, v36, v18\n"
"v_mul_f32 v37, v37, v18\n"
"v_mul_f32 v38, v38, v18\n"
"v_mul_f32 v39, v39, v18\n"
"v_mul_f32 v40, v40, v18\n"
"v_mul_f32 v41, v41, v18\n"
"v_mul_f32 v42, v42, v18\n"
"v_mul_f32 v43, v43, v18\n"
"v_mul_f32 v44, v44, v18\n"
"v_mul_f32 v45, v45, v18\n"
"v_mul_f32 v46, v46, v18\n"
"v_mul_f32 v47, v47, v18\n"
"s_waitcnt lgkmcnt(2)\n"
"buffer_store_dwordx4 v[72:75], v126, s[36:39], s75 offen sc1\n"
"s_waitcnt lgkmcnt(0)\n"
"buffer_store_dwordx4 v[76:79], v126, s[36:39], s75 offen offset:1024 sc1\n"
"v_cvt_pk_bf16_f32 v48, v32, v33\n"
"v_cvt_pk_bf16_f32 v49, v34, v35\n"
"v_cvt_pk_bf16_f32 v50, v36, v37\n"
"v_cvt_pk_bf16_f32 v51, v38, v39\n"
"v_cvt_pk_bf16_f32 v52, v40, v41\n"
"v_cvt_pk_bf16_f32 v53, v42, v43\n"
"v_cvt_pk_bf16_f32 v54, v44, v45\n"
"v_cvt_pk_bf16_f32 v55, v46, v47\n"
"v_add_f32 v96, v0, v32\n"
"v_add_f32 v97, v1, v33\n"
"v_add_f32 v98, v2, v34\n"
"v_add_f32 v99, v3, v35\n"
"v_add_f32 v100, v4, v36\n"
"v_add_f32 v101, v5, v37\n"
"v_add_f32 v102, v6, v38\n"
"v_add_f32 v103, v7, v39\n"
"v_add_f32 v104, v8, v40\n"
"v_add_f32 v105, v9, v41\n"
"v_add_f32 v106, v10, v42\n"
"v_add_f32 v107, v11, v43\n"
"v_add_f32 v108, v12, v44\n"
"v_add_f32 v109, v13, v45\n"
"v_add_f32 v110, v14, v46\n"
"v_add_f32 v111, v15, v47\n"
"v_cvt_pk_bf16_f32 v56, v96, v97\n"
"v_cvt_pk_bf16_f32 v57, v98, v99\n"
"v_cvt_pk_bf16_f32 v58, v100, v101\n"
"v_cvt_pk_bf16_f32 v59, v102, v103\n"
"v_cvt_pk_bf16_f32 v60, v104, v105\n"
"v_cvt_pk_bf16_f32 v61, v106, v107\n"
"v_cvt_pk_bf16_f32 v62, v108, v109\n"
"v_cvt_pk_bf16_f32 v63, v110, v111\n"
"s_mov_b64 exec, s[94:95]\n"
"ds_write_b128 v29, v[48:51] offset:0\n"
"ds_write_b128 v29, v[52:55] offset:64\n"
"ds_write_b128 v29, v[56:59] offset:256\n"
"ds_write_b128 v29, v[60:63] offset:320\n"
"ds_write_b128 v29, v[64:67] offset:512\n"
"ds_write_b128 v29, v[68:71] offset:576\n"
"s_mov_b64 exec, -1\n"
"ds_read2st64_b32 v[24:25], v89 offset0:0 offset1:1\n"
"ds_read_b32 v26, v89 offset:512\n"
"s_waitcnt lgkmcnt(0)\n"
"buffer_store_dwordx4 v[24:27], v95, s[36:39], s97 offen sc1\n"
"s_add_u32 s97, s97, 8192\n"
"buffer_store_dwordx4 v[24:27], v95, s[36:39], s97 offen sc1\n"
"s_and_b32 s76, s70, 4\n"
"s_add_u32 s76, s76, s70\n"
"s_lshl_b32 s76, s76, 11\n"
"s_add_u32 s76, s76, 0x40000\n"
"s_lshl_b32 s99, s69, 6\n"
"s_add_u32 s99, s99, s76\n"
"s_cmp_eq_u32 s71, 0\n"
"s_cselect_b32 s99, s99, 0x2800000\n"
"v_lshlrev_b32 v64, 14, v127\n"
"v_mov_b32 v65, s68\n"
"v_bfe_u32 v66, s64, v127, 1\n"
"v_lshlrev_b32 v64, 1, v64\n"
"v_lshl_add_u32 v64, v66, 13, v64\n"
"s_waitcnt vmcnt(2)\n"
"s_barrier\n"
"s_mov_b64 exec, 0xff\n"
"buffer_store_dword v65, v64, s[36:39], s99 offen sc1\n"
"s_mov_b64 exec, -1\n"
"s_add_u32 m0, s80, 81920\n"
"s_xor_b32 s99, s2, 128\n"
"buffer_load_dwordx4 v120, s[32:35], s99 offen offset:0 lds\n"
"buffer_load_dwordx4 v121, s[32:35], s99 offen offset:1024 lds\n"
"buffer_load_dwordx4 v122, s[32:35], s99 offen offset:2048 lds\n"
"buffer_load_dwordx4 v123, s[32:35], s99 offen offset:3072 lds\n"
"s_add_u32 m0, s80, 98304\n"
"s_xor_b32 s99, s2, 1024\n"
"buffer_load_dwordx4 v120, s[32:35], s99 offen offset:0 lds\n"
"buffer_load_dwordx4 v121, s[32:35], s99 offen offset:1024 lds\n"
"buffer_load_dwordx4 v122, s[32:35], s99 offen offset:2048 lds\n"
"buffer_load_dwordx4 v123, s[32:35], s99 offen offset:3072 lds\n"
"v_bfe_u32 v40, v127, 1, 1\n"
"v_lshlrev_b32 v40, 13, v40\n"
"v_lshrrev_b32 v41, 4, v127\n"
"v_lshl_add_u32 v40, v41, 7, v40\n"
"v_bfe_u32 v41, v127, 2, 2\n"
"v_lshl_add_u32 v40, v41, 4, v40\n"
"v_and_b32 v42, 1, v127\n"
"v_lshl_add_u32 v40, v42, 3, v40\n"
"s_add_u32 s73, s81, 131072\n"
"v_add_u32 v40, s73, v40\n"
"ds_read_b64_tr_b16 v[0:1], v40 offset:0\n"
"ds_read_b64_tr_b16 v[2:3], v40 offset:64\n"
"ds_read_b64_tr_b16 v[4:5], v40 offset:512\n"
"ds_read_b64_tr_b16 v[6:7], v40 offset:576\n"
"ds_read_b64_tr_b16 v[8:9], v40 offset:1024\n"
"ds_read_b64_tr_b16 v[10:11], v40 offset:1088\n"
"ds_read_b64_tr_b16 v[12:13], v40 offset:1536\n"
"ds_read_b64_tr_b16 v[14:15], v40 offset:1600\n"
"s_lshl_b32 s73, s71, 8\n"
"s_add_u32 s73, s73, 147456\n"
"v_lshl_add_u32 v40, v127, 2, s73\n"
"ds_read2st64_b32 v[32:33], v40 offset0:0 offset1:4\n"
"ds_read2st64_b32 v[34:35], v40 offset0:8 offset1:12\n"
"ds_read2st64_b32 v[36:37], v40 offset0:16 offset1:20\n"
"ds_read2st64_b32 v[38:39], v40 offset0:24 offset1:28\n"
"s_add_u32 s73, s81, 155648\n"
"v_lshl_add_u32 v41, v127, 2, s73\n"
"v_mul_u32_u24 v42, 320, v42\n"
"v_and_b32 v43, 48, v127\n"
"v_add3_u32 v42, v42, v43, s73\n"
"v_and_b32 v43, 15, v127\n"
"v_lshlrev_b32 v43, 3, v43\n"
"v_add_u32 v43, 156672, v43\n"
"v_add_u32 v40, s81, v43\n"
"s_waitcnt lgkmcnt(0)\n"
"v_lshlrev_b32 v16, 16, v32\n"
"v_and_b32 v17, 0xffff0000, v32\n"
"v_lshlrev_b32 v18, 16, v33\n"
"v_and_b32 v19, 0xffff0000, v33\n"
"v_lshlrev_b32 v20, 16, v34\n"
"v_and_b32 v21, 0xffff0000, v34\n"
"v_lshlrev_b32 v22, 16, v35\n"
"v_and_b32 v23, 0xffff0000, v35\n"
"v_lshlrev_b32 v24, 16, v36\n"
"v_and_b32 v25, 0xffff0000, v36\n"
"v_lshlrev_b32 v26, 16, v37\n"
"v_and_b32 v27, 0xffff0000, v37\n"
"v_lshlrev_b32 v28, 16, v38\n"
"v_and_b32 v29, 0xffff0000, v38\n"
"v_lshlrev_b32 v30, 16, v39\n"
"v_and_b32 v31, 0xffff0000, v39\n"
"v_mov_b32 v112, v43\n"
"s_lshl_b32 s73, s71, 8\n"
"s_add_u32 s73, s73, 147456\n"
"v_lshl_add_u32 v113, v127, 2, s73\n"
"v_mov_b32 v114, s68\n"
"s_lshl_b32 s1, s71, 6\n"
"v_xor_b32 v89, s1, v126\n"
"v_xor_b32 v90, 16, v89\n"
"v_xor_b32 v91, 32, v89\n"
"v_xor_b32 v92, 48, v89\n"
"s_lshl_b32 s1, s70, 3\n"
"s_add_u32 s1, s1, s71\n"
"s_lshl_b32 s1, s1, 13\n"
"s_lshl_b32 s29, s69, 10\n"
"s_and_b32 s29, s29, 0x2000\n"
"s_xor_b32 s29, s29, s65\n"
"s_lshl_b32 s28, s69, 18\n"
"s_add_u32 s28, s28, s29\n"
"s_add_u32 s28, s28, 0x2000000\n"
"s_and_b32 s29, s71, 1\n"
"s_lshl_b32 s29, s29, 17\n"
"s_add_u32 s28, s28, s29\n"
"s_lshr_b32 s29, s71, 1\n"
"s_lshl_b32 s29, s29, 15\n"
"s_add_u32 s28, s28, s29\n"
"s_and_b32 s29, s70, 4\n"
"s_add_u32 s29, s29, s70\n"
"s_lshl_b32 s29, s29, 11\n"
"s_add_u32 s28, s28, s29\n"
"s_add_u32 s29, s28, 0x10000\n"
"s_lshl_b32 s30, s25, 4\n"
"s_add_u32 s60, s30, 0x10000\n"
"s_cmp_eq_u32 s71, 3\n"
"s_cselect_b32 s60, s60, 0x2800000\n"
"s_add_u32 s31, s30, 0x400000\n"
"s_add_u32 s30, s30, 0xc00000\n"
"v_mov_b32 v72, 157184\n"
"s_cmp_lg_u32 s71, 2\n"
"s_cbranch_scc1 R12_%=\n"
"s_lshl_b32 s73, s85, 14\n"
"s_add_u32 s73, s73, s65\n"
"s_add_u32 s73, s73, 0x80000\n"
"s_add_u32 s74, s73, 4096\n"
"v_lshlrev_b32 v74, 6, v127\n"
"s_waitcnt vmcnt(0)\n"
"s_mov_b32 s77, 200000\n"
"P14_%=:\n"
"buffer_load_dwordx4 v[64:67], v74, s[36:39], s73 offen sc1\n"
"buffer_load_dwordx4 v[68:71], v74, s[36:39], s74 offen sc1\n"
"s_sub_u32 s77, s77, 1\n"
"s_cselect_b64 s[26:27], 0, -1\n"
"s_waitcnt vmcnt(0)\n"
"v_xor_b32 v73, s68, v65\n"
"v_bitop3_b32 v73, v73, v67, s68 bitop3:0xf6\n"
"v_bitop3_b32 v73, v73, v69, s68 bitop3:0xf6\n"
"v_bitop3_b32 v73, v73, v71, s68 bitop3:0xf6\n"
"v_cmp_ne_u32 vcc, 0, v73\n"
"s_nop 0\n"
"s_and_b64 vcc, vcc, s[26:27]\n"
"s_cbranch_vccnz P14_%=\n"
"v_max_u32 v73, v64, v68\n"
"s_nop 1\n"
"v_max_u32_dpp v73, v73, v73 row_shr:1 row_mask:0xf bank_mask:0xf bound_ctrl:1\n"
"s_nop 1\n"
"v_max_u32_dpp v73, v73, v73 row_shr:2 row_mask:0xf bank_mask:0xf bound_ctrl:1\n"
"s_nop 1\n"
"v_max_u32_dpp v73, v73, v73 row_shr:4 row_mask:0xf bank_mask:0xf bound_ctrl:1\n"
"s_nop 1\n"
"v_max_u32_dpp v73, v73, v73 row_shr:8 row_mask:0xf bank_mask:0xf bound_ctrl:1\n"
"s_nop 1\n"
"v_max_u32_dpp v73, v73, v73 row_bcast:15 row_mask:0xa bank_mask:0xf\n"
"s_nop 1\n"
"v_max_u32_dpp v73, v73, v73 row_bcast:31 row_mask:0xc bank_mask:0xf\n"
"s_nop 0\n"
"v_readlane_b32 s86, v73, 63\n"
"s_nop 1\n"
"v_cmp_eq_u32 vcc, s86, v64\n"
"v_cmp_eq_u32 s[94:95], s86, v68\n"
"s_nop 0\n"
"v_cndmask_b32 v64, v64, v66, vcc\n"
"v_cndmask_b32_e64 v68, v68, v70, s[94:95]\n"
"v_max_u32 v73, v64, v68\n"
"s_nop 1\n"
"v_max_u32_dpp v73, v73, v73 row_shr:1 row_mask:0xf bank_mask:0xf bound_ctrl:1\n"
"s_nop 1\n"
"v_max_u32_dpp v73, v73, v73 row_shr:2 row_mask:0xf bank_mask:0xf bound_ctrl:1\n"
"s_nop 1\n"
"v_max_u32_dpp v73, v73, v73 row_shr:4 row_mask:0xf bank_mask:0xf bound_ctrl:1\n"
"s_nop 1\n"
"v_max_u32_dpp v73, v73, v73 row_shr:8 row_mask:0xf bank_mask:0xf bound_ctrl:1\n"
"s_nop 1\n"
"v_max_u32_dpp v73, v73, v73 row_bcast:15 row_mask:0xa bank_mask:0xf\n"
"s_nop 1\n"
"v_max_u32_dpp v73, v73, v73 row_bcast:31 row_mask:0xc bank_mask:0xf\n"
"s_nop 0\n"
"v_readlane_b32 s87, v73, 63\n"
"v_mov_b32 v64, s86\n"
"s_nop 0\n"
"v_mov_b32 v65, s87\n"
"ds_write_b64 v72, v[64:65]\n"
"s_waitcnt lgkmcnt(0)\n"
"R12_%=:\n"
"s_cmp_lg_u32 s71, 0\n"
"s_cbranch_scc1 R15_%=\n"
"s_lshl_b32 s75, s85, 15\n"
"s_add_u32 s75, s75, s76\n"
"s_add_u32 s75, s75, s65\n"
"v_lshlrev_b32 v76, 6, v127\n"
"v_mov_b32 v77, s68\n"
"s_waitcnt vmcnt(0)\n"
"s_mov_b32 s77, 200000\n"
"P17_%=:\n"
"s_mov_b32 exec_hi, 0\n"
"buffer_load_dword v77, v76, s[36:39], s75 offen sc1\n"
"s_mov_b64 exec, -1\n"
"s_sub_u32 s77, s77, 1\n"
"s_cselect_b64 s[26:27], 0, -1\n"
"s_waitcnt vmcnt(0)\n"
"v_cmp_ne_u32 vcc, s68, v77\n"
"s_nop 0\n"
"s_and_b64 vcc, vcc, s[26:27]\n"
"s_cbranch_vccnz P17_%=\n"
"R15_%=:\n"
"s_barrier\n"
"ds_read_b64 v[64:65], v72\n"
"s_add_u32 m0, s81, 0\n"
"s_xor_b32 s74, s24, 16384\n"
"buffer_load_dwordx4 v126, s[36:39], s74 offen offset:0 sc1 lds\n"
"buffer_load_dwordx4 v126, s[36:39], s74 offen offset:1024 sc1 lds\n"
"s_add_u32 m0, s81, 8192\n"
"s_xor_b32 s74, s24, 0\n"
"buffer_load_dwordx4 v126, s[36:39], s74 offen offset:0 sc1 lds\n"
"buffer_load_dwordx4 v126, s[36:39], s74 offen offset:1024 sc1 lds\n"
"s_waitcnt lgkmcnt(0)\n"
"v_readfirstlane_b32 s86, v64\n"
"v_readfirstlane_b32 s87, v65\n"
"s_and_b32 s88, s86, 0x7ff\n"
"s_xor_b32 s88, s88, 0x7ff\n"
"s_and_b32 s89, s87, 0x7ff\n"
"s_xor_b32 s89, s89, 0x7ff\n"
"s_lshr_b32 s22, s88, 4\n"
"s_lshr_b32 s23, s89, 4\n"
"s_cmp_eq_u32 s22, s23\n"
"s_cselect_b32 s73, 4096, 0\n"
"s_lshl_b32 s22, s22, 14\n"
"s_lshl_b32 s23, s23, 14\n"
"s_add_u32 s73, s73, s65\n"
"s_add_u32 s22, s22, s65\n"
"s_add_u32 s23, s23, s73\n"
"s_lshl_b32 s96, s71, 10\n"
"s_add_u32 s96, s96, 0x400000\n"
"s_add_u32 s76, s96, s23\n"
"s_add_u32 s96, s96, s22\n"
"buffer_load_dwordx4 v[48:51], v126, s[36:39], s96 offen sc1\n"
"buffer_load_dwordx4 v[52:55], v126, s[36:39], s76 offen sc1\n"
"s_and_b32 s73, s86, 0xfffff800\n"
"s_or_b32 s73, s73, 0x400\n"
"s_ashr_i32 s74, s73, 31\n"
"s_not_b32 s74, s74\n"
"s_or_b32 s74, s74, 0x80000000\n"
"s_xor_b32 s73, s73, s74\n"
"v_mov_b32 v74, s73\n"
"s_and_b32 s73, s87, 0xfffff800\n"
"s_or_b32 s73, s73, 0x400\n"
"s_ashr_i32 s74, s73, 31\n"
"s_not_b32 s74, s74\n"
"s_or_b32 s74, s74, 0x80000000\n"
"s_xor_b32 s73, s73, s74\n"
"v_mov_b32 v75, s73\n"
"v_mul_f32 v74, 0xbfb8aa3b, v74\n"
"v_mul_f32 v75, 0xbfb8aa3b, v75\n"
"v_exp_f32 v74, v74\n"
"v_exp_f32 v75, v75\n"
"v_add_f32 v74, 1.0, v74\n"
"v_add_f32 v75, 1.0, v75\n"
"v_rcp_f32 v74, v74\n"
"v_rcp_f32 v75, v75\n"
"v_readfirstlane_b32 s20, v74\n"
"v_readfirstlane_b32 s21, v75\n"
"s_add_u32 m0, s81, 16384\n"
"s_xor_b32 s74, s24, 24576\n"
"buffer_load_dwordx4 v126, s[36:39], s74 offen offset:0 sc1 lds\n"
"buffer_load_dwordx4 v126, s[36:39], s74 offen offset:1024 sc1 lds\n"
"s_add_u32 m0, s81, 24576\n"
"s_xor_b32 s74, s24, 32768\n"
"buffer_load_dwordx4 v126, s[36:39], s74 offen offset:0 sc1 lds\n"
"buffer_load_dwordx4 v126, s[36:39], s74 offen offset:1024 sc1 lds\n"
"s_mov_b32 s52, s6\n"
"s_and_b32 s53, s7, 0xffff\n"
"s_mov_b32 s54, 0x800000\n"
"s_mov_b32 s55, 0x20000\n"
"s_mov_b32 s56, s8\n"
"s_and_b32 s57, s9, 0xffff\n"
"s_mov_b32 s58, 0x800000\n"
"s_mov_b32 s59, 0x20000\n"
"v_mov_b32 v78, s88\n"
"s_mov_b32 exec_lo, 0xffff0000\n"
"s_mov_b32 exec_hi, 0xffff0000\n"
"v_mov_b32 v78, s89\n"
"s_mov_b64 exec, -1\n"
"v_and_b32 v79, 15, v127\n"
"v_lshlrev_b32 v79, 12, v79\n"
"v_lshl_add_u32 v78, v78, 1, v79\n"
"s_lshl_b32 s73, s3, 16\n"
"s_mov_b32 exec_hi, 0\n"
"buffer_load_ushort v94, v78, s[52:55], s73 offen\n"
"s_not_b64 exec, exec\n"
"buffer_load_ushort v94, v78, s[56:59], s73 offen\n"
"s_mov_b64 exec, -1\n"
"s_lshl_b32 s61, s71, 10\n"
"s_add_u32 s61, s61, 0x10000\n"
"s_mov_b32 s62, s96\n"
"s_mov_b32 s63, s76\n"
"s_lshr_b32 s97, s25, 1\n"
"s_lshl_b32 s97, s97, 2\n"
"s_and_b32 s98, s25, 1\n"
"s_add_u32 s97, s97, s98\n"
"s_lshl_b32 s97, s97, 2\n"
"s_add_u32 s97, s97, s71\n"
"s_lshl_b32 s97, s97, 10\n"
"s_add_u32 s97, s97, 0xc00000\n"
"s_cmp_ge_u32 s69, 28\n"
"s_cselect_b32 s97, 0x2800000, s97\n"
"s_lshl_b32 s98, s71, 1\n"
"s_lshr_b32 s98, s64, s98\n"
"v_lshrrev_b32 v93, 5, v127\n"
"v_bfe_u32 v93, s98, v93, 1\n"
"v_lshl_add_u32 v93, v93, 13, v126\n"
"s_movk_i32 s98, 0xff\n"
"v_mov_b32 v47, s68\n"
"s_waitcnt vmcnt(6)\n"
"v_xor_b32 v45, s68, v51\n"
"v_bitop3_b32 v45, v45, v55, s68 bitop3:0xf6\n"
"v_cmp_ne_u32 vcc, 0, v45\n"
"s_cbranch_vccnz S18_%=\n"
"B19_%=:\n"
"ds_write2_b32 v41, v48, v52 offset0:0 offset1:80\n"
"ds_read_b128 v[48:51], v42 offset:0\n"
"ds_read_b128 v[52:55], v42 offset:64\n"
"ds_read_b128 v[56:59], v42 offset:128\n"
"ds_read_b128 v[60:63], v42 offset:192\n"
"s_waitcnt lgkmcnt(3)\n"
"v_mfma_f32_16x16x32_bf16 v[32:35], v[48:51], v[0:3], 0\n"
"s_waitcnt lgkmcnt(2)\n"
"v_mfma_f32_16x16x32_bf16 v[32:35], v[52:55], v[4:7], v[32:35]\n"
"s_waitcnt lgkmcnt(1)\n"
"v_mfma_f32_16x16x32_bf16 v[32:35], v[56:59], v[8:11], v[32:35]\n"
"s_waitcnt lgkmcnt(0)\n"
"v_mfma_f32_16x16x32_bf16 v[32:35], v[60:63], v[12:15], v[32:35]\n"
"s_nop 7\n"
"ds_write_b64 v40, v[32:33]\n"
"s_waitcnt lgkmcnt(0)\n"
"s_barrier\n"
"ds_read_b128 v[32:35], v124 offset:0\n"
"ds_read_b128 v[36:39], v124 offset:256\n"
"ds_read_b128 v[40:43], v124 offset:512\n"
"ds_read_b128 v[44:47], v124 offset:768\n"
"s_add_u32 m0, s81, 32768\n"
"s_xor_b32 s74, s24, 8192\n"
"buffer_load_dwordx4 v126, s[36:39], s74 offen offset:0 sc1 lds\n"
"s_waitcnt lgkmcnt(2)\n"
"v_mfma_f32_16x16x32_bf16 v[0:3], v[32:35], v[128:131], 0\n"
"ds_read_b128 v[48:51], v124 offset:4096\n"
"v_mfma_f32_16x16x32_bf16 v[16:19], v[32:35], v[132:135], 0\n"
"ds_read_b128 v[52:55], v124 offset:4352\n"
"v_mfma_f32_16x16x32_bf16 v[4:7], v[36:39], v[128:131], 0\n"
"buffer_load_dwordx4 v126, s[36:39], s74 offen offset:1024 sc1 lds\n"
"v_mfma_f32_16x16x32_bf16 v[20:23], v[36:39], v[132:135], 0\n"
"s_waitcnt lgkmcnt(2)\n"
"v_mfma_f32_16x16x32_bf16 v[8:11], v[40:43], v[128:131], 0\n"
"ds_read_b128 v[56:59], v124 offset:4608\n"
"s_add_u32 m0, s81, 40960\n"
"v_mfma_f32_16x16x32_bf16 v[24:27], v[40:43], v[132:135], 0\n"
"ds_read_b128 v[60:63], v124 offset:4864\n"
"s_xor_b32 s74, s24, 40960\n"
"v_mfma_f32_16x16x32_bf16 v[12:15], v[44:47], v[128:131], 0\n"
"buffer_load_dwordx4 v126, s[36:39], s74 offen offset:0 sc1 lds\n"
"v_mfma_f32_16x16x32_bf16 v[28:31], v[44:47], v[132:135], 0\n"
"s_waitcnt lgkmcnt(2)\n"
"v_mfma_f32_16x16x32_bf16 v[0:3], v[48:51], v[136:139], v[0:3]\n"
"ds_read_b128 v[32:35], v124 offset:8192\n"
"v_mfma_f32_16x16x32_bf16 v[16:19], v[48:51], v[140:143], v[16:19]\n"
"ds_read_b128 v[36:39], v124 offset:8448\n"
"v_mfma_f32_16x16x32_bf16 v[4:7], v[52:55], v[136:139], v[4:7]\n"
"ds_read_b128 v[64:67], v116 offset:0\n"
"buffer_load_dwordx4 v126, s[36:39], s74 offen offset:1024 sc1 lds\n"
"v_mfma_f32_16x16x32_bf16 v[20:23], v[52:55], v[140:143], v[20:23]\n"
"ds_read_b128 v[68:71], v116 offset:2048\n"
"s_waitcnt lgkmcnt(4)\n"
"v_mfma_f32_16x16x32_bf16 v[8:11], v[56:59], v[136:139], v[8:11]\n"
"ds_read_b128 v[40:43], v124 offset:8704\n"
"s_add_u32 m0, s81, 49152\n"
"v_mfma_f32_16x16x32_bf16 v[24:27], v[56:59], v[140:143], v[24:27]\n"
"ds_read_b128 v[44:47], v124 offset:8960\n"
"s_xor_b32 s74, s24, 49152\n"
"v_mfma_f32_16x16x32_bf16 v[12:15], v[60:63], v[136:139], v[12:15]\n"
"buffer_load_dwordx4 v126, s[36:39], s74 offen offset:0 sc1 lds\n"
"v_mfma_f32_16x16x32_bf16 v[28:31], v[60:63], v[140:143], v[28:31]\n"
"s_waitcnt lgkmcnt(2)\n"
"v_mfma_f32_16x16x32_bf16 v[0:3], v[32:35], v[64:67], v[0:3]\n"
"ds_read_b128 v[48:51], v124 offset:12288\n"
"ds_read_b64 v[128:129], v112 offset:0\n"
"ds_read_b64 v[130:131], v112 offset:2048\n"
"v_mfma_f32_16x16x32_bf16 v[16:19], v[32:35], v[68:71], v[16:19]\n"
"ds_read_b128 v[52:55], v124 offset:12544\n"
"ds_read_b64 v[132:133], v112 offset:4096\n"
"ds_read_b64 v[134:135], v112 offset:6144\n"
"v_mfma_f32_16x16x32_bf16 v[4:7], v[36:39], v[64:67], v[4:7]\n"
"ds_read_b128 v[72:75], v117 offset:0\n"
"buffer_load_dwordx4 v126, s[36:39], s74 offen offset:1024 sc1 lds\n"
"v_mfma_f32_16x16x32_bf16 v[20:23], v[36:39], v[68:71], v[20:23]\n"
"ds_read_b128 v[76:79], v117 offset:2048\n"
"ds_read2st64_b32 v[136:137], v113 offset0:0 offset1:4\n"
"s_waitcnt lgkmcnt(9)\n"
"v_mfma_f32_16x16x32_bf16 v[8:11], v[40:43], v[64:67], v[8:11]\n"
"ds_read_b128 v[56:59], v124 offset:12800\n"
"s_add_u32 m0, s80, 114688\n"
"ds_read2st64_b32 v[138:139], v113 offset0:8 offset1:12\n"
"v_mfma_f32_16x16x32_bf16 v[24:27], v[40:43], v[68:71], v[24:27]\n"
"ds_read_b128 v[60:63], v124 offset:13056\n"
"s_xor_b32 s73, s2, 1152\n"
"ds_read2st64_b32 v[140:141], v113 offset0:16 offset1:20\n"
"v_mfma_f32_16x16x32_bf16 v[12:15], v[44:47], v[64:67], v[12:15]\n"
"buffer_load_dwordx4 v120, s[32:35], s73 offen offset:0 lds\n"
"v_mfma_f32_16x16x32_bf16 v[28:31], v[44:47], v[68:71], v[28:31]\n"
"s_waitcnt vmcnt(9) lgkmcnt(5)\n"
"s_barrier\n"
"v_mfma_f32_16x16x32_bf16 v[0:3], v[48:51], v[72:75], v[0:3]\n"
"ds_read_b128 v[32:35], v124 offset:16384\n"
"s_add_u32 m0, s81, 57344\n"
"ds_read2st64_b32 v[142:143], v113 offset0:24 offset1:28\n"
"v_mfma_f32_16x16x32_bf16 v[16:19], v[48:51], v[76:79], v[16:19]\n"
"ds_read_b128 v[36:39], v124 offset:16640\n"
"s_xor_b32 s74, s24, 57344\n"
"v_mfma_f32_16x16x32_bf16 v[4:7], v[52:55], v[72:75], v[4:7]\n"
"buffer_load_dwordx4 v126, s[36:39], s74 offen offset:0 sc1 lds\n"
"v_mfma_f32_16x16x32_bf16 v[20:23], v[52:55], v[76:79], v[20:23]\n"
"s_waitcnt lgkmcnt(4)\n"
"v_mfma_f32_16x16x32_bf16 v[8:11], v[56:59], v[72:75], v[8:11]\n"
"ds_read_b128 v[40:43], v124 offset:16896\n"
"v_mfma_f32_16x16x32_bf16 v[24:27], v[56:59], v[76:79], v[24:27]\n"
"ds_read_b128 v[44:47], v124 offset:17152\n"
"v_mfma_f32_16x16x32_bf16 v[12:15], v[60:63], v[72:75], v[12:15]\n"
"buffer_load_dwordx4 v126, s[36:39], s74 offen offset:1024 sc1 lds\n"
"v_mfma_f32_16x16x32_bf16 v[28:31], v[60:63], v[76:79], v[28:31]\n"
"s_waitcnt lgkmcnt(2)\n"
"v_mfma_f32_16x16x32_bf16 v[0:3], v[32:35], v[144:147], v[0:3]\n"
"ds_read_b128 v[48:51], v124 offset:20480\n"
"s_add_u32 m0, s81, 0\n"
"v_add_f32 v128, v128, v130\n"
"v_mfma_f32_16x16x32_bf16 v[16:19], v[32:35], v[148:151], v[16:19]\n"
"ds_read_b128 v[52:55], v124 offset:20736\n"
"s_xor_b32 s74, s24, 65536\n"
"v_add_f32 v129, v129, v131\n"
"v_mfma_f32_16x16x32_bf16 v[4:7], v[36:39], v[144:147], v[4:7]\n"
"buffer_load_dwordx4 v126, s[36:39], s74 offen offset:0 sc1 lds\n"
"v_mfma_f32_16x16x32_bf16 v[20:23], v[36:39], v[148:151], v[20:23]\n"
"v_add_f32 v132, v132, v134\n"
"v_add_f32 v133, v133, v135\n"
"s_waitcnt lgkmcnt(2)\n"
"v_mfma_f32_16x16x32_bf16 v[8:11], v[40:43], v[144:147], v[8:11]\n"
"ds_read_b128 v[56:59], v124 offset:20992\n"
"v_add_f32 v128, v128, v132\n"
"v_add_f32 v129, v129, v133\n"
"v_mfma_f32_16x16x32_bf16 v[24:27], v[40:43], v[148:151], v[24:27]\n"
"ds_read_b128 v[60:63], v124 offset:21248\n"
"v_mul_f32 v130, 0xbfb8aa3b, v128\n"
"v_mul_f32 v131, 0xbfb8aa3b, v129\n"
"v_mfma_f32_16x16x32_bf16 v[12:15], v[44:47], v[144:147], v[12:15]\n"
"buffer_load_dwordx4 v126, s[36:39], s74 offen offset:1024 sc1 lds\n"
"v_mfma_f32_16x16x32_bf16 v[28:31], v[44:47], v[148:151], v[28:31]\n"
"s_waitcnt lgkmcnt(2)\n"
"v_mfma_f32_16x16x32_bf16 v[0:3], v[48:51], v[152:155], v[0:3]\n"
"ds_read_b128 v[32:35], v124 offset:24576\n"
"v_exp_f32 v130, v130\n"
"v_exp_f32 v131, v131\n"
"v_mfma_f32_16x16x32_bf16 v[16:19], v[48:51], v[156:159], v[16:19]\n"
"ds_read_b128 v[36:39], v124 offset:24832\n"
"v_mul_f32_dpp v132, v128, v128 row_ror:8 row_mask:0xf bank_mask:0xf\n"
"v_mul_f32_dpp v133, v129, v129 row_ror:8 row_mask:0xf bank_mask:0xf\n"
"v_mfma_f32_16x16x32_bf16 v[4:7], v[52:55], v[152:155], v[4:7]\n"
"v_add_f32 v130, 1.0, v130\n"
"v_add_f32 v131, 1.0, v131\n"
"v_rcp_f32 v130, v130\n"
"v_mfma_f32_16x16x32_bf16 v[20:23], v[52:55], v[156:159], v[20:23]\n"
"v_rcp_f32 v131, v131\n"
"v_mul_f32 v132, v132, v130\n"
"s_waitcnt lgkmcnt(2)\n"
"v_mfma_f32_16x16x32_bf16 v[8:11], v[56:59], v[152:155], v[8:11]\n"
"ds_read_b128 v[40:43], v124 offset:25088\n"
"s_add_u32 m0, s80, 114688\n"
"v_mul_f32 v133, v133, v131\n"
"v_mfma_f32_16x16x32_bf16 v[24:27], v[56:59], v[156:159], v[24:27]\n"
"ds_read_b128 v[44:47], v124 offset:25344\n"
"v_readlane_b32 s4, v132, 0\n"
"v_readlane_b32 s5, v132, 1\n"
"v_mfma_f32_16x16x32_bf16 v[12:15], v[60:63], v[152:155], v[12:15]\n"
"buffer_load_dwordx4 v121, s[32:35], s73 offen offset:1024 lds\n"
"v_mfma_f32_16x16x32_bf16 v[28:31], v[60:63], v[156:159], v[28:31]\n"
"v_readlane_b32 s6, v132, 2\n"
"v_readlane_b32 s7, v132, 3\n"
"s_waitcnt lgkmcnt(2)\n"
"v_mfma_f32_16x16x32_bf16 v[0:3], v[32:35], v[160:163], v[0:3]\n"
"ds_read_b128 v[48:51], v124 offset:28672\n"
"v_readlane_b32 s8, v132, 4\n"
"v_readlane_b32 s9, v132, 5\n"
"v_mfma_f32_16x16x32_bf16 v[16:19], v[32:35], v[164:167], v[16:19]\n"
"ds_read_b128 v[52:55], v124 offset:28928\n"
"v_readlane_b32 s10, v132, 6\n"
"v_readlane_b32 s11, v132, 7\n"
"v_mfma_f32_16x16x32_bf16 v[4:7], v[36:39], v[160:163], v[4:7]\n"
"v_readlane_b32 s12, v133, 0\n"
"v_readlane_b32 s13, v133, 1\n"
"v_readlane_b32 s14, v133, 2\n"
"v_mfma_f32_16x16x32_bf16 v[20:23], v[36:39], v[164:167], v[20:23]\n"
"v_readlane_b32 s15, v133, 3\n"
"v_readlane_b32 s16, v133, 4\n"
"s_waitcnt lgkmcnt(2)\n"
"v_mfma_f32_16x16x32_bf16 v[8:11], v[40:43], v[160:163], v[8:11]\n"
"ds_read_b128 v[56:59], v124 offset:29184\n"
"v_readlane_b32 s17, v133, 5\n"
"v_readlane_b32 s18, v133, 6\n"
"v_mfma_f32_16x16x32_bf16 v[24:27], v[40:43], v[164:167], v[24:27]\n"
"ds_read_b128 v[60:63], v124 offset:29440\n"
"v_readlane_b32 s19, v133, 7\n"
"v_lshlrev_b32 v144, 16, v136\n"
"v_mfma_f32_16x16x32_bf16 v[12:15], v[44:47], v[160:163], v[12:15]\n"
"buffer_load_dwordx4 v122, s[32:35], s73 offen offset:2048 lds\n"
"v_mfma_f32_16x16x32_bf16 v[28:31], v[44:47], v[164:167], v[28:31]\n"
"s_waitcnt vmcnt(9) lgkmcnt(2)\n"
"s_barrier\n"
"v_mfma_f32_16x16x32_bf16 v[0:3], v[48:51], v[168:171], v[0:3]\n"
"ds_read_b128 v[32:35], v124 offset:32768\n"
"s_add_u32 m0, s81, 8192\n"
"v_and_b32 v145, 0xffff0000, v136\n"
"v_mfma_f32_16x16x32_bf16 v[16:19], v[48:51], v[172:175], v[16:19]\n"
"ds_read_b128 v[36:39], v124 offset:33024\n"
"s_xor_b32 s74, s24, 81920\n"
"v_lshlrev_b32 v146, 16, v137\n"
"v_mfma_f32_16x16x32_bf16 v[4:7], v[52:55], v[168:171], v[4:7]\n"
"ds_read_b128 v[64:67], v116 offset:16384\n"
"buffer_load_dwordx4 v126, s[36:39], s74 offen offset:0 sc1 lds\n"
"v_mfma_f32_16x16x32_bf16 v[20:23], v[52:55], v[172:175], v[20:23]\n"
"ds_read_b128 v[68:71], v116 offset:18432\n"
"v_and_b32 v147, 0xffff0000, v137\n"
"s_waitcnt lgkmcnt(4)\n"
"v_mfma_f32_16x16x32_bf16 v[8:11], v[56:59], v[168:171], v[8:11]\n"
"ds_read_b128 v[40:43], v124 offset:33280\n"
"s_add_u32 m0, s80, 114688\n"
"v_lshlrev_b32 v148, 16, v138\n"
"v_mfma_f32_16x16x32_bf16 v[24:27], v[56:59], v[172:175], v[24:27]\n"
"ds_read_b128 v[44:47], v124 offset:33536\n"
"v_and_b32 v149, 0xffff0000, v138\n"
"v_lshlrev_b32 v150, 16, v139\n"
"v_mfma_f32_16x16x32_bf16 v[12:15], v[60:63], v[168:171], v[12:15]\n"
"buffer_load_dwordx4 v123, s[32:35], s73 offen offset:3072 lds\n"
"v_mfma_f32_16x16x32_bf16 v[28:31], v[60:63], v[172:175], v[28:31]\n"
"v_and_b32 v151, 0xffff0000, v139\n"
"v_lshlrev_b32 v152, 16, v140\n"
"s_waitcnt lgkmcnt(2)\n"
"v_mfma_f32_16x16x32_bf16 v[0:3], v[32:35], v[64:67], v[0:3]\n"
"ds_read_b128 v[48:51], v124 offset:36864\n"
"s_add_u32 m0, s81, 8192\n"
"v_and_b32 v153, 0xffff0000, v140\n"
"v_mfma_f32_16x16x32_bf16 v[16:19], v[32:35], v[68:71], v[16:19]\n"
"ds_read_b128 v[52:55], v124 offset:37120\n"
"v_lshlrev_b32 v154, 16, v141\n"
"v_and_b32 v155, 0xffff0000, v141\n"
"v_mfma_f32_16x16x32_bf16 v[4:7], v[36:39], v[64:67], v[4:7]\n"
"ds_read_b128 v[72:75], v117 offset:16384\n"
"buffer_load_dwordx4 v126, s[36:39], s74 offen offset:1024 sc1 lds\n"
"v_mfma_f32_16x16x32_bf16 v[20:23], v[36:39], v[68:71], v[20:23]\n"
"ds_read_b128 v[76:79], v117 offset:18432\n"
"v_lshlrev_b32 v156, 16, v142\n"
"s_waitcnt lgkmcnt(4)\n"
"v_mfma_f32_16x16x32_bf16 v[8:11], v[40:43], v[64:67], v[8:11]\n"
"ds_read_b128 v[56:59], v124 offset:37376\n"
"s_add_u32 m0, s80, 65536\n"
"v_and_b32 v157, 0xffff0000, v142\n"
"v_mfma_f32_16x16x32_bf16 v[24:27], v[40:43], v[68:71], v[24:27]\n"
"ds_read_b128 v[60:63], v124 offset:37632\n"
"s_xor_b32 s73, s2, 2048\n"
"v_lshlrev_b32 v158, 16, v143\n"
"v_mfma_f32_16x16x32_bf16 v[12:15], v[44:47], v[64:67], v[12:15]\n"
"buffer_load_dwordx4 v120, s[32:35], s73 offen offset:0 lds\n"
"v_mfma_f32_16x16x32_bf16 v[28:31], v[44:47], v[68:71], v[28:31]\n"
"s_waitcnt lgkmcnt(2)\n"
"v_mfma_f32_16x16x32_bf16 v[0:3], v[48:51], v[72:75], v[0:3]\n"
"ds_read_b128 v[32:35], v124 offset:40960\n"
"s_add_u32 m0, s81, 16384\n"
"v_and_b32 v159, 0xffff0000, v143\n"
"v_mfma_f32_16x16x32_bf16 v[16:19], v[48:51], v[76:79], v[16:19]\n"
"ds_read_b128 v[36:39], v124 offset:41216\n"
"s_xor_b32 s74, s24, 90112\n"
"v_mul_f32 v128, s4, v144\n"
"v_mfma_f32_16x16x32_bf16 v[4:7], v[52:55], v[72:75], v[4:7]\n"
"buffer_load_dwordx4 v126, s[36:39], s74 offen offset:0 sc1 lds\n"
"v_mfma_f32_16x16x32_bf16 v[20:23], v[52:55], v[76:79], v[20:23]\n"
"v_mul_f32 v129, s4, v145\n"
"v_mul_f32 v130, s12, v144\n"
"s_waitcnt lgkmcnt(2)\n"
"v_mfma_f32_16x16x32_bf16 v[8:11], v[56:59], v[72:75], v[8:11]\n"
"ds_read_b128 v[40:43], v124 offset:41472\n"
"s_add_u32 m0, s80, 65536\n"
"v_mul_f32 v131, s12, v145\n"
"v_mfma_f32_16x16x32_bf16 v[24:27], v[56:59], v[76:79], v[24:27]\n"
"ds_read_b128 v[44:47], v124 offset:41728\n"
"v_fmac_f32 v128, s5, v146\n"
"v_fmac_f32 v129, s5, v147\n"
"v_mfma_f32_16x16x32_bf16 v[12:15], v[60:63], v[72:75], v[12:15]\n"
"buffer_load_dwordx4 v121, s[32:35], s73 offen offset:1024 lds\n"
"v_mfma_f32_16x16x32_bf16 v[28:31], v[60:63], v[76:79], v[28:31]\n"
"v_fmac_f32 v130, s13, v146\n"
"v_fmac_f32 v131, s13, v147\n"
"s_waitcnt lgkmcnt(2)\n"
"v_mfma_f32_16x16x32_bf16 v[0:3], v[32:35], v[176:179], v[0:3]\n"
"ds_read_b128 v[48:51], v124 offset:45056\n"
"s_add_u32 m0, s81, 16384\n"
"v_fmac_f32 v128, s6, v148\n"
"v_mfma_f32_16x16x32_bf16 v[16:19], v[32:35], v[180:183], v[16:19]\n"
"ds_read_b128 v[52:55], v124 offset:45312\n"
"v_fmac_f32 v129, s6, v149\n"
"v_fmac_f32 v130, s14, v148\n"
"v_mfma_f32_16x16x32_bf16 v[4:7], v[36:39], v[176:179], v[4:7]\n"
"buffer_load_dwordx4 v126, s[36:39], s74 offen offset:1024 sc1 lds\n"
"v_mfma_f32_16x16x32_bf16 v[20:23], v[36:39], v[180:183], v[20:23]\n"
"v_fmac_f32 v131, s14, v149\n"
"v_fmac_f32 v128, s7, v150\n"
"s_waitcnt lgkmcnt(2)\n"
"v_mfma_f32_16x16x32_bf16 v[8:11], v[40:43], v[176:179], v[8:11]\n"
"ds_read_b128 v[56:59], v124 offset:45568\n"
"s_add_u32 m0, s80, 65536\n"
"v_fmac_f32 v129, s7, v151\n"
"v_mfma_f32_16x16x32_bf16 v[24:27], v[40:43], v[180:183], v[24:27]\n"
"ds_read_b128 v[60:63], v124 offset:45824\n"
"v_fmac_f32 v130, s15, v150\n"
"v_fmac_f32 v131, s15, v151\n"
"v_mfma_f32_16x16x32_bf16 v[12:15], v[44:47], v[176:179], v[12:15]\n"
"buffer_load_dwordx4 v122, s[32:35], s73 offen offset:2048 lds\n"
"v_mfma_f32_16x16x32_bf16 v[28:31], v[44:47], v[180:183], v[28:31]\n"
"s_waitcnt vmcnt(12) lgkmcnt(2)\n"
"s_barrier\n"
"v_mfma_f32_16x16x32_bf16 v[0:3], v[48:51], v[184:187], v[0:3]\n"
"ds_read_b128 v[32:35], v124 offset:49152\n"
"s_add_u32 m0, s81, 24576\n"
"v_fmac_f32 v128, s8, v152\n"
"v_mfma_f32_16x16x32_bf16 v[16:19], v[48:51], v[188:191], v[16:19]\n"
"ds_read_b128 v[36:39], v124 offset:49408\n"
"s_xor_b32 s74, s24, 73728\n"
"v_fmac_f32 v129, s8, v153\n"
"v_mfma_f32_16x16x32_bf16 v[4:7], v[52:55], v[184:187], v[4:7]\n"
"buffer_load_dwordx4 v126, s[36:39], s74 offen offset:0 sc1 lds\n"
"v_mfma_f32_16x16x32_bf16 v[20:23], v[52:55], v[188:191], v[20:23]\n"
"v_fmac_f32 v130, s16, v152\n"
"v_fmac_f32 v131, s16, v153\n"
"s_waitcnt lgkmcnt(2)\n"
"v_mfma_f32_16x16x32_bf16 v[8:11], v[56:59], v[184:187], v[8:11]\n"
"ds_read_b128 v[40:43], v124 offset:49664\n"
"s_add_u32 m0, s80, 65536\n"
"v_fmac_f32 v128, s9, v154\n"
"v_mfma_f32_16x16x32_bf16 v[24:27], v[56:59], v[188:191], v[24:27]\n"
"ds_read_b128 v[44:47], v124 offset:49920\n"
"v_fmac_f32 v129, s9, v155\n"
"v_fmac_f32 v130, s17, v154\n"
"v_mfma_f32_16x16x32_bf16 v[12:15], v[60:63], v[184:187], v[12:15]\n"
"buffer_load_dwordx4 v123, s[32:35], s73 offen offset:3072 lds\n"
"v_mfma_f32_16x16x32_bf16 v[28:31], v[60:63], v[188:191], v[28:31]\n"
"v_fmac_f32 v131, s17, v155\n"
"v_fmac_f32 v128, s10, v156\n"
"s_waitcnt lgkmcnt(2)\n"
"v_mfma_f32_16x16x32_bf16 v[0:3], v[32:35], v[192:195], v[0:3]\n"
"ds_read_b128 v[48:51], v124 offset:53248\n"
"s_add_u32 m0, s81, 24576\n"
"v_fmac_f32 v129, s10, v157\n"
"v_mfma_f32_16x16x32_bf16 v[16:19], v[32:35], v[196:199], v[16:19]\n"
"ds_read_b128 v[52:55], v124 offset:53504\n"
"v_fmac_f32 v130, s18, v156\n"
"v_fmac_f32 v131, s18, v157\n"
"v_mfma_f32_16x16x32_bf16 v[4:7], v[36:39], v[192:195], v[4:7]\n"
"buffer_load_dwordx4 v126, s[36:39], s74 offen offset:1024 sc1 lds\n"
"v_mfma_f32_16x16x32_bf16 v[20:23], v[36:39], v[196:199], v[20:23]\n"
"v_fmac_f32 v128, s11, v158\n"
"v_fmac_f32 v129, s11, v159\n"
"s_waitcnt lgkmcnt(2)\n"
"v_mfma_f32_16x16x32_bf16 v[8:11], v[40:43], v[192:195], v[8:11]\n"
"ds_read_b128 v[56:59], v124 offset:53760\n"
"s_add_u32 m0, s80, 81920\n"
"v_fmac_f32 v130, s19, v158\n"
"v_mfma_f32_16x16x32_bf16 v[24:27], v[40:43], v[196:199], v[24:27]\n"
"ds_read_b128 v[60:63], v124 offset:54016\n"
"s_xor_b32 s73, s2, 2176\n"
"v_fmac_f32 v131, s19, v159\n"
"v_mfma_f32_16x16x32_bf16 v[12:15], v[44:47], v[192:195], v[12:15]\n"
"buffer_load_dwordx4 v120, s[32:35], s73 offen offset:0 lds\n"
"v_mfma_f32_16x16x32_bf16 v[28:31], v[44:47], v[196:199], v[28:31]\n"
"s_waitcnt lgkmcnt(2)\n"
"v_mfma_f32_16x16x32_bf16 v[0:3], v[48:51], v[200:203], v[0:3]\n"
"ds_read_b128 v[32:35], v124 offset:57344\n"
"s_add_u32 m0, s81, 32768\n"
"v_bfi_b32 v128, s98, v114, v128\n"
"v_mfma_f32_16x16x32_bf16 v[16:19], v[48:51], v[204:207], v[16:19]\n"
"ds_read_b128 v[36:39], v124 offset:57600\n"
"s_xor_b32 s74, s24, 98304\n"
"v_bfi_b32 v129, s98, v114, v129\n"
"v_mfma_f32_16x16x32_bf16 v[4:7], v[52:55], v[200:203], v[4:7]\n"
"buffer_load_dwordx4 v126, s[36:39], s74 offen offset:0 sc1 lds\n"
"v_mfma_f32_16x16x32_bf16 v[20:23], v[52:55], v[204:207], v[20:23]\n"
"v_bfi_b32 v130, s98, v114, v130\n"
"v_bfi_b32 v131, s98, v114, v131\n"
"s_waitcnt lgkmcnt(2)\n"
"v_mfma_f32_16x16x32_bf16 v[8:11], v[56:59], v[200:203], v[8:11]\n"
"ds_read_b128 v[40:43], v124 offset:57856\n"
"v_mfma_f32_16x16x32_bf16 v[24:27], v[56:59], v[204:207], v[24:27]\n"
"ds_read_b128 v[44:47], v124 offset:58112\n"
"v_mfma_f32_16x16x32_bf16 v[12:15], v[60:63], v[200:203], v[12:15]\n"
"buffer_store_dwordx4 v[128:131], v126, s[36:39], s97 offen sc1\n"
"s_sub_u32 s98, s68, 1\n"
"v_mov_b32 v132, s98\n"
"v_mfma_f32_16x16x32_bf16 v[28:31], v[60:63], v[204:207], v[28:31]\n"
"v_mov_b32 v133, s98\n"
"v_mov_b32 v134, s98\n"
"s_waitcnt lgkmcnt(2)\n"
"v_mfma_f32_16x16x32_bf16 v[0:3], v[32:35], v[208:211], v[0:3]\n"
"ds_read_b128 v[48:51], v124 offset:61440\n"
"v_mov_b32 v135, s98\n"
"s_add_u32 s97, s97, 0x400000\n"
"v_mfma_f32_16x16x32_bf16 v[16:19], v[32:35], v[212:215], v[16:19]\n"
"ds_read_b128 v[52:55], v124 offset:61696\n"
"v_mfma_f32_16x16x32_bf16 v[4:7], v[36:39], v[208:211], v[4:7]\n"
"buffer_load_dwordx4 v126, s[36:39], s74 offen offset:1024 sc1 lds\n"
"v_mfma_f32_16x16x32_bf16 v[20:23], v[36:39], v[212:215], v[20:23]\n"
"s_waitcnt lgkmcnt(2)\n"
"v_mfma_f32_16x16x32_bf16 v[8:11], v[40:43], v[208:211], v[8:11]\n"
"ds_read_b128 v[56:59], v124 offset:61952\n"
"v_mfma_f32_16x16x32_bf16 v[24:27], v[40:43], v[212:215], v[24:27]\n"
"ds_read_b128 v[60:63], v124 offset:62208\n"
"v_mfma_f32_16x16x32_bf16 v[12:15], v[44:47], v[208:211], v[12:15]\n"
"buffer_store_dwordx4 v[132:135], v93, s[36:39], s97 offen sc1\n"
"s_or_b64 s[8:9], s[92:93], 1\n"
"v_and_b32 v144, 62, v127\n"
"v_mfma_f32_16x16x32_bf16 v[28:31], v[44:47], v[212:215], v[28:31]\n"
"s_waitcnt vmcnt(13) lgkmcnt(2)\n"
"s_barrier\n"
"v_mfma_f32_16x16x32_bf16 v[0:3], v[48:51], v[216:219], v[0:3]\n"
"ds_read_b128 v[32:35], v124 offset:0\n"
"s_add_u32 m0, s81, 40960\n"
"v_add_u32 v144, v144, v127\n"
"v_mfma_f32_16x16x32_bf16 v[16:19], v[48:51], v[220:223], v[16:19]\n"
"ds_read_b128 v[36:39], v124 offset:256\n"
"s_xor_b32 s74, s24, 106496\n"
"v_lshl_add_u32 v144, v144, 12, 0\n"
"v_mfma_f32_16x16x32_bf16 v[4:7], v[52:55], v[216:219], v[4:7]\n"
"ds_read_b128 v[64:67], v116 offset:32768\n"
"buffer_load_dwordx4 v126, s[36:39], s74 offen offset:0 sc1 lds\n"
"v_mfma_f32_16x16x32_bf16 v[20:23], v[52:55], v[220:223], v[20:23]\n"
"ds_read_b128 v[68:71], v116 offset:34816\n"
"v_mov_b32 v145, s22\n"
"s_waitcnt lgkmcnt(4)\n"
"v_mfma_f32_16x16x32_bf16 v[8:11], v[56:59], v[216:219], v[8:11]\n"
"ds_read_b128 v[40:43], v124 offset:512\n"
"s_add_u32 m0, s80, 81920\n"
"v_mov_b32 v147, s20\n"
"v_mfma_f32_16x16x32_bf16 v[24:27], v[56:59], v[220:223], v[24:27]\n"
"ds_read_b128 v[44:47], v124 offset:768\n"
"s_mov_b32 exec_lo, 0xffff0000\n"
"s_mov_b32 exec_hi, 0xffff0000\n"
"v_mov_b32 v145, s23\n"
"v_mov_b32 v147, s21\n"
"s_mov_b64 exec, -1\n"
"v_mfma_f32_16x16x32_bf16 v[12:15], v[60:63], v[216:219], v[12:15]\n"
"buffer_load_dwordx4 v121, s[32:35], s73 offen offset:1024 lds\n"
"v_mfma_f32_16x16x32_bf16 v[28:31], v[60:63], v[220:223], v[28:31]\n"
"v_mov_b32 v140, s0\n"
"v_mov_b32 v141, s0\n"
"s_waitcnt lgkmcnt(2)\n"
"v_mfma_f32_16x16x32_bf16 v[0:3], v[32:35], v[64:67], v[0:3]\n"
"ds_read_b128 v[48:51], v124 offset:4096\n"
"s_add_u32 m0, s81, 40960\n"
"v_mov_b32 v142, s0\n"
"v_mfma_f32_16x16x32_bf16 v[16:19], v[32:35], v[68:71], v[16:19]\n"
"ds_read_b128 v[52:55], v124 offset:4352\n"
"v_mov_b32 v143, s0\n"
"v_mfma_f32_16x16x32_bf16 v[4:7], v[36:39], v[64:67], v[4:7]\n"
"ds_read_b128 v[72:75], v117 offset:32768\n"
"buffer_load_dwordx4 v126, s[36:39], s74 offen offset:1024 sc1 lds\n"
"v_mfma_f32_16x16x32_bf16 v[20:23], v[36:39], v[68:71], v[20:23]\n"
"ds_read_b128 v[76:79], v117 offset:34816\n"
"s_waitcnt lgkmcnt(4)\n"
"v_mfma_f32_16x16x32_bf16 v[8:11], v[40:43], v[64:67], v[8:11]\n"
"ds_read_b128 v[56:59], v124 offset:4608\n"
"s_add_u32 m0, s80, 81920\n"
"v_mfma_f32_16x16x32_bf16 v[24:27], v[40:43], v[68:71], v[24:27]\n"
"ds_read_b128 v[60:63], v124 offset:4864\n"
"v_mfma_f32_16x16x32_bf16 v[12:15], v[44:47], v[64:67], v[12:15]\n"
"buffer_load_dwordx4 v122, s[32:35], s73 offen offset:2048 lds\n"
"v_mfma_f32_16x16x32_bf16 v[28:31], v[44:47], v[68:71], v[28:31]\n"
"s_waitcnt lgkmcnt(2)\n"
"v_mfma_f32_16x16x32_bf16 v[0:3], v[48:51], v[72:75], v[0:3]\n"
"ds_read_b128 v[32:35], v124 offset:8192\n"
"s_add_u32 m0, s81, 49152\n"
"v_mfma_f32_16x16x32_bf16 v[16:19], v[48:51], v[76:79], v[16:19]\n"
"ds_read_b128 v[36:39], v124 offset:8448\n"
"s_xor_b32 s74, s24, 114688\n"
"v_mfma_f32_16x16x32_bf16 v[4:7], v[52:55], v[72:75], v[4:7]\n"
"buffer_load_dwordx4 v126, s[36:39], s74 offen offset:0 sc1 lds\n"
"v_mfma_f32_16x16x32_bf16 v[20:23], v[52:55], v[76:79], v[20:23]\n"
"s_waitcnt lgkmcnt(2)\n"
"v_mfma_f32_16x16x32_bf16 v[8:11], v[56:59], v[72:75], v[8:11]\n"
"ds_read_b128 v[40:43], v124 offset:8704\n"
"s_add_u32 m0, s80, 81920\n"
"v_mfma_f32_16x16x32_bf16 v[24:27], v[56:59], v[76:79], v[24:27]\n"
"ds_read_b128 v[44:47], v124 offset:8960\n"
"v_mfma_f32_16x16x32_bf16 v[12:15], v[60:63], v[72:75], v[12:15]\n"
"buffer_load_dwordx4 v123, s[32:35], s73 offen offset:3072 lds\n"
"v_mfma_f32_16x16x32_bf16 v[28:31], v[60:63], v[76:79], v[28:31]\n"
"s_waitcnt lgkmcnt(2)\n"
"v_mfma_f32_16x16x32_bf16 v[0:3], v[32:35], v[224:227], v[0:3]\n"
"ds_read_b128 v[48:51], v124 offset:12288\n"
"s_add_u32 m0, s81, 49152\n"
"v_mfma_f32_16x16x32_bf16 v[16:19], v[32:35], v[228:231], v[16:19]\n"
"ds_read_b128 v[52:55], v124 offset:12544\n"
"v_mfma_f32_16x16x32_bf16 v[4:7], v[36:39], v[224:227], v[4:7]\n"
"buffer_load_dwordx4 v126, s[36:39], s74 offen offset:1024 sc1 lds\n"
"v_mfma_f32_16x16x32_bf16 v[20:23], v[36:39], v[228:231], v[20:23]\n"
"s_waitcnt lgkmcnt(2)\n"
"v_mfma_f32_16x16x32_bf16 v[8:11], v[40:43], v[224:227], v[8:11]\n"
"ds_read_b128 v[56:59], v124 offset:12800\n"
"v_mfma_f32_16x16x32_bf16 v[24:27], v[40:43], v[228:231], v[24:27]\n"
"ds_read_b128 v[60:63], v124 offset:13056\n"
"v_mfma_f32_16x16x32_bf16 v[12:15], v[44:47], v[224:227], v[12:15]\n"
"v_mfma_f32_16x16x32_bf16 v[28:31], v[44:47], v[228:231], v[28:31]\n"
"s_waitcnt vmcnt(12) lgkmcnt(2)\n"
"s_barrier\n"
"v_mfma_f32_16x16x32_bf16 v[0:3], v[48:51], v[232:235], v[0:3]\n"
"ds_read_b128 v[32:35], v124 offset:16384\n"
"s_add_u32 m0, s81, 57344\n"
"v_mfma_f32_16x16x32_bf16 v[16:19], v[48:51], v[236:239], v[16:19]\n"
"ds_read_b128 v[36:39], v124 offset:16640\n"
"s_xor_b32 s74, s24, 131072\n"
"v_mfma_f32_16x16x32_bf16 v[4:7], v[52:55], v[232:235], v[4:7]\n"
"buffer_load_dwordx4 v126, s[36:39], s74 offen offset:0 sc1 lds\n"
"v_mfma_f32_16x16x32_bf16 v[20:23], v[52:55], v[236:239], v[20:23]\n"
"s_waitcnt lgkmcnt(2)\n"
"v_mfma_f32_16x16x32_bf16 v[8:11], v[56:59], v[232:235], v[8:11]\n"
"ds_read_b128 v[40:43], v124 offset:16896\n"
"s_add_u32 m0, s80, 98304\n"
"v_mfma_f32_16x16x32_bf16 v[24:27], v[56:59], v[236:239], v[24:27]\n"
"ds_read_b128 v[44:47], v124 offset:17152\n"
"s_xor_b32 s73, s2, 3072\n"
"v_mfma_f32_16x16x32_bf16 v[12:15], v[60:63], v[232:235], v[12:15]\n"
"buffer_load_dwordx4 v120, s[32:35], s73 offen offset:0 lds\n"
"v_mfma_f32_16x16x32_bf16 v[28:31], v[60:63], v[236:239], v[28:31]\n"
"s_waitcnt lgkmcnt(2)\n"
"v_mfma_f32_16x16x32_bf16 v[0:3], v[32:35], v[240:243], v[0:3]\n"
"ds_read_b128 v[48:51], v124 offset:20480\n"
"s_add_u32 m0, s81, 57344\n"
"v_mfma_f32_16x16x32_bf16 v[16:19], v[32:35], v[244:247], v[16:19]\n"
"ds_read_b128 v[52:55], v124 offset:20736\n"
"v_mfma_f32_16x16x32_bf16 v[4:7], v[36:39], v[240:243], v[4:7]\n"
"buffer_load_dwordx4 v126, s[36:39], s74 offen offset:1024 sc1 lds\n"
"v_mfma_f32_16x16x32_bf16 v[20:23], v[36:39], v[244:247], v[20:23]\n"
"s_waitcnt lgkmcnt(2)\n"
"v_mfma_f32_16x16x32_bf16 v[8:11], v[40:43], v[240:243], v[8:11]\n"
"ds_read_b128 v[56:59], v124 offset:20992\n"
"s_add_u32 m0, s80, 98304\n"
"v_mfma_f32_16x16x32_bf16 v[24:27], v[40:43], v[244:247], v[24:27]\n"
"ds_read_b128 v[60:63], v124 offset:21248\n"
"v_mfma_f32_16x16x32_bf16 v[12:15], v[44:47], v[240:243], v[12:15]\n"
"buffer_load_dwordx4 v121, s[32:35], s73 offen offset:1024 lds\n"
"v_mfma_f32_16x16x32_bf16 v[28:31], v[44:47], v[244:247], v[28:31]\n"
"s_waitcnt lgkmcnt(2)\n"
"v_mfma_f32_16x16x32_bf16 v[0:3], v[48:51], v[248:251], v[0:3]\n"
"ds_read_b128 v[32:35], v124 offset:24576\n"
"s_add_u32 m0, s81, 0\n"
"v_mfma_f32_16x16x32_bf16 v[16:19], v[48:51], v[252:255], v[16:19]\n"
"ds_read_b128 v[36:39], v124 offset:24832\n"
"s_xor_b32 s74, s24, 122880\n"
"v_mfma_f32_16x16x32_bf16 v[4:7], v[52:55], v[248:251], v[4:7]\n"
"ds_read_b128 v[64:67], v116 offset:49152\n"
"buffer_load_dwordx4 v126, s[36:39], s74 offen offset:0 sc1 lds\n"
"v_mfma_f32_16x16x32_bf16 v[20:23], v[52:55], v[252:255], v[20:23]\n"
"ds_read_b128 v[68:71], v116 offset:51200\n"
"s_waitcnt lgkmcnt(4)\n"
"v_mfma_f32_16x16x32_bf16 v[8:11], v[56:59], v[248:251], v[8:11]\n"
"ds_read_b128 v[40:43], v124 offset:25088\n"
"v_mfma_f32_16x16x32_bf16 v[24:27], v[56:59], v[252:255], v[24:27]\n"
"ds_read_b128 v[44:47], v124 offset:25344\n"
"v_mfma_f32_16x16x32_bf16 v[12:15], v[60:63], v[248:251], v[12:15]\n"
"s_mov_b64 exec, s[8:9]\n"
"buffer_load_dwordx4 v[148:151], v145, s[36:39], s31 offen sc1\n"
"s_mov_b64 exec, -1\n"
"v_mfma_f32_16x16x32_bf16 v[28:31], v[60:63], v[252:255], v[28:31]\n"
"s_waitcnt lgkmcnt(2)\n"
"v_mfma_f32_16x16x32_bf16 v[0:3], v[32:35], v[64:67], v[0:3]\n"
"ds_read_b128 v[48:51], v124 offset:28672\n"
"v_mfma_f32_16x16x32_bf16 v[16:19], v[32:35], v[68:71], v[16:19]\n"
"ds_read_b128 v[52:55], v124 offset:28928\n"
"v_mfma_f32_16x16x32_bf16 v[4:7], v[36:39], v[64:67], v[4:7]\n"
"ds_read_b128 v[72:75], v117 offset:49152\n"
"buffer_load_dwordx4 v126, s[36:39], s74 offen offset:1024 sc1 lds\n"
"v_mfma_f32_16x16x32_bf16 v[20:23], v[36:39], v[68:71], v[20:23]\n"
"ds_read_b128 v[76:79], v117 offset:51200\n"
"s_waitcnt lgkmcnt(4)\n"
"v_mfma_f32_16x16x32_bf16 v[8:11], v[40:43], v[64:67], v[8:11]\n"
"ds_read_b128 v[56:59], v124 offset:29184\n"
"v_mfma_f32_16x16x32_bf16 v[24:27], v[40:43], v[68:71], v[24:27]\n"
"ds_read_b128 v[60:63], v124 offset:29440\n"
"v_mfma_f32_16x16x32_bf16 v[12:15], v[44:47], v[64:67], v[12:15]\n"
"s_mov_b64 exec, s[8:9]\n"
"s_add_u32 s99, s30, 0\n"
"buffer_load_dwordx4 v[128:131], v144, s[36:39], s99 offen sc1\n"
"s_mov_b64 exec, -1\n"
"v_mfma_f32_16x16x32_bf16 v[28:31], v[44:47], v[68:71], v[28:31]\n"
"s_waitcnt vmcnt(12) lgkmcnt(2)\n"
"s_barrier\n"
"v_mfma_f32_16x16x32_bf16 v[0:3], v[48:51], v[72:75], v[0:3]\n"
"ds_read_b128 v[32:35], v124 offset:32768\n"
"s_add_u32 m0, s81, 8192\n"
"v_mfma_f32_16x16x32_bf16 v[16:19], v[48:51], v[76:79], v[16:19]\n"
"ds_read_b128 v[36:39], v124 offset:33024\n"
"s_xor_b32 s74, s24, 147456\n"
"v_mfma_f32_16x16x32_bf16 v[4:7], v[52:55], v[72:75], v[4:7]\n"
"buffer_load_dwordx4 v126, s[36:39], s74 offen offset:0 sc1 lds\n"
"v_mfma_f32_16x16x32_bf16 v[20:23], v[52:55], v[76:79], v[20:23]\n"
"s_waitcnt lgkmcnt(2)\n"
"v_mfma_f32_16x16x32_bf16 v[8:11], v[56:59], v[72:75], v[8:11]\n"
"ds_read_b128 v[40:43], v124 offset:33280\n"
"v_mfma_f32_16x16x32_bf16 v[24:27], v[56:59], v[76:79], v[24:27]\n"
"ds_read_b128 v[44:47], v124 offset:33536\n"
"v_mfma_f32_16x16x32_bf16 v[12:15], v[60:63], v[72:75], v[12:15]\n"
"s_mov_b64 exec, s[8:9]\n"
"s_add_u32 s99, s30, 524288\n"
"buffer_load_dwordx4 v[132:135], v144, s[36:39], s99 offen sc1\n"
"s_mov_b64 exec, -1\n"
"v_mfma_f32_16x16x32_bf16 v[28:31], v[60:63], v[76:79], v[28:31]\n"
"s_waitcnt lgkmcnt(2)\n"
"v_mfma_f32_16x16x32_bf16 v[0:3], v[32:35], a[0:3], v[0:3]\n"
"ds_read_b128 v[48:51], v124 offset:36864\n"
"v_mfma_f32_16x16x32_bf16 v[16:19], v[32:35], a[4:7], v[16:19]\n"
"ds_read_b128 v[52:55], v124 offset:37120\n"
"v_mfma_f32_16x16x32_bf16 v[4:7], v[36:39], a[0:3], v[4:7]\n"
"buffer_load_dwordx4 v126, s[36:39], s74 offen offset:1024 sc1 lds\n"
"v_mfma_f32_16x16x32_bf16 v[20:23], v[36:39], a[4:7], v[20:23]\n"
"s_waitcnt lgkmcnt(2)\n"
"v_mfma_f32_16x16x32_bf16 v[8:11], v[40:43], a[0:3], v[8:11]\n"
"ds_read_b128 v[56:59], v124 offset:37376\n"
"v_mfma_f32_16x16x32_bf16 v[24:27], v[40:43], a[4:7], v[24:27]\n"
"ds_read_b128 v[60:63], v124 offset:37632\n"
"v_mfma_f32_16x16x32_bf16 v[12:15], v[44:47], a[0:3], v[12:15]\n"
"s_mov_b64 exec, s[8:9]\n"
"s_add_u32 s99, s30, 1048576\n"
"buffer_load_dwordx4 v[136:139], v144, s[36:39], s99 offen sc1\n"
"s_mov_b64 exec, -1\n"
"v_mfma_f32_16x16x32_bf16 v[28:31], v[44:47], a[4:7], v[28:31]\n"
"s_waitcnt lgkmcnt(2)\n"
"v_mfma_f32_16x16x32_bf16 v[0:3], v[48:51], a[8:11], v[0:3]\n"
"ds_read_b128 v[32:35], v124 offset:40960\n"
"s_add_u32 m0, s81, 16384\n"
"v_mfma_f32_16x16x32_bf16 v[16:19], v[48:51], a[12:15], v[16:19]\n"
"ds_read_b128 v[36:39], v124 offset:41216\n"
"s_xor_b32 s74, s24, 139264\n"
"v_mfma_f32_16x16x32_bf16 v[4:7], v[52:55], a[8:11], v[4:7]\n"
"buffer_load_dwordx4 v126, s[36:39], s74 offen offset:0 sc1 lds\n"
"v_mfma_f32_16x16x32_bf16 v[20:23], v[52:55], a[12:15], v[20:23]\n"
"s_waitcnt lgkmcnt(2)\n"
"v_mfma_f32_16x16x32_bf16 v[8:11], v[56:59], a[8:11], v[8:11]\n"
"ds_read_b128 v[40:43], v124 offset:41472\n"
"v_mfma_f32_16x16x32_bf16 v[24:27], v[56:59], a[12:15], v[24:27]\n"
"ds_read_b128 v[44:47], v124 offset:41728\n"
"v_mfma_f32_16x16x32_bf16 v[12:15], v[60:63], a[8:11], v[12:15]\n"
"s_mov_b64 exec, s[8:9]\n"
"s_mov_b32 exec_hi, 0\n"
"s_add_u32 s99, s30, 1572864\n"
"buffer_load_dwordx4 v[140:143], v144, s[36:39], s99 offen sc1\n"
"s_mov_b64 exec, -1\n"
"v_mfma_f32_16x16x32_bf16 v[28:31], v[60:63], a[12:15], v[28:31]\n"
"v_and_b32 v186, 15, v127\n"
"v_lshrrev_b32 v187, 4, v127\n"
"s_waitcnt lgkmcnt(2)\n"
"v_mfma_f32_16x16x32_bf16 v[0:3], v[32:35], a[16:19], v[0:3]\n"
"ds_read_b128 v[48:51], v124 offset:45056\n"
"v_xor_b32 v187, v187, v186\n"
"v_lshlrev_b32 v187, 4, v187\n"
"v_mfma_f32_16x16x32_bf16 v[16:19], v[32:35], a[20:23], v[16:19]\n"
"ds_read_b128 v[52:55], v124 offset:45312\n"
"v_lshl_add_u32 v112, v186, 10, v187\n"
"v_xor_b32 v113, 64, v112\n"
"v_mfma_f32_16x16x32_bf16 v[4:7], v[36:39], a[16:19], v[4:7]\n"
"buffer_load_dwordx4 v126, s[36:39], s74 offen offset:1024 sc1 lds\n"
"v_mfma_f32_16x16x32_bf16 v[20:23], v[36:39], a[20:23], v[20:23]\n"
"v_xor_b32 v114, 128, v112\n"
"v_xor_b32 v115, 192, v112\n"
"s_waitcnt lgkmcnt(2)\n"
"v_mfma_f32_16x16x32_bf16 v[8:11], v[40:43], a[16:19], v[8:11]\n"
"ds_read_b128 v[56:59], v124 offset:45568\n"
"s_add_u32 m0, s80, 98304\n"
"s_lshl_b32 s99, s71, 14\n"
"v_mfma_f32_16x16x32_bf16 v[24:27], v[40:43], a[20:23], v[24:27]\n"
"ds_read_b128 v[60:63], v124 offset:45824\n"
"v_add_u32 v178, s99, v112\n"
"v_add_u32 v182, 0x10000, v178\n"
"v_mfma_f32_16x16x32_bf16 v[12:15], v[44:47], a[16:19], v[12:15]\n"
"buffer_load_dwordx4 v122, s[32:35], s73 offen offset:2048 lds\n"
"v_mfma_f32_16x16x32_bf16 v[28:31], v[44:47], a[20:23], v[28:31]\n"
"s_waitcnt vmcnt(13) lgkmcnt(2)\n"
"s_barrier\n"
"v_mfma_f32_16x16x32_bf16 v[0:3], v[48:51], a[24:27], v[0:3]\n"
"ds_read_b128 v[32:35], v124 offset:49152\n"
"s_add_u32 m0, s81, 24576\n"
"v_add_u32 v179, s99, v113\n"
"v_mfma_f32_16x16x32_bf16 v[16:19], v[48:51], a[28:31], v[16:19]\n"
"ds_read_b128 v[36:39], v124 offset:49408\n"
"s_xor_b32 s74, s24, 155648\n"
"v_add_u32 v183, 0x10000, v179\n"
"v_mfma_f32_16x16x32_bf16 v[4:7], v[52:55], a[24:27], v[4:7]\n"
"buffer_load_dwordx4 v126, s[36:39], s74 offen offset:0 sc1 lds\n"
"v_mfma_f32_16x16x32_bf16 v[20:23], v[52:55], a[28:31], v[20:23]\n"
"v_add_u32 v180, s99, v114\n"
"v_add_u32 v184, 0x10000, v180\n"
"s_waitcnt lgkmcnt(2)\n"
"v_mfma_f32_16x16x32_bf16 v[8:11], v[56:59], a[24:27], v[8:11]\n"
"ds_read_b128 v[40:43], v124 offset:49664\n"
"s_add_u32 m0, s80, 98304\n"
"v_add_u32 v181, s99, v115\n"
"v_mfma_f32_16x16x32_bf16 v[24:27], v[56:59], a[28:31], v[24:27]\n"
"ds_read_b128 v[44:47], v124 offset:49920\n"
"v_add_u32 v185, 0x10000, v181\n"
"v_lshlrev_b32 v187, 3, v186\n"
"v_mfma_f32_16x16x32_bf16 v[12:15], v[60:63], a[24:27], v[12:15]\n"
"buffer_load_dwordx4 v123, s[32:35], s73 offen offset:3072 lds\n"
"v_mfma_f32_16x16x32_bf16 v[28:31], v[60:63], a[28:31], v[28:31]\n"
"v_and_b32 v186, 1, v127\n"
"v_mul_u32_u24 v186, 1088, v186\n"
"s_waitcnt lgkmcnt(2)\n"
"v_mfma_f32_16x16x32_bf16 v[0:3], v[32:35], a[32:35], v[0:3]\n"
"ds_read_b128 v[48:51], v124 offset:53248\n"
"s_add_u32 m0, s81, 24576\n"
"v_and_or_b32 v186, v127, 48, v186\n"
"v_mfma_f32_16x16x32_bf16 v[16:19], v[32:35], a[36:39], v[16:19]\n"
"ds_read_b128 v[52:55], v124 offset:53504\n"
"v_add_u32 v186, 157696, v186\n"
"v_lshrrev_b32 v188, 4, v127\n"
"v_mfma_f32_16x16x32_bf16 v[4:7], v[36:39], a[32:35], v[4:7]\n"
"buffer_load_dwordx4 v126, s[36:39], s74 offen offset:1024 sc1 lds\n"
"v_mfma_f32_16x16x32_bf16 v[20:23], v[36:39], a[36:39], v[20:23]\n"
"v_lshl_add_u32 v188, v188, 1, v187\n"
"v_add_u32 v188, 155904, v188\n"
"s_waitcnt lgkmcnt(2)\n"
"v_mfma_f32_16x16x32_bf16 v[8:11], v[40:43], a[32:35], v[8:11]\n"
"ds_read_b128 v[56:59], v124 offset:53760\n"
"s_add_u32 m0, s80, 114688\n"
"s_lshl_b32 s99, s71, 7\n"
"v_mfma_f32_16x16x32_bf16 v[24:27], v[40:43], a[36:39], v[24:27]\n"
"ds_read_b128 v[60:63], v124 offset:54016\n"
"s_xor_b32 s73, s2, 3200\n"
"s_add_u32 s99, s99, 155904\n"
"v_mfma_f32_16x16x32_bf16 v[12:15], v[44:47], a[32:35], v[12:15]\n"
"buffer_load_dwordx4 v120, s[32:35], s73 offen offset:0 lds\n"
"v_mfma_f32_16x16x32_bf16 v[28:31], v[44:47], a[36:39], v[28:31]\n"
"s_waitcnt lgkmcnt(2)\n"
"v_mfma_f32_16x16x32_bf16 v[0:3], v[48:51], a[40:43], v[0:3]\n"
"ds_read_b128 v[32:35], v124 offset:57344\n"
"s_add_u32 m0, s81, 32768\n"
"v_add_u32 v187, s99, v187\n"
"v_mfma_f32_16x16x32_bf16 v[16:19], v[48:51], a[44:47], v[16:19]\n"
"ds_read_b128 v[36:39], v124 offset:57600\n"
"s_xor_b32 s74, s24, 163840\n"
"v_accvgpr_write_b32 a0, v94\n"
"v_mfma_f32_16x16x32_bf16 v[4:7], v[52:55], a[40:43], v[4:7]\n"
"ds_read_b128 v[64:67], v116 offset:0\n"
"buffer_load_dwordx4 v126, s[36:39], s74 offen offset:0 sc1 lds\n"
"v_mfma_f32_16x16x32_bf16 v[20:23], v[52:55], a[44:47], v[20:23]\n"
"ds_read_b128 v[68:71], v116 offset:2048\n"
"v_accvgpr_write_b32 a1, 0\n"
"s_waitcnt lgkmcnt(4)\n"
"v_mfma_f32_16x16x32_bf16 v[8:11], v[56:59], a[40:43], v[8:11]\n"
"ds_read_b128 v[40:43], v124 offset:57856\n"
"s_add_u32 m0, s80, 114688\n"
"v_accvgpr_write_b32 a2, 0\n"
"v_mfma_f32_16x16x32_bf16 v[24:27], v[56:59], a[44:47], v[24:27]\n"
"ds_read_b128 v[44:47], v124 offset:58112\n"
"v_accvgpr_write_b32 a3, 0\n"
"v_mfma_f32_16x16x32_bf16 v[12:15], v[60:63], a[40:43], v[12:15]\n"
"buffer_load_dwordx4 v121, s[32:35], s73 offen offset:1024 lds\n"
"v_mfma_f32_16x16x32_bf16 v[28:31], v[60:63], a[44:47], v[28:31]\n"
"s_waitcnt lgkmcnt(2)\n"
"v_mfma_f32_16x16x32_bf16 v[0:3], v[32:35], v[64:67], v[0:3]\n"
"ds_read_b128 v[48:51], v124 offset:61440\n"
"s_add_u32 m0, s81, 32768\n"
"v_mfma_f32_16x16x32_bf16 v[16:19], v[32:35], v[68:71], v[16:19]\n"
"ds_read_b128 v[52:55], v124 offset:61696\n"
"v_mfma_f32_16x16x32_bf16 v[4:7], v[36:39], v[64:67], v[4:7]\n"
"ds_read_b128 v[72:75], v117 offset:0\n"
"buffer_load_dwordx4 v126, s[36:39], s74 offen offset:1024 sc1 lds\n"
"v_mfma_f32_16x16x32_bf16 v[20:23], v[36:39], v[68:71], v[20:23]\n"
"ds_read_b128 v[76:79], v117 offset:2048\n"
"s_waitcnt lgkmcnt(4)\n"
"v_mfma_f32_16x16x32_bf16 v[8:11], v[40:43], v[64:67], v[8:11]\n"
"ds_read_b128 v[56:59], v124 offset:61952\n"
"s_add_u32 m0, s80, 114688\n"
"v_mfma_f32_16x16x32_bf16 v[24:27], v[40:43], v[68:71], v[24:27]\n"
"ds_read_b128 v[60:63], v124 offset:62208\n"
"v_mfma_f32_16x16x32_bf16 v[12:15], v[44:47], v[64:67], v[12:15]\n"
"buffer_load_dwordx4 v122, s[32:35], s73 offen offset:2048 lds\n"
"v_mfma_f32_16x16x32_bf16 v[28:31], v[44:47], v[68:71], v[28:31]\n"
"s_waitcnt vmcnt(13) lgkmcnt(2)\n"
"s_barrier\n"
"v_mfma_f32_16x16x32_bf16 v[0:3], v[48:51], v[72:75], v[0:3]\n"
"ds_read_b128 v[32:35], v124 offset:0\n"
"s_add_u32 m0, s81, 40960\n"
"v_mfma_f32_16x16x32_bf16 v[16:19], v[48:51], v[76:79], v[16:19]\n"
"ds_read_b128 v[36:39], v124 offset:256\n"
"s_xor_b32 s74, s24, 172032\n"
"v_mfma_f32_16x16x32_bf16 v[4:7], v[52:55], v[72:75], v[4:7]\n"
"buffer_load_dwordx4 v126, s[36:39], s74 offen offset:0 sc1 lds\n"
"v_mfma_f32_16x16x32_bf16 v[20:23], v[52:55], v[76:79], v[20:23]\n"
"s_waitcnt lgkmcnt(2)\n"
"v_mfma_f32_16x16x32_bf16 v[8:11], v[56:59], v[72:75], v[8:11]\n"
"ds_read_b128 v[40:43], v124 offset:512\n"
"s_add_u32 m0, s80, 114688\n"
"v_mfma_f32_16x16x32_bf16 v[24:27], v[56:59], v[76:79], v[24:27]\n"
"ds_read_b128 v[44:47], v124 offset:768\n"
"v_mfma_f32_16x16x32_bf16 v[12:15], v[60:63], v[72:75], v[12:15]\n"
"buffer_load_dwordx4 v123, s[32:35], s73 offen offset:3072 lds\n"
"v_mfma_f32_16x16x32_bf16 v[28:31], v[60:63], v[76:79], v[28:31]\n"
"s_waitcnt lgkmcnt(2)\n"
"v_mfma_f32_16x16x32_bf16 v[0:3], v[32:35], a[48:51], v[0:3]\n"
"ds_read_b128 v[48:51], v124 offset:4096\n"
"s_add_u32 m0, s81, 40960\n"
"v_mfma_f32_16x16x32_bf16 v[16:19], v[32:35], a[52:55], v[16:19]\n"
"ds_read_b128 v[52:55], v124 offset:4352\n"
"v_mfma_f32_16x16x32_bf16 v[4:7], v[36:39], a[48:51], v[4:7]\n"
"buffer_load_dwordx4 v126, s[36:39], s74 offen offset:1024 sc1 lds\n"
"v_mfma_f32_16x16x32_bf16 v[20:23], v[36:39], a[52:55], v[20:23]\n"
"s_waitcnt lgkmcnt(2)\n"
"v_mfma_f32_16x16x32_bf16 v[8:11], v[40:43], a[48:51], v[8:11]\n"
"ds_read_b128 v[56:59], v124 offset:4608\n"
"s_waitcnt vmcnt(13)\n"
"v_xor_b32 v128, s0, v128\n"
"v_mfma_f32_16x16x32_bf16 v[24:27], v[40:43], a[52:55], v[24:27]\n"
"ds_read_b128 v[60:63], v124 offset:4864\n"
"v_xor_b32 v129, s0, v129\n"
"v_xor_b32 v130, s0, v130\n"
"v_mfma_f32_16x16x32_bf16 v[12:15], v[44:47], a[48:51], v[12:15]\n"
"v_xor_b32 v131, s0, v131\n"
"v_xor_b32 v132, s0, v132\n"
"v_xor_b32 v133, s0, v133\n"
"v_mfma_f32_16x16x32_bf16 v[28:31], v[44:47], a[52:55], v[28:31]\n"
"s_waitcnt lgkmcnt(2)\n"
"v_mfma_f32_16x16x32_bf16 v[0:3], v[48:51], a[56:59], v[0:3]\n"
"ds_read_b128 v[32:35], v124 offset:8192\n"
"s_add_u32 m0, s81, 49152\n"
"v_xor_b32 v134, s0, v134\n"
"v_mfma_f32_16x16x32_bf16 v[16:19], v[48:51], a[60:63], v[16:19]\n"
"ds_read_b128 v[36:39], v124 offset:8448\n"
"s_xor_b32 s74, s24, 196608\n"
"v_xor_b32 v135, s0, v135\n"
"v_mfma_f32_16x16x32_bf16 v[4:7], v[52:55], a[56:59], v[4:7]\n"
"buffer_load_dwordx4 v126, s[36:39], s74 offen offset:0 sc1 lds\n"
"v_mfma_f32_16x16x32_bf16 v[20:23], v[52:55], a[60:63], v[20:23]\n"
"v_xor_b32 v136, s0, v136\n"
"v_xor_b32 v137, s0, v137\n"
"s_waitcnt lgkmcnt(2)\n"
"v_mfma_f32_16x16x32_bf16 v[8:11], v[56:59], a[56:59], v[8:11]\n"
"ds_read_b128 v[40:43], v124 offset:8704\n"
"v_xor_b32 v138, s0, v138\n"
"v_xor_b32 v139, s0, v139\n"
"v_mfma_f32_16x16x32_bf16 v[24:27], v[56:59], a[60:63], v[24:27]\n"
"ds_read_b128 v[44:47], v124 offset:8960\n"
"v_xor_b32 v140, s0, v140\n"
"v_xor_b32 v141, s0, v141\n"
"v_mfma_f32_16x16x32_bf16 v[12:15], v[60:63], a[56:59], v[12:15]\n"
"v_xor_b32 v142, s0, v142\n"
"v_xor_b32 v143, s0, v143\n"
"v_or_b32 v146, v128, v129\n"
"v_mfma_f32_16x16x32_bf16 v[28:31], v[60:63], a[60:63], v[28:31]\n"
"v_or3_b32 v146, v146, v130, v131\n"
"v_or3_b32 v146, v146, v132, v133\n"
"s_waitcnt lgkmcnt(2)\n"
"v_mfma_f32_16x16x32_bf16 v[0:3], v[32:35], a[64:67], v[0:3]\n"
"ds_read_b128 v[48:51], v124 offset:12288\n"
"v_or3_b32 v146, v146, v134, v135\n"
"v_or3_b32 v146, v146, v136, v137\n"
"v_mfma_f32_16x16x32_bf16 v[16:19], v[32:35], a[68:71], v[16:19]\n"
"ds_read_b128 v[52:55], v124 offset:12544\n"
"v_or3_b32 v146, v146, v138, v139\n"
"v_or3_b32 v146, v146, v140, v141\n"
"v_mfma_f32_16x16x32_bf16 v[4:7], v[36:39], a[64:67], v[4:7]\n"
"buffer_load_dwordx4 v126, s[36:39], s74 offen offset:1024 sc1 lds\n"
"v_mfma_f32_16x16x32_bf16 v[20:23], v[36:39], a[68:71], v[20:23]\n"
"v_or3_b32 v146, v146, v142, v143\n"
"v_and_b32 v146, 0xff, v146\n"
"s_waitcnt lgkmcnt(2)\n"
"v_mfma_f32_16x16x32_bf16 v[8:11], v[40:43], a[64:67], v[8:11]\n"
"ds_read_b128 v[56:59], v124 offset:12800\n"
"v_bitop3_b32 v146, v146, v151, s68 bitop3:0xf6\n"
"v_cmp_ne_u32 vcc, 0, v146\n"
"v_mfma_f32_16x16x32_bf16 v[24:27], v[40:43], a[68:71], v[24:27]\n"
"ds_read_b128 v[60:63], v124 offset:13056\n"
"s_and_b64 vcc, vcc, s[92:93]\n"
"s_cbranch_vccnz S20_%=\n"
"B21_%=:\n"
"v_mfma_f32_16x16x32_bf16 v[12:15], v[44:47], a[64:67], v[12:15]\n"
"v_add_f32 v128, v128, v132\n"
"v_add_f32 v129, v129, v133\n"
"v_add_f32 v130, v130, v134\n"
"v_mfma_f32_16x16x32_bf16 v[28:31], v[44:47], a[68:71], v[28:31]\n"
"s_waitcnt vmcnt(10) lgkmcnt(2)\n"
"s_barrier\n"
"v_mfma_f32_16x16x32_bf16 v[0:3], v[48:51], a[72:75], v[0:3]\n"
"ds_read_b128 v[32:35], v124 offset:16384\n"
"s_add_u32 m0, s81, 57344\n"
"v_add_f32 v131, v131, v135\n"
"v_mfma_f32_16x16x32_bf16 v[16:19], v[48:51], a[76:79], v[16:19]\n"
"ds_read_b128 v[36:39], v124 offset:16640\n"
"s_xor_b32 s74, s24, 180224\n"
"v_add_f32 v136, v136, v140\n"
"v_mfma_f32_16x16x32_bf16 v[4:7], v[52:55], a[72:75], v[4:7]\n"
"ds_read_b128 v[64:67], v116 offset:16384\n"
"buffer_load_dwordx4 v126, s[36:39], s74 offen offset:0 sc1 lds\n"
"v_mfma_f32_16x16x32_bf16 v[20:23], v[52:55], a[76:79], v[20:23]\n"
"ds_read_b128 v[68:71], v116 offset:18432\n"
"v_add_f32 v137, v137, v141\n"
"s_waitcnt lgkmcnt(4)\n"
"v_mfma_f32_16x16x32_bf16 v[8:11], v[56:59], a[72:75], v[8:11]\n"
"ds_read_b128 v[40:43], v124 offset:16896\n"
"s_add_u32 m0, s80, 65536\n"
"v_add_f32 v138, v138, v142\n"
"v_mfma_f32_16x16x32_bf16 v[24:27], v[56:59], a[76:79], v[24:27]\n"
"ds_read_b128 v[44:47], v124 offset:17152\n"
"s_add_u32 s73, s1, 0\n"
"v_add_f32 v139, v139, v143\n"
"v_mfma_f32_16x16x32_bf16 v[12:15], v[60:63], a[72:75], v[12:15]\n"
"buffer_load_dwordx4 v89, s[44:47], s73 offen offset:0 lds\n"
"v_mfma_f32_16x16x32_bf16 v[28:31], v[60:63], a[76:79], v[28:31]\n"
"v_and_b32 v148, 0xffff0000, v149\n"
"v_add_f32 v128, v128, v136\n"
"s_waitcnt lgkmcnt(2)\n"
"v_mfma_f32_16x16x32_bf16 v[0:3], v[32:35], v[64:67], v[0:3]\n"
"ds_read_b128 v[48:51], v124 offset:20480\n"
"s_add_u32 m0, s81, 57344\n"
"v_add_f32 v129, v129, v137\n"
"v_mfma_f32_16x16x32_bf16 v[16:19], v[32:35], v[68:71], v[16:19]\n"
"ds_read_b128 v[52:55], v124 offset:20736\n"
"v_add_f32 v130, v130, v138\n"
"v_add_f32 v131, v131, v139\n"
"v_mfma_f32_16x16x32_bf16 v[4:7], v[36:39], v[64:67], v[4:7]\n"
"ds_read_b128 v[72:75], v117 offset:16384\n"
"buffer_load_dwordx4 v126, s[36:39], s74 offen offset:1024 sc1 lds\n"
"v_mfma_f32_16x16x32_bf16 v[20:23], v[36:39], v[68:71], v[20:23]\n"
"ds_read_b128 v[76:79], v117 offset:18432\n"
"s_mov_b32 exec_hi, 0\n"
"v_lshlrev_b32 v148, 16, v149\n"
"s_mov_b64 exec, -1\n"
"s_waitcnt lgkmcnt(4)\n"
"v_mfma_f32_16x16x32_bf16 v[8:11], v[40:43], v[64:67], v[8:11]\n"
"ds_read_b128 v[56:59], v124 offset:20992\n"
"s_add_u32 m0, s80, 65536\n"
"v_permlane32_swap_b32 v128, v129\n"
"v_mfma_f32_16x16x32_bf16 v[24:27], v[40:43], v[68:71], v[24:27]\n"
"ds_read_b128 v[60:63], v124 offset:21248\n"
"v_permlane32_swap_b32 v130, v131\n"
"v_add_f32 v128, v128, v129\n"
"v_mfma_f32_16x16x32_bf16 v[12:15], v[44:47], v[64:67], v[12:15]\n"
"buffer_load_dwordx4 v90, s[44:47], s73 offen offset:1024 lds\n"
"v_mfma_f32_16x16x32_bf16 v[28:31], v[44:47], v[68:71], v[28:31]\n"
"s_waitcnt lgkmcnt(2)\n"
"v_mfma_f32_16x16x32_bf16 v[0:3], v[48:51], v[72:75], v[0:3]\n"
"ds_read_b128 v[32:35], v124 offset:24576\n"
"s_add_u32 m0, s81, 0\n"
"v_add_f32 v130, v130, v131\n"
"v_mfma_f32_16x16x32_bf16 v[16:19], v[48:51], v[76:79], v[16:19]\n"
"ds_read_b128 v[36:39], v124 offset:24832\n"
"s_xor_b32 s74, s24, 188416\n"
"v_permlane16_swap_b32 v128, v130\n"
"v_mfma_f32_16x16x32_bf16 v[4:7], v[52:55], v[72:75], v[4:7]\n"
"buffer_load_dwordx4 v126, s[36:39], s74 offen offset:0 sc1 lds\n"
"v_mfma_f32_16x16x32_bf16 v[20:23], v[52:55], v[76:79], v[20:23]\n"
"v_add_f32 v128, v128, v130\n"
"s_nop 1\n"
"v_add_f32_dpp v128, v128, v128 row_ror:8 row_mask:0xf bank_mask:0xf\n"
"s_waitcnt lgkmcnt(2)\n"
"v_mfma_f32_16x16x32_bf16 v[8:11], v[56:59], v[72:75], v[8:11]\n"
"ds_read_b128 v[40:43], v124 offset:25088\n"
"s_add_u32 m0, s80, 65536\n"
"v_add_f32_dpp v128, v128, v128 row_ror:4 row_mask:0xf bank_mask:0xf\n"
"v_mfma_f32_16x16x32_bf16 v[24:27], v[56:59], v[76:79], v[24:27]\n"
"ds_read_b128 v[44:47], v124 offset:25344\n"
"v_add_f32_dpp v128, v128, v128 row_ror:2 row_mask:0xf bank_mask:0xf\n"
"s_nop 1\n"
"v_add_f32_dpp v128, v128, v128 row_ror:1 row_mask:0xf bank_mask:0xf\n"
"v_mfma_f32_16x16x32_bf16 v[12:15], v[60:63], v[72:75], v[12:15]\n"
"buffer_load_dwordx4 v91, s[44:47], s73 offen offset:2048 lds\n"
"v_mfma_f32_16x16x32_bf16 v[28:31], v[60:63], v[76:79], v[28:31]\n"
"v_fma_f32 v128, v128, v147, v148\n"
"v_and_b32 v128, 0xffffff00, v128\n"
"s_waitcnt lgkmcnt(2)\n"
"v_mfma_f32_16x16x32_bf16 v[0:3], v[32:35], a[80:83], v[0:3]\n"
"ds_read_b128 v[48:51], v124 offset:28672\n"
"s_add_u32 m0, s81, 0\n"
"v_or_b32 v128, s0, v128\n"
"v_mfma_f32_16x16x32_bf16 v[16:19], v[32:35], a[84:87], v[16:19]\n"
"ds_read_b128 v[52:55], v124 offset:28928\n"
"v_lshrrev_b32 v132, 3, v127\n"
"v_lshrrev_b32 v133, 1, v127\n"
"v_mfma_f32_16x16x32_bf16 v[4:7], v[36:39], a[80:83], v[4:7]\n"
"buffer_load_dwordx4 v126, s[36:39], s74 offen offset:1024 sc1 lds\n"
"v_mfma_f32_16x16x32_bf16 v[20:23], v[36:39], a[84:87], v[20:23]\n"
"v_and_b32 v132, 4, v132\n"
"v_and_or_b32 v132, v133, 8, v132\n"
"s_waitcnt lgkmcnt(2)\n"
"v_mfma_f32_16x16x32_bf16 v[8:11], v[40:43], a[80:83], v[8:11]\n"
"ds_read_b128 v[56:59], v124 offset:29184\n"
"v_mfma_f32_16x16x32_bf16 v[24:27], v[40:43], a[84:87], v[24:27]\n"
"ds_read_b128 v[60:63], v124 offset:29440\n"
"v_mfma_f32_16x16x32_bf16 v[12:15], v[44:47], a[80:83], v[12:15]\n"
"s_mov_b32 exec_lo, 0x10001\n"
"s_mov_b32 exec_hi, 0x10001\n"
"buffer_store_dword v128, v132, s[36:39], s60 offen sc1\n"
"s_mov_b64 exec, -1\n"
"v_mfma_f32_16x16x32_bf16 v[28:31], v[44:47], a[84:87], v[28:31]\n"
"s_waitcnt vmcnt(10) lgkmcnt(2)\n"
"s_barrier\n"
"v_mfma_f32_16x16x32_bf16 v[0:3], v[48:51], a[88:91], v[0:3]\n"
"ds_read_b128 v[32:35], v124 offset:32768\n"
"s_add_u32 m0, s81, 8192\n"
"v_mfma_f32_16x16x32_bf16 v[16:19], v[48:51], a[92:95], v[16:19]\n"
"ds_read_b128 v[36:39], v124 offset:33024\n"
"s_xor_b32 s74, s24, 212992\n"
"v_mfma_f32_16x16x32_bf16 v[4:7], v[52:55], a[88:91], v[4:7]\n"
"buffer_load_dwordx4 v126, s[36:39], s74 offen offset:0 sc1 lds\n"
"v_mfma_f32_16x16x32_bf16 v[20:23], v[52:55], a[92:95], v[20:23]\n"
"s_waitcnt lgkmcnt(2)\n"
"v_mfma_f32_16x16x32_bf16 v[8:11], v[56:59], a[88:91], v[8:11]\n"
"ds_read_b128 v[40:43], v124 offset:33280\n"
"s_add_u32 m0, s80, 65536\n"
"v_mfma_f32_16x16x32_bf16 v[24:27], v[56:59], a[92:95], v[24:27]\n"
"ds_read_b128 v[44:47], v124 offset:33536\n"
"v_mfma_f32_16x16x32_bf16 v[12:15], v[60:63], a[88:91], v[12:15]\n"
"buffer_load_dwordx4 v92, s[44:47], s73 offen offset:3072 lds\n"
"v_mfma_f32_16x16x32_bf16 v[28:31], v[60:63], a[92:95], v[28:31]\n"
"s_waitcnt lgkmcnt(2)\n"
"v_mfma_f32_16x16x32_bf16 v[0:3], v[32:35], a[96:99], v[0:3]\n"
"ds_read_b128 v[48:51], v124 offset:36864\n"
"s_add_u32 m0, s81, 8192\n"
"v_mfma_f32_16x16x32_bf16 v[16:19], v[32:35], a[100:103], v[16:19]\n"
"ds_read_b128 v[52:55], v124 offset:37120\n"
"v_mfma_f32_16x16x32_bf16 v[4:7], v[36:39], a[96:99], v[4:7]\n"
"buffer_load_dwordx4 v126, s[36:39], s74 offen offset:1024 sc1 lds\n"
"v_mfma_f32_16x16x32_bf16 v[20:23], v[36:39], a[100:103], v[20:23]\n"
"s_waitcnt lgkmcnt(2)\n"
"v_mfma_f32_16x16x32_bf16 v[8:11], v[40:43], a[96:99], v[8:11]\n"
"ds_read_b128 v[56:59], v124 offset:37376\n"
"s_add_u32 m0, s80, 81920\n"
"v_mfma_f32_16x16x32_bf16 v[24:27], v[40:43], a[100:103], v[24:27]\n"
"ds_read_b128 v[60:63], v124 offset:37632\n"
"s_add_u32 s73, s1, 4096\n"
"v_mfma_f32_16x16x32_bf16 v[12:15], v[44:47], a[96:99], v[12:15]\n"
"buffer_load_dwordx4 v89, s[44:47], s73 offen offset:0 lds\n"
"v_mfma_f32_16x16x32_bf16 v[28:31], v[44:47], a[100:103], v[28:31]\n"
"s_waitcnt lgkmcnt(2)\n"
"v_mfma_f32_16x16x32_bf16 v[0:3], v[48:51], a[104:107], v[0:3]\n"
"ds_read_b128 v[32:35], v124 offset:40960\n"
"s_add_u32 m0, s81, 16384\n"
"v_mfma_f32_16x16x32_bf16 v[16:19], v[48:51], a[108:111], v[16:19]\n"
"ds_read_b128 v[36:39], v124 offset:41216\n"
"s_xor_b32 s74, s24, 204800\n"
"v_mfma_f32_16x16x32_bf16 v[4:7], v[52:55], a[104:107], v[4:7]\n"
"buffer_load_dwordx4 v126, s[36:39], s74 offen offset:0 sc1 lds\n"
"v_mfma_f32_16x16x32_bf16 v[20:23], v[52:55], a[108:111], v[20:23]\n"
"s_waitcnt lgkmcnt(2)\n"
"v_mfma_f32_16x16x32_bf16 v[8:11], v[56:59], a[104:107], v[8:11]\n"
"ds_read_b128 v[40:43], v124 offset:41472\n"
"s_add_u32 m0, s80, 81920\n"
"v_mfma_f32_16x16x32_bf16 v[24:27], v[56:59], a[108:111], v[24:27]\n"
"ds_read_b128 v[44:47], v124 offset:41728\n"
"v_mfma_f32_16x16x32_bf16 v[12:15], v[60:63], a[104:107], v[12:15]\n"
"buffer_load_dwordx4 v90, s[44:47], s73 offen offset:1024 lds\n"
"v_mfma_f32_16x16x32_bf16 v[28:31], v[60:63], a[108:111], v[28:31]\n"
"s_waitcnt lgkmcnt(2)\n"
"v_mfma_f32_16x16x32_bf16 v[0:3], v[32:35], a[112:115], v[0:3]\n"
"ds_read_b128 v[48:51], v124 offset:45056\n"
"s_add_u32 m0, s81, 16384\n"
"v_mfma_f32_16x16x32_bf16 v[16:19], v[32:35], a[116:119], v[16:19]\n"
"ds_read_b128 v[52:55], v124 offset:45312\n"
"v_mfma_f32_16x16x32_bf16 v[4:7], v[36:39], a[112:115], v[4:7]\n"
"buffer_load_dwordx4 v126, s[36:39], s74 offen offset:1024 sc1 lds\n"
"v_mfma_f32_16x16x32_bf16 v[20:23], v[36:39], a[116:119], v[20:23]\n"
"s_waitcnt lgkmcnt(2)\n"
"v_mfma_f32_16x16x32_bf16 v[8:11], v[40:43], a[112:115], v[8:11]\n"
"ds_read_b128 v[56:59], v124 offset:45568\n"
"s_add_u32 m0, s80, 81920\n"
"v_mfma_f32_16x16x32_bf16 v[24:27], v[40:43], a[116:119], v[24:27]\n"
"ds_read_b128 v[60:63], v124 offset:45824\n"
"v_mfma_f32_16x16x32_bf16 v[12:15], v[44:47], a[112:115], v[12:15]\n"
"buffer_load_dwordx4 v91, s[44:47], s73 offen offset:2048 lds\n"
"v_mfma_f32_16x16x32_bf16 v[28:31], v[44:47], a[116:119], v[28:31]\n"
"s_waitcnt vmcnt(13) lgkmcnt(2)\n"
"s_barrier\n"
"v_mfma_f32_16x16x32_bf16 v[0:3], v[48:51], a[120:123], v[0:3]\n"
"ds_read_b128 v[32:35], v124 offset:49152\n"
"s_add_u32 m0, s81, 24576\n"
"v_mfma_f32_16x16x32_bf16 v[16:19], v[48:51], a[124:127], v[16:19]\n"
"ds_read_b128 v[36:39], v124 offset:49408\n"
"s_xor_b32 s74, s24, 221184\n"
"v_mfma_f32_16x16x32_bf16 v[4:7], v[52:55], a[120:123], v[4:7]\n"
"ds_read_b128 v[64:67], v116 offset:32768\n"
"buffer_load_dwordx4 v126, s[36:39], s74 offen offset:0 sc1 lds\n"
"v_mfma_f32_16x16x32_bf16 v[20:23], v[52:55], a[124:127], v[20:23]\n"
"ds_read_b128 v[68:71], v116 offset:34816\n"
"s_waitcnt lgkmcnt(4)\n"
"v_mfma_f32_16x16x32_bf16 v[8:11], v[56:59], a[120:123], v[8:11]\n"
"ds_read_b128 v[40:43], v124 offset:49664\n"
"s_add_u32 m0, s80, 81920\n"
"v_mfma_f32_16x16x32_bf16 v[24:27], v[56:59], a[124:127], v[24:27]\n"
"ds_read_b128 v[44:47], v124 offset:49920\n"
"v_mfma_f32_16x16x32_bf16 v[12:15], v[60:63], a[120:123], v[12:15]\n"
"buffer_load_dwordx4 v92, s[44:47], s73 offen offset:3072 lds\n"
"v_mfma_f32_16x16x32_bf16 v[28:31], v[60:63], a[124:127], v[28:31]\n"
"s_waitcnt lgkmcnt(2)\n"
"v_mfma_f32_16x16x32_bf16 v[0:3], v[32:35], v[64:67], v[0:3]\n"
"ds_read_b128 v[48:51], v124 offset:53248\n"
"s_add_u32 m0, s81, 24576\n"
"s_lshl_b32 s99, s84, 2\n"
"v_mfma_f32_16x16x32_bf16 v[16:19], v[32:35], v[68:71], v[16:19]\n"
"ds_read_b128 v[52:55], v124 offset:53504\n"
"s_add_u32 s99, s99, s71\n"
"s_mul_i32 s99, s99, 128\n"
"v_mfma_f32_16x16x32_bf16 v[4:7], v[36:39], v[64:67], v[4:7]\n"
"ds_read_b128 v[72:75], v117 offset:32768\n"
"buffer_load_dwordx4 v126, s[36:39], s74 offen offset:1024 sc1 lds\n"
"v_mfma_f32_16x16x32_bf16 v[20:23], v[36:39], v[68:71], v[20:23]\n"
"ds_read_b128 v[76:79], v117 offset:34816\n"
"v_lshl_add_u32 v160, v127, 7, s99\n"
"s_waitcnt lgkmcnt(4)\n"
"v_mfma_f32_16x16x32_bf16 v[8:11], v[40:43], v[64:67], v[8:11]\n"
"ds_read_b128 v[56:59], v124 offset:53760\n"
"v_min_u32 v160, END_%=-PC3_%=, v160\n"
"v_mfma_f32_16x16x32_bf16 v[24:27], v[40:43], v[68:71], v[24:27]\n"
"ds_read_b128 v[60:63], v124 offset:54016\n"
"v_mfma_f32_16x16x32_bf16 v[12:15], v[44:47], v[64:67], v[12:15]\n"
"s_getpc_b64 s[26:27]\n"
"PC3_%=:\n"
"s_mov_b64 exec, 1\n"
"global_load_dword v161, v160, s[26:27]\n"
"s_mov_b64 exec, -1\n"
"v_mfma_f32_16x16x32_bf16 v[28:31], v[44:47], v[68:71], v[28:31]\n"
"s_waitcnt lgkmcnt(2)\n"
"v_mfma_f32_16x16x32_bf16 v[0:3], v[48:51], v[72:75], v[0:3]\n"
"ds_read_b128 v[32:35], v124 offset:57344\n"
"s_add_u32 m0, s81, 32768\n"
"v_mfma_f32_16x16x32_bf16 v[16:19], v[48:51], v[76:79], v[16:19]\n"
"ds_read_b128 v[36:39], v124 offset:57600\n"
"s_xor_b32 s74, s24, 229376\n"
"v_mfma_f32_16x16x32_bf16 v[4:7], v[52:55], v[72:75], v[4:7]\n"
"buffer_load_dwordx4 v126, s[36:39], s74 offen offset:0 sc1 lds\n"
"v_mfma_f32_16x16x32_bf16 v[20:23], v[52:55], v[76:79], v[20:23]\n"
"s_waitcnt lgkmcnt(2)\n"
"v_mfma_f32_16x16x32_bf16 v[8:11], v[56:59], v[72:75], v[8:11]\n"
"ds_read_b128 v[40:43], v124 offset:57856\n"
"v_mfma_f32_16x16x32_bf16 v[24:27], v[56:59], v[76:79], v[24:27]\n"
"ds_read_b128 v[44:47], v124 offset:58112\n"
"v_mfma_f32_16x16x32_bf16 v[12:15], v[60:63], v[72:75], v[12:15]\n"
"v_mfma_f32_16x16x32_bf16 v[28:31], v[60:63], v[76:79], v[28:31]\n"
"s_waitcnt lgkmcnt(2)\n"
"v_mfma_f32_16x16x32_bf16 v[0:3], v[32:35], a[128:131], v[0:3]\n"
"ds_read_b128 v[48:51], v124 offset:61440\n"
"v_mfma_f32_16x16x32_bf16 v[16:19], v[32:35], a[132:135], v[16:19]\n"
"ds_read_b128 v[52:55], v124 offset:61696\n"
"v_mfma_f32_16x16x32_bf16 v[4:7], v[36:39], a[128:131], v[4:7]\n"
"buffer_load_dwordx4 v126, s[36:39], s74 offen offset:1024 sc1 lds\n"
"v_mfma_f32_16x16x32_bf16 v[20:23], v[36:39], a[132:135], v[20:23]\n"
"s_waitcnt lgkmcnt(2)\n"
"v_mfma_f32_16x16x32_bf16 v[8:11], v[40:43], a[128:131], v[8:11]\n"
"ds_read_b128 v[56:59], v124 offset:61952\n"
"v_mfma_f32_16x16x32_bf16 v[24:27], v[40:43], a[132:135], v[24:27]\n"
"ds_read_b128 v[60:63], v124 offset:62208\n"
"v_mfma_f32_16x16x32_bf16 v[12:15], v[44:47], a[128:131], v[12:15]\n"
"v_mfma_f32_16x16x32_bf16 v[28:31], v[44:47], a[132:135], v[28:31]\n"
"s_waitcnt vmcnt(11) lgkmcnt(2)\n"
"s_barrier\n"
"v_mfma_f32_16x16x32_bf16 v[0:3], v[48:51], a[136:139], v[0:3]\n"
"ds_read_b128 v[32:35], v124 offset:0\n"
"s_add_u32 m0, s81, 40960\n"
"v_mfma_f32_16x16x32_bf16 v[16:19], v[48:51], a[140:143], v[16:19]\n"
"ds_read_b128 v[36:39], v124 offset:256\n"
"s_xor_b32 s74, s24, 237568\n"
"v_mfma_f32_16x16x32_bf16 v[4:7], v[52:55], a[136:139], v[4:7]\n"
"buffer_load_dwordx4 v126, s[36:39], s74 offen offset:0 sc1 lds\n"
"v_mfma_f32_16x16x32_bf16 v[20:23], v[52:55], a[140:143], v[20:23]\n"
"s_waitcnt lgkmcnt(2)\n"
"v_mfma_f32_16x16x32_bf16 v[8:11], v[56:59], a[136:139], v[8:11]\n"
"ds_read_b128 v[40:43], v124 offset:512\n"
"s_add_u32 m0, s80, 98304\n"
"v_mfma_f32_16x16x32_bf16 v[24:27], v[56:59], a[140:143], v[24:27]\n"
"ds_read_b128 v[44:47], v124 offset:768\n"
"s_add_u32 s73, s1, 32768\n"
"v_mfma_f32_16x16x32_bf16 v[12:15], v[60:63], a[136:139], v[12:15]\n"
"buffer_load_dwordx4 v89, s[44:47], s73 offen offset:0 lds\n"
"v_mfma_f32_16x16x32_bf16 v[28:31], v[60:63], a[140:143], v[28:31]\n"
"s_waitcnt lgkmcnt(2)\n"
"v_mfma_f32_16x16x32_bf16 v[0:3], v[32:35], a[144:147], v[0:3]\n"
"ds_read_b128 v[48:51], v124 offset:4096\n"
"s_add_u32 m0, s81, 40960\n"
"v_mfma_f32_16x16x32_bf16 v[16:19], v[32:35], a[148:151], v[16:19]\n"
"ds_read_b128 v[52:55], v124 offset:4352\n"
"v_mfma_f32_16x16x32_bf16 v[4:7], v[36:39], a[144:147], v[4:7]\n"
"buffer_load_dwordx4 v126, s[36:39], s74 offen offset:1024 sc1 lds\n"
"v_mfma_f32_16x16x32_bf16 v[20:23], v[36:39], a[148:151], v[20:23]\n"
"s_waitcnt lgkmcnt(2)\n"
"v_mfma_f32_16x16x32_bf16 v[8:11], v[40:43], a[144:147], v[8:11]\n"
"ds_read_b128 v[56:59], v124 offset:4608\n"
"s_add_u32 m0, s80, 98304\n"
"v_mfma_f32_16x16x32_bf16 v[24:27], v[40:43], a[148:151], v[24:27]\n"
"ds_read_b128 v[60:63], v124 offset:4864\n"
"v_mfma_f32_16x16x32_bf16 v[12:15], v[44:47], a[144:147], v[12:15]\n"
"buffer_load_dwordx4 v90, s[44:47], s73 offen offset:1024 lds\n"
"v_mfma_f32_16x16x32_bf16 v[28:31], v[44:47], a[148:151], v[28:31]\n"
"s_waitcnt lgkmcnt(2)\n"
"s_barrier\n"
"v_mfma_f32_16x16x32_bf16 v[0:3], v[48:51], a[152:155], v[0:3]\n"
"ds_read_b128 v[32:35], v124 offset:8192\n"
"s_add_u32 m0, s81, 131072\n"
"v_mfma_f32_16x16x32_bf16 v[16:19], v[48:51], a[156:159], v[16:19]\n"
"ds_read_b128 v[36:39], v124 offset:8448\n"
"s_xor_b32 s74, s24, 245760\n"
"v_mfma_f32_16x16x32_bf16 v[4:7], v[52:55], a[152:155], v[4:7]\n"
"buffer_load_dwordx4 v126, s[36:39], s74 offen offset:0 sc1 lds\n"
"v_mfma_f32_16x16x32_bf16 v[20:23], v[52:55], a[156:159], v[20:23]\n"
"s_waitcnt lgkmcnt(2)\n"
"v_mfma_f32_16x16x32_bf16 v[8:11], v[56:59], a[152:155], v[8:11]\n"
"ds_read_b128 v[40:43], v124 offset:8704\n"
"s_add_u32 m0, s80, 98304\n"
"v_mfma_f32_16x16x32_bf16 v[24:27], v[56:59], a[156:159], v[24:27]\n"
"ds_read_b128 v[44:47], v124 offset:8960\n"
"v_mfma_f32_16x16x32_bf16 v[12:15], v[60:63], a[152:155], v[12:15]\n"
"buffer_load_dwordx4 v91, s[44:47], s73 offen offset:2048 lds\n"
"v_mfma_f32_16x16x32_bf16 v[28:31], v[60:63], a[156:159], v[28:31]\n"
"s_waitcnt lgkmcnt(2)\n"
"v_mfma_f32_16x16x32_bf16 v[0:3], v[32:35], a[160:163], v[0:3]\n"
"ds_read_b128 v[48:51], v124 offset:12288\n"
"s_add_u32 m0, s81, 131072\n"
"v_mfma_f32_16x16x32_bf16 v[16:19], v[32:35], a[164:167], v[16:19]\n"
"ds_read_b128 v[52:55], v124 offset:12544\n"
"v_mfma_f32_16x16x32_bf16 v[4:7], v[36:39], a[160:163], v[4:7]\n"
"buffer_load_dwordx4 v126, s[36:39], s74 offen offset:1024 sc1 lds\n"
"v_mfma_f32_16x16x32_bf16 v[20:23], v[36:39], a[164:167], v[20:23]\n"
"s_waitcnt lgkmcnt(2)\n"
"v_mfma_f32_16x16x32_bf16 v[8:11], v[40:43], a[160:163], v[8:11]\n"
"ds_read_b128 v[56:59], v124 offset:12800\n"
"s_add_u32 m0, s80, 98304\n"
"v_mfma_f32_16x16x32_bf16 v[24:27], v[40:43], a[164:167], v[24:27]\n"
"ds_read_b128 v[60:63], v124 offset:13056\n"
"v_mfma_f32_16x16x32_bf16 v[12:15], v[44:47], a[160:163], v[12:15]\n"
"buffer_load_dwordx4 v92, s[44:47], s73 offen offset:3072 lds\n"
"v_mfma_f32_16x16x32_bf16 v[28:31], v[44:47], a[164:167], v[28:31]\n"
"s_waitcnt vmcnt(15) lgkmcnt(2)\n"
"s_barrier\n"
"v_mfma_f32_16x16x32_bf16 v[0:3], v[48:51], a[168:171], v[0:3]\n"
"ds_read_b128 v[32:35], v124 offset:16384\n"
"s_add_u32 m0, s81, 139264\n"
"v_mfma_f32_16x16x32_bf16 v[16:19], v[48:51], a[172:175], v[16:19]\n"
"ds_read_b128 v[36:39], v124 offset:16640\n"
"s_xor_b32 s74, s24, 253952\n"
"v_mfma_f32_16x16x32_bf16 v[4:7], v[52:55], a[168:171], v[4:7]\n"
"ds_read_b128 v[64:67], v116 offset:49152\n"
"buffer_load_dwordx4 v126, s[36:39], s74 offen offset:0 sc1 lds\n"
"v_mfma_f32_16x16x32_bf16 v[20:23], v[52:55], a[172:175], v[20:23]\n"
"ds_read_b128 v[68:71], v116 offset:51200\n"
"s_waitcnt lgkmcnt(4)\n"
"v_mfma_f32_16x16x32_bf16 v[8:11], v[56:59], a[168:171], v[8:11]\n"
"ds_read_b128 v[40:43], v124 offset:16896\n"
"s_add_u32 m0, s80, 49152\n"
"v_mfma_f32_16x16x32_bf16 v[24:27], v[56:59], a[172:175], v[24:27]\n"
"ds_read_b128 v[44:47], v124 offset:17152\n"
"s_add_u32 s73, s1, 36864\n"
"v_mfma_f32_16x16x32_bf16 v[12:15], v[60:63], a[168:171], v[12:15]\n"
"buffer_load_dwordx4 v89, s[40:43], s73 offen offset:0 lds\n"
"v_mfma_f32_16x16x32_bf16 v[28:31], v[60:63], a[172:175], v[28:31]\n"
"s_waitcnt lgkmcnt(2)\n"
"v_mfma_f32_16x16x32_bf16 v[0:3], v[32:35], v[64:67], v[0:3]\n"
"ds_read_b128 v[48:51], v124 offset:20480\n"
"s_add_u32 m0, s81, 139264\n"
"v_mfma_f32_16x16x32_bf16 v[16:19], v[32:35], v[68:71], v[16:19]\n"
"ds_read_b128 v[52:55], v124 offset:20736\n"
"v_mfma_f32_16x16x32_bf16 v[4:7], v[36:39], v[64:67], v[4:7]\n"
"ds_read_b128 v[72:75], v117 offset:49152\n"
"buffer_load_dwordx4 v126, s[36:39], s74 offen offset:1024 sc1 lds\n"
"v_mfma_f32_16x16x32_bf16 v[20:23], v[36:39], v[68:71], v[20:23]\n"
"ds_read_b128 v[76:79], v117 offset:51200\n"
"s_waitcnt lgkmcnt(4)\n"
"v_mfma_f32_16x16x32_bf16 v[8:11], v[40:43], v[64:67], v[8:11]\n"
"ds_read_b128 v[56:59], v124 offset:20992\n"
"s_add_u32 m0, s80, 49152\n"
"v_mfma_f32_16x16x32_bf16 v[24:27], v[40:43], v[68:71], v[24:27]\n"
"ds_read_b128 v[60:63], v124 offset:21248\n"
"v_mfma_f32_16x16x32_bf16 v[12:15], v[44:47], v[64:67], v[12:15]\n"
"buffer_load_dwordx4 v90, s[40:43], s73 offen offset:1024 lds\n"
"v_mfma_f32_16x16x32_bf16 v[28:31], v[44:47], v[68:71], v[28:31]\n"
"s_waitcnt vmcnt(15) lgkmcnt(2)\n"
"s_barrier\n"
"v_mfma_f32_16x16x32_bf16 v[0:3], v[48:51], v[72:75], v[0:3]\n"
"ds_read_b128 v[32:35], v124 offset:24576\n"
"v_mfma_f32_16x16x32_bf16 v[16:19], v[48:51], v[76:79], v[16:19]\n"
"ds_read_b128 v[36:39], v124 offset:24832\n"
"v_mfma_f32_16x16x32_bf16 v[4:7], v[52:55], v[72:75], v[4:7]\n"
"buffer_load_dwordx4 v91, s[40:43], s73 offen offset:2048 lds\n"
"v_mfma_f32_16x16x32_bf16 v[20:23], v[52:55], v[76:79], v[20:23]\n"
"s_waitcnt lgkmcnt(2)\n"
"v_mfma_f32_16x16x32_bf16 v[8:11], v[56:59], v[72:75], v[8:11]\n"
"ds_read_b128 v[40:43], v124 offset:25088\n"
"v_mfma_f32_16x16x32_bf16 v[24:27], v[56:59], v[76:79], v[24:27]\n"
"ds_read_b128 v[44:47], v124 offset:25344\n"
"v_mfma_f32_16x16x32_bf16 v[12:15], v[60:63], v[72:75], v[12:15]\n"
"buffer_load_dwordx4 v92, s[40:43], s73 offen offset:3072 lds\n"
"v_mfma_f32_16x16x32_bf16 v[28:31], v[60:63], v[76:79], v[28:31]\n"
"s_waitcnt lgkmcnt(2)\n"
"v_mfma_f32_16x16x32_bf16 v[0:3], v[32:35], a[176:179], v[0:3]\n"
"ds_read_b128 v[48:51], v124 offset:28672\n"
"s_add_u32 m0, s80, 0\n"
"v_mfma_f32_16x16x32_bf16 v[16:19], v[32:35], a[180:183], v[16:19]\n"
"ds_read_b128 v[52:55], v124 offset:28928\n"
"s_add_u32 s73, s1, 0\n"
"v_mfma_f32_16x16x32_bf16 v[4:7], v[36:39], a[176:179], v[4:7]\n"
"buffer_load_dwordx4 v89, s[40:43], s73 offen offset:0 lds\n"
"v_mfma_f32_16x16x32_bf16 v[20:23], v[36:39], a[180:183], v[20:23]\n"
"s_waitcnt lgkmcnt(2)\n"
"v_mfma_f32_16x16x32_bf16 v[8:11], v[40:43], a[176:179], v[8:11]\n"
"ds_read_b128 v[56:59], v124 offset:29184\n"
"v_mfma_f32_16x16x32_bf16 v[24:27], v[40:43], a[180:183], v[24:27]\n"
"ds_read_b128 v[60:63], v124 offset:29440\n"
"v_mfma_f32_16x16x32_bf16 v[12:15], v[44:47], a[176:179], v[12:15]\n"
"buffer_load_dwordx4 v90, s[40:43], s73 offen offset:1024 lds\n"
"v_mfma_f32_16x16x32_bf16 v[28:31], v[44:47], a[180:183], v[28:31]\n"
"s_waitcnt vmcnt(16) lgkmcnt(2)\n"
"s_barrier\n"
"v_mfma_f32_16x16x32_bf16 v[0:3], v[48:51], a[184:187], v[0:3]\n"
"ds_read_b128 v[32:35], v124 offset:32768\n"
"v_mfma_f32_16x16x32_bf16 v[16:19], v[48:51], a[188:191], v[16:19]\n"
"ds_read_b128 v[36:39], v124 offset:33024\n"
"v_mfma_f32_16x16x32_bf16 v[4:7], v[52:55], a[184:187], v[4:7]\n"
"buffer_load_dwordx4 v91, s[40:43], s73 offen offset:2048 lds\n"
"v_mfma_f32_16x16x32_bf16 v[20:23], v[52:55], a[188:191], v[20:23]\n"
"s_waitcnt lgkmcnt(2)\n"
"v_mfma_f32_16x16x32_bf16 v[8:11], v[56:59], a[184:187], v[8:11]\n"
"ds_read_b128 v[40:43], v124 offset:33280\n"
"v_mfma_f32_16x16x32_bf16 v[24:27], v[56:59], a[188:191], v[24:27]\n"
"ds_read_b128 v[44:47], v124 offset:33536\n"
"v_mfma_f32_16x16x32_bf16 v[12:15], v[60:63], a[184:187], v[12:15]\n"
"buffer_load_dwordx4 v92, s[40:43], s73 offen offset:3072 lds\n"
"v_mfma_f32_16x16x32_bf16 v[28:31], v[60:63], a[188:191], v[28:31]\n"
"s_waitcnt lgkmcnt(2)\n"
"v_mfma_f32_16x16x32_bf16 v[0:3], v[32:35], a[192:195], v[0:3]\n"
"ds_read_b128 v[48:51], v124 offset:36864\n"
"s_add_u32 m0, s80, 114688\n"
"v_mfma_f32_16x16x32_bf16 v[16:19], v[32:35], a[196:199], v[16:19]\n"
"ds_read_b128 v[52:55], v124 offset:37120\n"
"s_add_u32 s73, s1, 36864\n"
"v_mfma_f32_16x16x32_bf16 v[4:7], v[36:39], a[192:195], v[4:7]\n"
"buffer_load_dwordx4 v89, s[44:47], s73 offen offset:0 lds\n"
"v_mfma_f32_16x16x32_bf16 v[20:23], v[36:39], a[196:199], v[20:23]\n"
"s_waitcnt lgkmcnt(2)\n"
"v_mfma_f32_16x16x32_bf16 v[8:11], v[40:43], a[192:195], v[8:11]\n"
"ds_read_b128 v[56:59], v124 offset:37376\n"
"v_mfma_f32_16x16x32_bf16 v[24:27], v[40:43], a[196:199], v[24:27]\n"
"ds_read_b128 v[60:63], v124 offset:37632\n"
"v_mfma_f32_16x16x32_bf16 v[12:15], v[44:47], a[192:195], v[12:15]\n"
"buffer_load_dwordx4 v90, s[44:47], s73 offen offset:1024 lds\n"
"v_mfma_f32_16x16x32_bf16 v[28:31], v[44:47], a[196:199], v[28:31]\n"
"s_waitcnt vmcnt(17) lgkmcnt(2)\n"
"s_barrier\n"
"v_mfma_f32_16x16x32_bf16 v[0:3], v[48:51], a[200:203], v[0:3]\n"
"ds_read_b128 v[32:35], v124 offset:40960\n"
"v_mfma_f32_16x16x32_bf16 v[16:19], v[48:51], a[204:207], v[16:19]\n"
"ds_read_b128 v[36:39], v124 offset:41216\n"
"v_mfma_f32_16x16x32_bf16 v[4:7], v[52:55], a[200:203], v[4:7]\n"
"buffer_load_dwordx4 v91, s[44:47], s73 offen offset:2048 lds\n"
"v_mfma_f32_16x16x32_bf16 v[20:23], v[52:55], a[204:207], v[20:23]\n"
"s_waitcnt lgkmcnt(2)\n"
"v_mfma_f32_16x16x32_bf16 v[8:11], v[56:59], a[200:203], v[8:11]\n"
"ds_read_b128 v[40:43], v124 offset:41472\n"
"v_mfma_f32_16x16x32_bf16 v[24:27], v[56:59], a[204:207], v[24:27]\n"
"ds_read_b128 v[44:47], v124 offset:41728\n"
"v_mfma_f32_16x16x32_bf16 v[12:15], v[60:63], a[200:203], v[12:15]\n"
"buffer_load_dwordx4 v92, s[44:47], s73 offen offset:3072 lds\n"
"v_mfma_f32_16x16x32_bf16 v[28:31], v[60:63], a[204:207], v[28:31]\n"
"s_waitcnt lgkmcnt(2)\n"
"v_mfma_f32_16x16x32_bf16 v[0:3], v[32:35], a[208:211], v[0:3]\n"
"ds_read_b128 v[48:51], v124 offset:45056\n"
"s_add_u32 m0, s80, 16384\n"
"v_mfma_f32_16x16x32_bf16 v[16:19], v[32:35], a[212:215], v[16:19]\n"
"ds_read_b128 v[52:55], v124 offset:45312\n"
"s_add_u32 s73, s1, 4096\n"
"v_mfma_f32_16x16x32_bf16 v[4:7], v[36:39], a[208:211], v[4:7]\n"
"buffer_load_dwordx4 v89, s[40:43], s73 offen offset:0 lds\n"
"v_mfma_f32_16x16x32_bf16 v[20:23], v[36:39], a[212:215], v[20:23]\n"
"s_waitcnt lgkmcnt(2)\n"
"v_mfma_f32_16x16x32_bf16 v[8:11], v[40:43], a[208:211], v[8:11]\n"
"ds_read_b128 v[56:59], v124 offset:45568\n"
"v_mfma_f32_16x16x32_bf16 v[24:27], v[40:43], a[212:215], v[24:27]\n"
"ds_read_b128 v[60:63], v124 offset:45824\n"
"v_mfma_f32_16x16x32_bf16 v[12:15], v[44:47], a[208:211], v[12:15]\n"
"buffer_load_dwordx4 v90, s[40:43], s73 offen offset:1024 lds\n"
"v_mfma_f32_16x16x32_bf16 v[28:31], v[44:47], a[212:215], v[28:31]\n"
"s_waitcnt vmcnt(17) lgkmcnt(2)\n"
"s_barrier\n"
"v_mfma_f32_16x16x32_bf16 v[0:3], v[48:51], a[216:219], v[0:3]\n"
"v_add_u32 v124, 81920, v124\n"
"ds_read_b128 v[32:35], v124 offset:49152\n"
"v_mfma_f32_16x16x32_bf16 v[16:19], v[48:51], a[220:223], v[16:19]\n"
"ds_read_b128 v[36:39], v124 offset:49408\n"
"v_mfma_f32_16x16x32_bf16 v[4:7], v[52:55], a[216:219], v[4:7]\n"
"buffer_load_dwordx4 v91, s[40:43], s73 offen offset:2048 lds\n"
"v_mfma_f32_16x16x32_bf16 v[20:23], v[52:55], a[220:223], v[20:23]\n"
"s_waitcnt lgkmcnt(2)\n"
"v_mfma_f32_16x16x32_bf16 v[8:11], v[56:59], a[216:219], v[8:11]\n"
"ds_read_b128 v[40:43], v124 offset:49664\n"
"v_mfma_f32_16x16x32_bf16 v[24:27], v[56:59], a[220:223], v[24:27]\n"
"ds_read_b128 v[44:47], v124 offset:49920\n"
"v_mfma_f32_16x16x32_bf16 v[12:15], v[60:63], a[216:219], v[12:15]\n"
"buffer_load_dwordx4 v[128:131], v126, s[36:39], s61 offen sc1\n"
"buffer_load_dwordx2 v[132:133], v126, s[36:39], s62 offen offset:8 sc1\n"
"buffer_load_dwordx2 v[134:135], v126, s[36:39], s63 offen offset:8 sc1\n"
"v_mfma_f32_16x16x32_bf16 v[28:31], v[60:63], a[220:223], v[28:31]\n"
"s_waitcnt lgkmcnt(2)\n"
"v_mfma_f32_16x16x32_bf16 v[0:3], v[32:35], a[224:227], v[0:3]\n"
"ds_read_b128 v[48:51], v124 offset:53248\n"
"v_mfma_f32_16x16x32_bf16 v[16:19], v[32:35], a[228:231], v[16:19]\n"
"ds_read_b128 v[52:55], v124 offset:53504\n"
"v_mfma_f32_16x16x32_bf16 v[4:7], v[36:39], a[224:227], v[4:7]\n"
"buffer_load_dwordx4 v92, s[40:43], s73 offen offset:3072 lds\n"
"v_mfma_f32_16x16x32_bf16 v[20:23], v[36:39], a[228:231], v[20:23]\n"
"s_waitcnt lgkmcnt(2)\n"
"v_mfma_f32_16x16x32_bf16 v[8:11], v[40:43], a[224:227], v[8:11]\n"
"ds_read_b128 v[56:59], v124 offset:53760\n"
"v_mfma_f32_16x16x32_bf16 v[24:27], v[40:43], a[228:231], v[24:27]\n"
"ds_read_b128 v[60:63], v124 offset:54016\n"
"v_mfma_f32_16x16x32_bf16 v[12:15], v[44:47], a[224:227], v[12:15]\n"
"v_mfma_f32_16x16x32_bf16 v[28:31], v[44:47], a[228:231], v[28:31]\n"
"s_waitcnt vmcnt(18) lgkmcnt(2)\n"
"s_barrier\n"
"v_mfma_f32_16x16x32_bf16 v[0:3], v[48:51], a[232:235], v[0:3]\n"
"ds_read_b128 v[32:35], v124 offset:57344\n"
"s_add_u32 m0, s80, 32768\n"
"v_mfma_f32_16x16x32_bf16 v[16:19], v[48:51], a[236:239], v[16:19]\n"
"ds_read_b128 v[36:39], v124 offset:57600\n"
"s_add_u32 s73, s1, 32768\n"
"v_mfma_f32_16x16x32_bf16 v[4:7], v[52:55], a[232:235], v[4:7]\n"
"buffer_load_dwordx4 v89, s[40:43], s73 offen offset:0 lds\n"
"v_mfma_f32_16x16x32_bf16 v[20:23], v[52:55], a[236:239], v[20:23]\n"
"s_waitcnt lgkmcnt(2)\n"
"v_mfma_f32_16x16x32_bf16 v[8:11], v[56:59], a[232:235], v[8:11]\n"
"ds_read_b128 v[40:43], v124 offset:57856\n"
"v_mfma_f32_16x16x32_bf16 v[24:27], v[56:59], a[236:239], v[24:27]\n"
"ds_read_b128 v[44:47], v124 offset:58112\n"
"v_mfma_f32_16x16x32_bf16 v[12:15], v[60:63], a[232:235], v[12:15]\n"
"buffer_load_dwordx4 v90, s[40:43], s73 offen offset:1024 lds\n"
"v_mfma_f32_16x16x32_bf16 v[28:31], v[60:63], a[236:239], v[28:31]\n"
"s_waitcnt lgkmcnt(2)\n"
"v_mfma_f32_16x16x32_bf16 v[0:3], v[32:35], a[240:243], v[0:3]\n"
"ds_read_b128 v[48:51], v124 offset:61440\n"
"v_mfma_f32_16x16x32_bf16 v[16:19], v[32:35], a[244:247], v[16:19]\n"
"ds_read_b128 v[52:55], v124 offset:61696\n"
"v_mfma_f32_16x16x32_bf16 v[4:7], v[36:39], a[240:243], v[4:7]\n"
"buffer_load_dwordx4 v91, s[40:43], s73 offen offset:2048 lds\n"
"v_mfma_f32_16x16x32_bf16 v[20:23], v[36:39], a[244:247], v[20:23]\n"
"s_waitcnt lgkmcnt(2)\n"
"v_mfma_f32_16x16x32_bf16 v[8:11], v[40:43], a[240:243], v[8:11]\n"
"ds_read_b128 v[56:59], v124 offset:61952\n"
"v_mfma_f32_16x16x32_bf16 v[24:27], v[40:43], a[244:247], v[24:27]\n"
"ds_read_b128 v[60:63], v124 offset:62208\n"
"v_mfma_f32_16x16x32_bf16 v[12:15], v[44:47], a[240:243], v[12:15]\n"
"buffer_load_dwordx4 v92, s[40:43], s73 offen offset:3072 lds\n"
"v_mfma_f32_16x16x32_bf16 v[28:31], v[44:47], a[244:247], v[28:31]\n"
"s_waitcnt lgkmcnt(2)\n"
"v_mfma_f32_16x16x32_bf16 v[0:3], v[48:51], a[248:251], v[0:3]\n"
"v_mfma_f32_16x16x32_bf16 v[16:19], v[48:51], a[252:255], v[16:19]\n"
"v_mfma_f32_16x16x32_bf16 v[4:7], v[52:55], a[248:251], v[4:7]\n"
"v_mfma_f32_16x16x32_bf16 v[20:23], v[52:55], a[252:255], v[20:23]\n"
"s_waitcnt lgkmcnt(0)\n"
"v_mfma_f32_16x16x32_bf16 v[8:11], v[56:59], a[248:251], v[8:11]\n"
"v_mfma_f32_16x16x32_bf16 v[24:27], v[56:59], a[252:255], v[24:27]\n"
"v_mfma_f32_16x16x32_bf16 v[12:15], v[60:63], a[248:251], v[12:15]\n"
"v_mfma_f32_16x16x32_bf16 v[28:31], v[60:63], a[252:255], v[28:31]\n"
"v_subrev_u32 v124, 81920, v124\n"
"v_cvt_pk_bf16_f32 v64, v0, v1\n"
"v_cvt_pk_bf16_f32 v65, v2, v3\n"
"v_cvt_pk_bf16_f32 v66, v4, v5\n"
"v_cvt_pk_bf16_f32 v67, v6, v7\n"
"v_xor_b32 v64, s83, v64\n"
"v_xor_b32 v65, s83, v65\n"
"v_xor_b32 v66, s83, v66\n"
"v_xor_b32 v67, s83, v67\n"
"buffer_store_dwordx4 v[64:67], v126, s[36:39], s28 offen offset:0 sc1\n"
"v_cvt_pk_bf16_f32 v72, v16, v17\n"
"v_cvt_pk_bf16_f32 v73, v18, v19\n"
"v_cvt_pk_bf16_f32 v74, v20, v21\n"
"v_cvt_pk_bf16_f32 v75, v22, v23\n"
"v_xor_b32 v72, s83, v72\n"
"v_xor_b32 v73, s83, v73\n"
"v_xor_b32 v74, s83, v74\n"
"v_xor_b32 v75, s83, v75\n"
"buffer_store_dwordx4 v[72:75], v126, s[36:39], s29 offen offset:0 sc1\n"
"v_cvt_pk_bf16_f32 v68, v8, v9\n"
"v_cvt_pk_bf16_f32 v69, v10, v11\n"
"v_cvt_pk_bf16_f32 v70, v12, v13\n"
"v_cvt_pk_bf16_f32 v71, v14, v15\n"
"v_xor_b32 v68, s83, v68\n"
"v_xor_b32 v69, s83, v69\n"
"v_xor_b32 v70, s83, v70\n"
"v_xor_b32 v71, s83, v71\n"
"buffer_store_dwordx4 v[68:71], v126, s[36:39], s28 offen offset:1024 sc1\n"
"v_cvt_pk_bf16_f32 v76, v24, v25\n"
"v_cvt_pk_bf16_f32 v77, v26, v27\n"
"v_cvt_pk_bf16_f32 v78, v28, v29\n"
"v_cvt_pk_bf16_f32 v79, v30, v31\n"
"v_xor_b32 v76, s83, v76\n"
"v_xor_b32 v77, s83, v77\n"
"v_xor_b32 v78, s83, v78\n"
"v_xor_b32 v79, s83, v79\n"
"buffer_store_dwordx4 v[76:79], v126, s[36:39], s29 offen offset:1024 sc1\n"
"s_waitcnt vmcnt(9)\n"
"v_xor_b32 v128, s0, v128\n"
"v_xor_b32 v129, s0, v129\n"
"v_xor_b32 v130, s0, v130\n"
"v_xor_b32 v131, s0, v131\n"
"v_or_b32 v137, v128, v129\n"
"v_or3_b32 v137, v137, v130, v131\n"
"v_and_b32 v137, 0xff, v137\n"
"v_bitop3_b32 v137, v137, v133, s68 bitop3:0xf6\n"
"v_bitop3_b32 v137, v137, v135, s68 bitop3:0xf6\n"
"v_cmp_ne_u32 vcc, 0, v137\n"
"s_cbranch_vccnz S22_%=\n"
"B23_%=:\n"
"v_add_f32 v140, v128, v129\n"
"v_mul_f32 v141, v128, v128\n"
"v_add_f32 v142, v130, v131\n"
"v_mul_f32 v143, v130, v130\n"
"v_fmac_f32 v141, v129, v129\n"
"v_fmac_f32 v143, v131, v131\n"
"s_lshl_b32 s99, s71, 4\n"
"s_add_u32 s99, s99, 155648\n"
"v_lshrrev_b32 v137, 3, v127\n"
"v_lshrrev_b32 v138, 1, v127\n"
"v_and_b32 v137, 4, v137\n"
"v_and_or_b32 v137, v138, 8, v137\n"
"v_add_u32 v137, s99, v137\n"
"v_permlane32_swap_b32 v140, v141\n"
"v_permlane32_swap_b32 v142, v143\n"
"v_add_f32 v140, v140, v141\n"
"v_add_f32 v142, v142, v143\n"
"s_nop 1\n"
"v_permlane16_swap_b32 v140, v142\n"
"v_add_f32 v140, v140, v142\n"
"s_nop 1\n"
"v_add_f32_dpp v140, v140, v140 row_ror:8 row_mask:0xf bank_mask:0xf\n"
"s_nop 1\n"
"v_add_f32_dpp v140, v140, v140 row_ror:4 row_mask:0xf bank_mask:0xf\n"
"s_nop 1\n"
"v_add_f32_dpp v140, v140, v140 row_ror:2 row_mask:0xf bank_mask:0xf\n"
"s_nop 1\n"
"v_add_f32_dpp v140, v140, v140 row_ror:1 row_mask:0xf bank_mask:0xf\n"
"ds_write_b32 v137, v140\n"
"s_waitcnt lgkmcnt(0)\n"
"s_barrier\n"
"v_cmp_le_u32 vcc, s87, v88\n"
"s_cbranch_vccz X24_%=\n"
"s_mov_b64 exec, vcc\n"
"s_lshl_b32 s97, s70, 9\n"
"s_add_u32 s97, s97, 0x10000\n"
"v_cmp_eq_u32 vcc, s87, v88\n"
"v_and_b32 v66, 48, v127\n"
"v_lshlrev_b32 v66, 2, v66\n"
"v_cndmask_b32_e64 v67, 0, 8, vcc\n"
"v_add_u32 v66, v66, v67\n"
"buffer_load_dwordx2 v[96:97], v66, s[36:39], s97 offen offset:0 sc1\n"
"buffer_load_dwordx2 v[98:99], v66, s[36:39], s97 offen offset:16 sc1\n"
"buffer_load_dwordx2 v[100:101], v66, s[36:39], s97 offen offset:32 sc1\n"
"buffer_load_dwordx2 v[102:103], v66, s[36:39], s97 offen offset:48 sc1\n"
"buffer_load_dwordx2 v[104:105], v66, s[36:39], s97 offen offset:256 sc1\n"
"buffer_load_dwordx2 v[106:107], v66, s[36:39], s97 offen offset:272 sc1\n"
"buffer_load_dwordx2 v[108:109], v66, s[36:39], s97 offen offset:288 sc1\n"
"buffer_load_dwordx2 v[110:111], v66, s[36:39], s97 offen offset:304 sc1\n"
"s_mov_b64 exec, -1\n"
"X24_%=:\n"
"v_mov_b32 v137, 155648\n"
"ds_read_b128 v[140:143], v137 offset:0\n"
"ds_read_b128 v[144:147], v137 offset:16\n"
"ds_read_b128 v[148:151], v137 offset:32\n"
"ds_read_b128 v[152:155], v137 offset:48\n"
"s_lshl_b32 s99, s71, 8\n"
"s_add_u32 s99, s99, 157696\n"
"v_lshl_add_u32 v137, v127, 2, s99\n"
"v_lshlrev_b32 v156, 16, v132\n"
"v_and_b32 v157, 0xffff0000, v132\n"
"v_lshlrev_b32 v158, 16, v134\n"
"v_and_b32 v159, 0xffff0000, v134\n"
"s_waitcnt lgkmcnt(0)\n"
"v_add_f32 v140, v140, v144\n"
"v_add_f32 v141, v141, v145\n"
"v_add_f32 v142, v142, v146\n"
"v_add_f32 v143, v143, v147\n"
"v_add_f32 v148, v148, v152\n"
"v_add_f32 v149, v149, v153\n"
"v_add_f32 v150, v150, v154\n"
"v_add_f32 v151, v151, v155\n"
"v_add_f32 v140, v140, v148\n"
"v_add_f32 v141, v141, v149\n"
"v_add_f32 v142, v142, v150\n"
"v_add_f32 v143, v143, v151\n"
"v_mul_f32 v140, 0x3b000000, v140\n"
"v_mul_f32 v141, 0x3b000000, v141\n"
"v_mul_f32 v142, 0x3b000000, v142\n"
"v_mul_f32 v143, 0x3b000000, v143\n"
"v_fma_f32 v141, -v140, v140, v141\n"
"v_fma_f32 v143, -v142, v142, v143\n"
"v_max_f32 v141, 0, v141\n"
"v_max_f32 v143, 0, v143\n"
"v_add_f32 v141, 0x3727c5ac, v141\n"
"v_add_f32 v143, 0x3727c5ac, v143\n"
"v_rsq_f32 v141, v141\n"
"v_rsq_f32 v143, v143\n"
"v_sub_f32 v128, v128, v140\n"
"v_sub_f32 v129, v129, v140\n"
"v_sub_f32 v130, v130, v142\n"
"v_sub_f32 v131, v131, v142\n"
"v_fma_f32 v128, v128, v141, -v156\n"
"v_fma_f32 v129, v129, v141, -v157\n"
"v_fma_f32 v130, v130, v143, -v158\n"
"v_fma_f32 v131, v131, v143, -v159\n"
"v_cvt_pk_bf16_f32 v128, v128, v129\n"
"ds_write_b32 v137, v128 offset:0\n"
"v_cvt_pk_bf16_f32 v129, v130, v131\n"
"ds_write_b32 v137, v129 offset:1088\n"
"s_waitcnt lgkmcnt(0)\n"
"s_lshl_b32 s76, s69, 10\n"
"s_and_b32 s76, s76, 0x2000\n"
"s_xor_b32 s76, s76, s65\n"
"s_lshl_b32 s75, s69, 18\n"
"s_add_u32 s75, s75, s76\n"
"s_add_u32 s75, s75, 0x2000000\n"
"s_lshl_b32 s76, s71, 16\n"
"s_add_u32 s75, s75, s76\n"
"s_lshl_b32 s73, s85, 14\n"
"s_lshr_b32 s76, s84, 3\n"
"s_lshl_b32 s76, s76, 9\n"
"s_add_u32 s73, s73, s76\n"
"s_add_u32 s73, s73, s65\n"
"s_add_u32 s73, s73, 0xc0800\n"
"v_lshlrev_b32 v38, 6, v127\n"
"v_mov_b32 v39, s68\n"
"s_lshl_b32 s99, s70, 6\n"
"s_add_u32 s99, s99, s73\n"
"s_cmp_eq_u32 s71, 1\n"
"s_cselect_b32 s99, s99, 0x2800000\n"
"v_lshlrev_b32 v36, 14, v127\n"
"v_mov_b32 v37, s68\n"
"s_waitcnt vmcnt(0)\n"
"s_barrier\n"
"s_mov_b64 exec, 0x1\n"
"buffer_store_dword v37, v36, s[36:39], s99 offen sc1\n"
"s_mov_b64 exec, -1\n"
"ds_read_b128 v[192:195], v186 offset:0\n"
"ds_read_b128 v[196:199], v178 offset:0\n"
"ds_read_b128 v[200:203], v182 offset:0\n"
"ds_read_b128 v[204:207], v186 offset:64\n"
"ds_read_b128 v[208:211], v179 offset:0\n"
"ds_read_b128 v[212:215], v183 offset:0\n"
"ds_read_b128 v[216:219], v186 offset:128\n"
"ds_read_b128 v[220:223], v180 offset:0\n"
"ds_read_b128 v[224:227], v184 offset:0\n"
"ds_read_b128 v[228:231], v186 offset:192\n"
"ds_read_b128 v[232:235], v181 offset:0\n"
"ds_read_b128 v[236:239], v185 offset:0\n"
"s_waitcnt lgkmcnt(9)\n"
"v_mfma_f32_16x16x32_bf16 v[168:171], v[192:195], v[196:199], 0\n"
"v_mfma_f32_16x16x32_bf16 v[172:175], v[192:195], v[200:203], 0\n"
"ds_read_b128 v[240:243], v186 offset:256\n"
"ds_read_b128 v[244:247], v178 offset:256\n"
"ds_read_b128 v[248:251], v182 offset:256\n"
"s_waitcnt lgkmcnt(9)\n"
"v_mfma_f32_16x16x32_bf16 v[168:171], v[204:207], v[208:211], v[168:171]\n"
"v_mfma_f32_16x16x32_bf16 v[172:175], v[204:207], v[212:215], v[172:175]\n"
"ds_read_b128 v[192:195], v186 offset:320\n"
"ds_read_b128 v[196:199], v179 offset:256\n"
"ds_read_b128 v[200:203], v183 offset:256\n"
"s_waitcnt lgkmcnt(9)\n"
"v_mfma_f32_16x16x32_bf16 v[168:171], v[216:219], v[220:223], v[168:171]\n"
"v_mfma_f32_16x16x32_bf16 v[172:175], v[216:219], v[224:227], v[172:175]\n"
"ds_read_b128 v[204:207], v186 offset:384\n"
"ds_read_b128 v[208:211], v180 offset:256\n"
"ds_read_b128 v[212:215], v184 offset:256\n"
"s_waitcnt lgkmcnt(9)\n"
"v_mfma_f32_16x16x32_bf16 v[168:171], v[228:231], v[232:235], v[168:171]\n"
"v_mfma_f32_16x16x32_bf16 v[172:175], v[228:231], v[236:239], v[172:175]\n"
"ds_read_b128 v[216:219], v186 offset:448\n"
"ds_read_b128 v[220:223], v181 offset:256\n"
"ds_read_b128 v[224:227], v185 offset:256\n"
"s_waitcnt lgkmcnt(9)\n"
"v_mfma_f32_16x16x32_bf16 v[168:171], v[240:243], v[244:247], v[168:171]\n"
"v_mfma_f32_16x16x32_bf16 v[172:175], v[240:243], v[248:251], v[172:175]\n"
"ds_read_b128 v[228:231], v186 offset:512\n"
"ds_read_b128 v[232:235], v178 offset:512\n"
"ds_read_b128 v[236:239], v182 offset:512\n"
"s_waitcnt lgkmcnt(9)\n"
"v_mfma_f32_16x16x32_bf16 v[168:171], v[192:195], v[196:199], v[168:171]\n"
"v_mfma_f32_16x16x32_bf16 v[172:175], v[192:195], v[200:203], v[172:175]\n"
"ds_read_b128 v[240:243], v186 offset:576\n"
"ds_read_b128 v[244:247], v179 offset:512\n"
"ds_read_b128 v[248:251], v183 offset:512\n"
"s_waitcnt lgkmcnt(9)\n"
"v_mfma_f32_16x16x32_bf16 v[168:171], v[204:207], v[208:211], v[168:171]\n"
"v_mfma_f32_16x16x32_bf16 v[172:175], v[204:207], v[212:215], v[172:175]\n"
"ds_read_b128 v[192:195], v186 offset:640\n"
"ds_read_b128 v[196:199], v180 offset:512\n"
"ds_read_b128 v[200:203], v184 offset:512\n"
"s_waitcnt lgkmcnt(9)\n"
"v_mfma_f32_16x16x32_bf16 v[168:171], v[216:219], v[220:223], v[168:171]\n"
"v_mfma_f32_16x16x32_bf16 v[172:175], v[216:219], v[224:227], v[172:175]\n"
"ds_read_b128 v[204:207], v186 offset:704\n"
"ds_read_b128 v[208:211], v181 offset:512\n"
"ds_read_b128 v[212:215], v185 offset:512\n"
"s_waitcnt lgkmcnt(9)\n"
"v_mfma_f32_16x16x32_bf16 v[168:171], v[228:231], v[232:235], v[168:171]\n"
"v_mfma_f32_16x16x32_bf16 v[172:175], v[228:231], v[236:239], v[172:175]\n"
"ds_read_b128 v[216:219], v186 offset:768\n"
"ds_read_b128 v[220:223], v178 offset:768\n"
"ds_read_b128 v[224:227], v182 offset:768\n"
"s_cmp_lg_u32 s71, 1\n"
"s_cbranch_scc1 R26_%=\n"
"s_mov_b64 exec, 0xff\n"
"buffer_load_dword v39, v38, s[36:39], s73 offen sc1\n"
"s_mov_b64 exec, -1\n"
"R26_%=:\n"
"s_waitcnt lgkmcnt(9)\n"
"v_mfma_f32_16x16x32_bf16 v[168:171], v[240:243], v[244:247], v[168:171]\n"
"v_mfma_f32_16x16x32_bf16 v[172:175], v[240:243], v[248:251], v[172:175]\n"
"ds_read_b128 v[228:231], v186 offset:832\n"
"ds_read_b128 v[232:235], v179 offset:768\n"
"ds_read_b128 v[236:239], v183 offset:768\n"
"s_waitcnt lgkmcnt(9)\n"
"v_mfma_f32_16x16x32_bf16 v[168:171], v[192:195], v[196:199], v[168:171]\n"
"v_mfma_f32_16x16x32_bf16 v[172:175], v[192:195], v[200:203], v[172:175]\n"
"ds_read_b128 v[240:243], v186 offset:896\n"
"ds_read_b128 v[244:247], v180 offset:768\n"
"ds_read_b128 v[248:251], v184 offset:768\n"
"s_waitcnt lgkmcnt(9)\n"
"v_mfma_f32_16x16x32_bf16 v[168:171], v[204:207], v[208:211], v[168:171]\n"
"v_mfma_f32_16x16x32_bf16 v[172:175], v[204:207], v[212:215], v[172:175]\n"
"ds_read_b128 v[192:195], v186 offset:960\n"
"ds_read_b128 v[196:199], v181 offset:768\n"
"ds_read_b128 v[200:203], v185 offset:768\n"
"s_waitcnt lgkmcnt(9)\n"
"v_mfma_f32_16x16x32_bf16 v[168:171], v[216:219], v[220:223], v[168:171]\n"
"v_mfma_f32_16x16x32_bf16 v[172:175], v[216:219], v[224:227], v[172:175]\n"
"s_waitcnt lgkmcnt(6)\n"
"v_mfma_f32_16x16x32_bf16 v[168:171], v[228:231], v[232:235], v[168:171]\n"
"v_mfma_f32_16x16x32_bf16 v[172:175], v[228:231], v[236:239], v[172:175]\n"
"s_waitcnt lgkmcnt(3)\n"
"v_mfma_f32_16x16x32_bf16 v[168:171], v[240:243], v[244:247], v[168:171]\n"
"v_mfma_f32_16x16x32_bf16 v[172:175], v[240:243], v[248:251], v[172:175]\n"
"s_waitcnt lgkmcnt(0)\n"
"v_mfma_f32_16x16x32_bf16 v[168:171], v[192:195], v[196:199], v[168:171]\n"
"v_mfma_f32_16x16x32_bf16 v[172:175], v[192:195], v[200:203], v[172:175]\n"
"s_nop 6\n"
"v_cvt_pk_bf16_f32 v176, v168, v169\n"
"v_cvt_pk_bf16_f32 v177, -v172, -v173\n"
"ds_write_b64 v187, v[176:177]\n"
"s_waitcnt vmcnt(1)\n"
"v_and_b32 v96, 0xffffff00, v96\n"
"v_and_b32 v97, 0xffffff00, v97\n"
"v_and_b32 v98, 0xffffff00, v98\n"
"v_and_b32 v99, 0xffffff00, v99\n"
"v_and_b32 v100, 0xffffff00, v100\n"
"v_and_b32 v101, 0xffffff00, v101\n"
"v_and_b32 v102, 0xffffff00, v102\n"
"v_and_b32 v103, 0xffffff00, v103\n"
"v_and_b32 v104, 0xffffff00, v104\n"
"v_and_b32 v105, 0xffffff00, v105\n"
"v_and_b32 v106, 0xffffff00, v106\n"
"v_and_b32 v107, 0xffffff00, v107\n"
"v_and_b32 v108, 0xffffff00, v108\n"
"v_and_b32 v109, 0xffffff00, v109\n"
"v_and_b32 v110, 0xffffff00, v110\n"
"v_and_b32 v111, 0xffffff00, v111\n"
"v_lshlrev_b32 v48, 16, v80\n"
"v_and_b32 v49, 0xffff0000, v80\n"
"v_lshlrev_b32 v50, 16, v81\n"
"v_and_b32 v51, 0xffff0000, v81\n"
"v_lshlrev_b32 v52, 16, v82\n"
"v_and_b32 v53, 0xffff0000, v82\n"
"v_lshlrev_b32 v54, 16, v83\n"
"v_and_b32 v55, 0xffff0000, v83\n"
"v_lshlrev_b32 v56, 16, v84\n"
"v_and_b32 v57, 0xffff0000, v84\n"
"v_lshlrev_b32 v58, 16, v85\n"
"v_and_b32 v59, 0xffff0000, v85\n"
"v_lshlrev_b32 v60, 16, v86\n"
"v_and_b32 v61, 0xffff0000, v86\n"
"v_lshlrev_b32 v62, 16, v87\n"
"v_and_b32 v63, 0xffff0000, v87\n"
"s_waitcnt lgkmcnt(0)\n"
"s_cmp_lg_u32 s71, 1\n"
"s_cbranch_scc1 R28_%=\n"
"s_waitcnt vmcnt(0)\n"
"v_cmp_ne_u32 vcc, s68, v39\n"
"s_cbranch_vccnz S30_%=\n"
"B31_%=:\n"
"R28_%=:\n"
"s_barrier\n"
"s_lshl_b32 s73, s69, 14\n"
"s_add_u32 s73, s73, s81\n"
"s_add_u32 s73, s73, s65\n"
"s_add_u32 s73, s73, 0x180000\n"
"s_lshl_b32 s74, s70, 8\n"
"s_add_u32 s74, s74, s73\n"
"buffer_load_dwordx4 v[128:131], v126, s[36:39], s75 offen offset:0 sc1\n"
"buffer_load_dwordx4 v[132:135], v126, s[36:39], s75 offen offset:1024 sc1\n"
"buffer_load_dwordx4 v[136:139], v126, s[36:39], s75 offen offset:2048 sc1\n"
"buffer_load_dwordx4 v[140:143], v126, s[36:39], s75 offen offset:3072 sc1\n"
"s_add_u32 s75, s75, 4096\n"
"buffer_load_dwordx4 v[144:147], v126, s[36:39], s75 offen offset:0 sc1\n"
"buffer_load_dwordx4 v[148:151], v126, s[36:39], s75 offen offset:1024 sc1\n"
"buffer_load_dwordx4 v[152:155], v126, s[36:39], s75 offen offset:2048 sc1\n"
"buffer_load_dwordx4 v[156:159], v126, s[36:39], s75 offen offset:3072 sc1\n"
"ds_read_u16 a4, v188 offset:0\n"
"ds_read_u16 a8, v188 offset:128\n"
"ds_read_u16 a12, v188 offset:256\n"
"ds_read_u16 a16, v188 offset:384\n"
"s_waitcnt lgkmcnt(0)\n"
"v_mfma_f32_16x16x32_bf16 v[0:3], a[4:7], a[0:3], v[96:99]\n"
"v_mfma_f32_16x16x32_bf16 v[4:7], a[8:11], a[0:3], v[100:103]\n"
"v_mfma_f32_16x16x32_bf16 v[8:11], a[12:15], a[0:3], v[104:107]\n"
"v_mfma_f32_16x16x32_bf16 v[12:15], a[16:19], a[0:3], v[108:111]\n"
"ds_read_b128 v[208:211], v112 offset:0\n"
"ds_read_b128 v[212:215], v112 offset:16384\n"
"ds_read_b128 v[216:219], v112 offset:32768\n"
"ds_read_b128 v[220:223], v112 offset:49152\n"
"ds_read_b128 v[224:227], v113 offset:0\n"
"ds_read_b128 v[228:231], v113 offset:16384\n"
"ds_read_b128 v[232:235], v113 offset:32768\n"
"ds_read_b128 v[236:239], v113 offset:49152\n"
"ds_read_b128 v[240:243], v114 offset:0\n"
"ds_read_b128 v[244:247], v114 offset:16384\n"
"ds_read_b128 v[248:251], v114 offset:32768\n"
"s_waitcnt vmcnt(7) lgkmcnt(7)\n"
"v_mfma_f32_16x16x32_bf16 v[0:3], v[208:211], v[128:131], v[0:3]\n"
"ds_read_b128 v[252:255], v114 offset:49152\n"
"v_mfma_f32_16x16x32_bf16 v[4:7], v[212:215], v[128:131], v[4:7]\n"
"ds_read_b128 v[208:211], v115 offset:0\n"
"s_add_u32 s75, s75, 12288\n"
"buffer_load_dwordx4 v[160:163], v126, s[36:39], s75 offen offset:0 sc1\n"
"v_mfma_f32_16x16x32_bf16 v[8:11], v[216:219], v[128:131], v[8:11]\n"
"ds_read_b128 v[212:215], v115 offset:16384\n"
"buffer_load_dwordx4 v[164:167], v126, s[36:39], s75 offen offset:1024 sc1\n"
"v_mfma_f32_16x16x32_bf16 v[12:15], v[220:223], v[128:131], v[12:15]\n"
"ds_read_b128 v[216:219], v115 offset:32768\n"
"s_waitcnt vmcnt(8) lgkmcnt(7)\n"
"v_mfma_f32_16x16x32_bf16 v[0:3], v[224:227], v[132:135], v[0:3]\n"
"ds_read_b128 v[220:223], v115 offset:49152\n"
"v_mfma_f32_16x16x32_bf16 v[4:7], v[228:231], v[132:135], v[4:7]\n"
"ds_read_b128 v[224:227], v112 offset:256\n"
"buffer_load_dwordx4 v[128:131], v126, s[36:39], s75 offen offset:2048 sc1\n"
"v_mfma_f32_16x16x32_bf16 v[8:11], v[232:235], v[132:135], v[8:11]\n"
"ds_read_b128 v[228:231], v112 offset:16640\n"
"v_mfma_f32_16x16x32_bf16 v[12:15], v[236:239], v[132:135], v[12:15]\n"
"ds_read_b128 v[232:235], v112 offset:33024\n"
"s_waitcnt vmcnt(8) lgkmcnt(7)\n"
"v_mfma_f32_16x16x32_bf16 v[0:3], v[240:243], v[136:139], v[0:3]\n"
"ds_read_b128 v[236:239], v112 offset:49408\n"
"v_mfma_f32_16x16x32_bf16 v[4:7], v[244:247], v[136:139], v[4:7]\n"
"ds_read_b128 v[240:243], v113 offset:256\n"
"buffer_load_dwordx4 v[132:135], v126, s[36:39], s75 offen offset:3072 sc1\n"
"v_mfma_f32_16x16x32_bf16 v[8:11], v[248:251], v[136:139], v[8:11]\n"
"ds_read_b128 v[244:247], v113 offset:16640\n"
"v_mfma_f32_16x16x32_bf16 v[12:15], v[252:255], v[136:139], v[12:15]\n"
"ds_read_b128 v[248:251], v113 offset:33024\n"
"s_waitcnt vmcnt(8) lgkmcnt(7)\n"
"v_mfma_f32_16x16x32_bf16 v[0:3], v[208:211], v[140:143], v[0:3]\n"
"ds_read_b128 v[252:255], v113 offset:49408\n"
"v_mfma_f32_16x16x32_bf16 v[4:7], v[212:215], v[140:143], v[4:7]\n"
"ds_read_b128 v[208:211], v114 offset:256\n"
"s_add_u32 s75, s75, 4096\n"
"buffer_load_dwordx4 v[136:139], v126, s[36:39], s75 offen offset:0 sc1\n"
"v_mfma_f32_16x16x32_bf16 v[8:11], v[216:219], v[140:143], v[8:11]\n"
"ds_read_b128 v[212:215], v114 offset:16640\n"
"v_mfma_f32_16x16x32_bf16 v[12:15], v[220:223], v[140:143], v[12:15]\n"
"ds_read_b128 v[216:219], v114 offset:33024\n"
"s_waitcnt vmcnt(8) lgkmcnt(7)\n"
"v_mfma_f32_16x16x32_bf16 v[0:3], v[224:227], v[144:147], v[0:3]\n"
"ds_read_b128 v[220:223], v114 offset:49408\n"
"v_mfma_f32_16x16x32_bf16 v[4:7], v[228:231], v[144:147], v[4:7]\n"
"ds_read_b128 v[224:227], v115 offset:256\n"
"buffer_load_dwordx4 v[140:143], v126, s[36:39], s75 offen offset:1024 sc1\n"
"v_mfma_f32_16x16x32_bf16 v[8:11], v[232:235], v[144:147], v[8:11]\n"
"ds_read_b128 v[228:231], v115 offset:16640\n"
"v_mfma_f32_16x16x32_bf16 v[12:15], v[236:239], v[144:147], v[12:15]\n"
"ds_read_b128 v[232:235], v115 offset:33024\n"
"s_waitcnt vmcnt(8) lgkmcnt(7)\n"
"v_mfma_f32_16x16x32_bf16 v[0:3], v[240:243], v[148:151], v[0:3]\n"
"ds_read_b128 v[236:239], v115 offset:49408\n"
"v_mfma_f32_16x16x32_bf16 v[4:7], v[244:247], v[148:151], v[4:7]\n"
"ds_read_b128 v[240:243], v112 offset:512\n"
"buffer_load_dwordx4 v[144:147], v126, s[36:39], s75 offen offset:2048 sc1\n"
"v_mfma_f32_16x16x32_bf16 v[8:11], v[248:251], v[148:151], v[8:11]\n"
"ds_read_b128 v[244:247], v112 offset:16896\n"
"v_mfma_f32_16x16x32_bf16 v[12:15], v[252:255], v[148:151], v[12:15]\n"
"ds_read_b128 v[248:251], v112 offset:33280\n"
"s_waitcnt vmcnt(8) lgkmcnt(7)\n"
"v_mfma_f32_16x16x32_bf16 v[0:3], v[208:211], v[152:155], v[0:3]\n"
"ds_read_b128 v[252:255], v112 offset:49664\n"
"v_mfma_f32_16x16x32_bf16 v[4:7], v[212:215], v[152:155], v[4:7]\n"
"ds_read_b128 v[208:211], v113 offset:512\n"
"buffer_load_dwordx4 v[148:151], v126, s[36:39], s75 offen offset:3072 sc1\n"
"v_mfma_f32_16x16x32_bf16 v[8:11], v[216:219], v[152:155], v[8:11]\n"
"ds_read_b128 v[212:215], v113 offset:16896\n"
"v_mfma_f32_16x16x32_bf16 v[12:15], v[220:223], v[152:155], v[12:15]\n"
"ds_read_b128 v[216:219], v113 offset:33280\n"
"s_waitcnt vmcnt(8) lgkmcnt(7)\n"
"v_mfma_f32_16x16x32_bf16 v[0:3], v[224:227], v[156:159], v[0:3]\n"
"ds_read_b128 v[220:223], v113 offset:49664\n"
"v_mfma_f32_16x16x32_bf16 v[4:7], v[228:231], v[156:159], v[4:7]\n"
"ds_read_b128 v[224:227], v114 offset:512\n"
"s_add_u32 s75, s75, 12288\n"
"buffer_load_dwordx4 v[152:155], v126, s[36:39], s75 offen offset:0 sc1\n"
"v_mfma_f32_16x16x32_bf16 v[8:11], v[232:235], v[156:159], v[8:11]\n"
"ds_read_b128 v[228:231], v114 offset:16896\n"
"v_mfma_f32_16x16x32_bf16 v[12:15], v[236:239], v[156:159], v[12:15]\n"
"ds_read_b128 v[232:235], v114 offset:33280\n"
"s_waitcnt vmcnt(8) lgkmcnt(7)\n"
"v_mfma_f32_16x16x32_bf16 v[0:3], v[240:243], v[160:163], v[0:3]\n"
"ds_read_b128 v[236:239], v114 offset:49664\n"
"v_mfma_f32_16x16x32_bf16 v[4:7], v[244:247], v[160:163], v[4:7]\n"
"ds_read_b128 v[240:243], v115 offset:512\n"
"buffer_load_dwordx4 v[156:159], v126, s[36:39], s75 offen offset:1024 sc1\n"
"v_mfma_f32_16x16x32_bf16 v[8:11], v[248:251], v[160:163], v[8:11]\n"
"ds_read_b128 v[244:247], v115 offset:16896\n"
"v_mfma_f32_16x16x32_bf16 v[12:15], v[252:255], v[160:163], v[12:15]\n"
"ds_read_b128 v[248:251], v115 offset:33280\n"
"s_waitcnt vmcnt(8) lgkmcnt(7)\n"
"v_mfma_f32_16x16x32_bf16 v[0:3], v[208:211], v[164:167], v[0:3]\n"
"ds_read_b128 v[252:255], v115 offset:49664\n"
"v_mfma_f32_16x16x32_bf16 v[4:7], v[212:215], v[164:167], v[4:7]\n"
"ds_read_b128 v[208:211], v112 offset:768\n"
"buffer_load_dwordx4 v[160:163], v126, s[36:39], s75 offen offset:2048 sc1\n"
"v_mfma_f32_16x16x32_bf16 v[8:11], v[216:219], v[164:167], v[8:11]\n"
"ds_read_b128 v[212:215], v112 offset:17152\n"
"v_mfma_f32_16x16x32_bf16 v[12:15], v[220:223], v[164:167], v[12:15]\n"
"ds_read_b128 v[216:219], v112 offset:33536\n"
"s_waitcnt vmcnt(8) lgkmcnt(7)\n"
"v_mfma_f32_16x16x32_bf16 v[0:3], v[224:227], v[128:131], v[0:3]\n"
"ds_read_b128 v[220:223], v112 offset:49920\n"
"v_mfma_f32_16x16x32_bf16 v[4:7], v[228:231], v[128:131], v[4:7]\n"
"ds_read_b128 v[224:227], v113 offset:768\n"
"buffer_load_dwordx4 v[164:167], v126, s[36:39], s75 offen offset:3072 sc1\n"
"v_mfma_f32_16x16x32_bf16 v[8:11], v[232:235], v[128:131], v[8:11]\n"
"ds_read_b128 v[228:231], v113 offset:17152\n"
"v_mfma_f32_16x16x32_bf16 v[12:15], v[236:239], v[128:131], v[12:15]\n"
"ds_read_b128 v[232:235], v113 offset:33536\n"
"s_waitcnt vmcnt(8) lgkmcnt(7)\n"
"v_mfma_f32_16x16x32_bf16 v[0:3], v[240:243], v[132:135], v[0:3]\n"
"ds_read_b128 v[236:239], v113 offset:49920\n"
"v_mfma_f32_16x16x32_bf16 v[4:7], v[244:247], v[132:135], v[4:7]\n"
"ds_read_b128 v[240:243], v114 offset:768\n"
"s_add_u32 s75, s75, 4096\n"
"buffer_load_dwordx4 v[128:131], v126, s[36:39], s75 offen offset:0 sc1\n"
"v_mfma_f32_16x16x32_bf16 v[8:11], v[248:251], v[132:135], v[8:11]\n"
"ds_read_b128 v[244:247], v114 offset:17152\n"
"v_mfma_f32_16x16x32_bf16 v[12:15], v[252:255], v[132:135], v[12:15]\n"
"ds_read_b128 v[248:251], v114 offset:33536\n"
"s_waitcnt vmcnt(8) lgkmcnt(7)\n"
"v_mfma_f32_16x16x32_bf16 v[0:3], v[208:211], v[136:139], v[0:3]\n"
"ds_read_b128 v[252:255], v114 offset:49920\n"
"v_mfma_f32_16x16x32_bf16 v[4:7], v[212:215], v[136:139], v[4:7]\n"
"ds_read_b128 v[208:211], v115 offset:768\n"
"buffer_load_dwordx4 v[132:135], v126, s[36:39], s75 offen offset:1024 sc1\n"
"v_mfma_f32_16x16x32_bf16 v[8:11], v[216:219], v[136:139], v[8:11]\n"
"ds_read_b128 v[212:215], v115 offset:17152\n"
"v_mfma_f32_16x16x32_bf16 v[12:15], v[220:223], v[136:139], v[12:15]\n"
"ds_read_b128 v[216:219], v115 offset:33536\n"
"s_waitcnt vmcnt(8) lgkmcnt(7)\n"
"v_mfma_f32_16x16x32_bf16 v[0:3], v[224:227], v[140:143], v[0:3]\n"
"ds_read_b128 v[220:223], v115 offset:49920\n"
"v_mfma_f32_16x16x32_bf16 v[4:7], v[228:231], v[140:143], v[4:7]\n"
"v_add_u32 v112, 0x10000, v112\n"
"ds_read_b128 v[224:227], v112 offset:0\n"
"buffer_load_dwordx4 v[136:139], v126, s[36:39], s75 offen offset:2048 sc1\n"
"v_mfma_f32_16x16x32_bf16 v[8:11], v[232:235], v[140:143], v[8:11]\n"
"ds_read_b128 v[228:231], v112 offset:16384\n"
"v_mfma_f32_16x16x32_bf16 v[12:15], v[236:239], v[140:143], v[12:15]\n"
"ds_read_b128 v[232:235], v112 offset:32768\n"
"s_waitcnt vmcnt(8) lgkmcnt(7)\n"
"v_mfma_f32_16x16x32_bf16 v[0:3], v[240:243], v[144:147], v[0:3]\n"
"ds_read_b128 v[236:239], v112 offset:49152\n"
"v_mfma_f32_16x16x32_bf16 v[4:7], v[244:247], v[144:147], v[4:7]\n"
"v_add_u32 v113, 0x10000, v113\n"
"ds_read_b128 v[240:243], v113 offset:0\n"
"buffer_load_dwordx4 v[140:143], v126, s[36:39], s75 offen offset:3072 sc1\n"
"v_mfma_f32_16x16x32_bf16 v[8:11], v[248:251], v[144:147], v[8:11]\n"
"ds_read_b128 v[244:247], v113 offset:16384\n"
"v_mfma_f32_16x16x32_bf16 v[12:15], v[252:255], v[144:147], v[12:15]\n"
"ds_read_b128 v[248:251], v113 offset:32768\n"
"s_waitcnt vmcnt(8) lgkmcnt(7)\n"
"v_mfma_f32_16x16x32_bf16 v[0:3], v[208:211], v[148:151], v[0:3]\n"
"ds_read_b128 v[252:255], v113 offset:49152\n"
"v_mfma_f32_16x16x32_bf16 v[4:7], v[212:215], v[148:151], v[4:7]\n"
"v_add_u32 v114, 0x10000, v114\n"
"ds_read_b128 v[208:211], v114 offset:0\n"
"s_add_u32 s75, s75, 12288\n"
"buffer_load_dwordx4 v[144:147], v126, s[36:39], s75 offen offset:0 sc1\n"
"v_mfma_f32_16x16x32_bf16 v[8:11], v[216:219], v[148:151], v[8:11]\n"
"ds_read_b128 v[212:215], v114 offset:16384\n"
"v_mfma_f32_16x16x32_bf16 v[12:15], v[220:223], v[148:151], v[12:15]\n"
"ds_read_b128 v[216:219], v114 offset:32768\n"
"s_waitcnt vmcnt(8) lgkmcnt(7)\n"
"v_mfma_f32_16x16x32_bf16 v[0:3], v[224:227], v[152:155], v[0:3]\n"
"ds_read_b128 v[220:223], v114 offset:49152\n"
"v_mfma_f32_16x16x32_bf16 v[4:7], v[228:231], v[152:155], v[4:7]\n"
"v_add_u32 v115, 0x10000, v115\n"
"ds_read_b128 v[224:227], v115 offset:0\n"
"buffer_load_dwordx4 v[148:151], v126, s[36:39], s75 offen offset:1024 sc1\n"
"v_mfma_f32_16x16x32_bf16 v[8:11], v[232:235], v[152:155], v[8:11]\n"
"ds_read_b128 v[228:231], v115 offset:16384\n"
"v_mfma_f32_16x16x32_bf16 v[12:15], v[236:239], v[152:155], v[12:15]\n"
"ds_read_b128 v[232:235], v115 offset:32768\n"
"s_waitcnt vmcnt(8) lgkmcnt(7)\n"
"v_mfma_f32_16x16x32_bf16 v[0:3], v[240:243], v[156:159], v[0:3]\n"
"ds_read_b128 v[236:239], v115 offset:49152\n"
"v_mfma_f32_16x16x32_bf16 v[4:7], v[244:247], v[156:159], v[4:7]\n"
"ds_read_b128 v[240:243], v112 offset:256\n"
"buffer_load_dwordx4 v[152:155], v126, s[36:39], s75 offen offset:2048 sc1\n"
"v_mfma_f32_16x16x32_bf16 v[8:11], v[248:251], v[156:159], v[8:11]\n"
"ds_read_b128 v[244:247], v112 offset:16640\n"
"v_mfma_f32_16x16x32_bf16 v[12:15], v[252:255], v[156:159], v[12:15]\n"
"ds_read_b128 v[248:251], v112 offset:33024\n"
"s_waitcnt vmcnt(8) lgkmcnt(7)\n"
"v_mfma_f32_16x16x32_bf16 v[0:3], v[208:211], v[160:163], v[0:3]\n"
"ds_read_b128 v[252:255], v112 offset:49408\n"
"v_mfma_f32_16x16x32_bf16 v[4:7], v[212:215], v[160:163], v[4:7]\n"
"ds_read_b128 v[208:211], v113 offset:256\n"
"buffer_load_dwordx4 v[156:159], v126, s[36:39], s75 offen offset:3072 sc1\n"
"v_mfma_f32_16x16x32_bf16 v[8:11], v[216:219], v[160:163], v[8:11]\n"
"ds_read_b128 v[212:215], v113 offset:16640\n"
"v_mfma_f32_16x16x32_bf16 v[12:15], v[220:223], v[160:163], v[12:15]\n"
"ds_read_b128 v[216:219], v113 offset:33024\n"
"s_waitcnt vmcnt(8) lgkmcnt(7)\n"
"v_mfma_f32_16x16x32_bf16 v[0:3], v[224:227], v[164:167], v[0:3]\n"
"ds_read_b128 v[220:223], v113 offset:49408\n"
"v_mfma_f32_16x16x32_bf16 v[4:7], v[228:231], v[164:167], v[4:7]\n"
"ds_read_b128 v[224:227], v114 offset:256\n"
"s_add_u32 s75, s75, 4096\n"
"buffer_load_dwordx4 v[160:163], v126, s[36:39], s75 offen offset:0 sc1\n"
"v_mfma_f32_16x16x32_bf16 v[8:11], v[232:235], v[164:167], v[8:11]\n"
"ds_read_b128 v[228:231], v114 offset:16640\n"
"v_mfma_f32_16x16x32_bf16 v[12:15], v[236:239], v[164:167], v[12:15]\n"
"ds_read_b128 v[232:235], v114 offset:33024\n"
"s_waitcnt vmcnt(8) lgkmcnt(7)\n"
"v_mfma_f32_16x16x32_bf16 v[0:3], v[240:243], v[128:131], v[0:3]\n"
"ds_read_b128 v[236:239], v114 offset:49408\n"
"v_mfma_f32_16x16x32_bf16 v[4:7], v[244:247], v[128:131], v[4:7]\n"
"ds_read_b128 v[240:243], v115 offset:256\n"
"buffer_load_dwordx4 v[164:167], v126, s[36:39], s75 offen offset:1024 sc1\n"
"v_mfma_f32_16x16x32_bf16 v[8:11], v[248:251], v[128:131], v[8:11]\n"
"ds_read_b128 v[244:247], v115 offset:16640\n"
"v_mfma_f32_16x16x32_bf16 v[12:15], v[252:255], v[128:131], v[12:15]\n"
"ds_read_b128 v[248:251], v115 offset:33024\n"
"s_waitcnt vmcnt(8) lgkmcnt(7)\n"
"v_mfma_f32_16x16x32_bf16 v[0:3], v[208:211], v[132:135], v[0:3]\n"
"ds_read_b128 v[252:255], v115 offset:49408\n"
"v_mfma_f32_16x16x32_bf16 v[4:7], v[212:215], v[132:135], v[4:7]\n"
"ds_read_b128 v[208:211], v112 offset:512\n"
"buffer_load_dwordx4 v[128:131], v126, s[36:39], s75 offen offset:2048 sc1\n"
"v_mfma_f32_16x16x32_bf16 v[8:11], v[216:219], v[132:135], v[8:11]\n"
"ds_read_b128 v[212:215], v112 offset:16896\n"
"v_mfma_f32_16x16x32_bf16 v[12:15], v[220:223], v[132:135], v[12:15]\n"
"ds_read_b128 v[216:219], v112 offset:33280\n"
"s_waitcnt vmcnt(8) lgkmcnt(7)\n"
"v_mfma_f32_16x16x32_bf16 v[0:3], v[224:227], v[136:139], v[0:3]\n"
"ds_read_b128 v[220:223], v112 offset:49664\n"
"v_mfma_f32_16x16x32_bf16 v[4:7], v[228:231], v[136:139], v[4:7]\n"
"ds_read_b128 v[224:227], v113 offset:512\n"
"buffer_load_dwordx4 v[132:135], v126, s[36:39], s75 offen offset:3072 sc1\n"
"v_mfma_f32_16x16x32_bf16 v[8:11], v[232:235], v[136:139], v[8:11]\n"
"ds_read_b128 v[228:231], v113 offset:16896\n"
"v_mfma_f32_16x16x32_bf16 v[12:15], v[236:239], v[136:139], v[12:15]\n"
"ds_read_b128 v[232:235], v113 offset:33280\n"
"s_waitcnt vmcnt(8) lgkmcnt(7)\n"
"v_mfma_f32_16x16x32_bf16 v[0:3], v[240:243], v[140:143], v[0:3]\n"
"ds_read_b128 v[236:239], v113 offset:49664\n"
"v_mfma_f32_16x16x32_bf16 v[4:7], v[244:247], v[140:143], v[4:7]\n"
"ds_read_b128 v[240:243], v114 offset:512\n"
"s_lshl_b32 s96, s25, 14\n"
"s_add_u32 s96, s96, s81\n"
"s_add_u32 s96, s96, 0x1400000\n"
"s_add_u32 m0, s81, 131072\n"
"s_nop 0\n"
"buffer_load_dwordx4 v126, s[36:39], s96 offen offset:0 sc1 lds\n"
"v_mfma_f32_16x16x32_bf16 v[8:11], v[248:251], v[140:143], v[8:11]\n"
"ds_read_b128 v[244:247], v114 offset:16896\n"
"v_mfma_f32_16x16x32_bf16 v[12:15], v[252:255], v[140:143], v[12:15]\n"
"ds_read_b128 v[248:251], v114 offset:33280\n"
"s_waitcnt vmcnt(8) lgkmcnt(7)\n"
"v_mfma_f32_16x16x32_bf16 v[0:3], v[208:211], v[144:147], v[0:3]\n"
"ds_read_b128 v[252:255], v114 offset:49664\n"
"v_mfma_f32_16x16x32_bf16 v[4:7], v[212:215], v[144:147], v[4:7]\n"
"ds_read_b128 v[208:211], v115 offset:512\n"
"buffer_load_dwordx4 v126, s[36:39], s96 offen offset:1024 sc1 lds\n"
"v_mfma_f32_16x16x32_bf16 v[8:11], v[216:219], v[144:147], v[8:11]\n"
"ds_read_b128 v[212:215], v115 offset:16896\n"
"v_mfma_f32_16x16x32_bf16 v[12:15], v[220:223], v[144:147], v[12:15]\n"
"ds_read_b128 v[216:219], v115 offset:33280\n"
"s_waitcnt vmcnt(8) lgkmcnt(7)\n"
"v_mfma_f32_16x16x32_bf16 v[0:3], v[224:227], v[148:151], v[0:3]\n"
"ds_read_b128 v[220:223], v115 offset:49664\n"
"v_mfma_f32_16x16x32_bf16 v[4:7], v[228:231], v[148:151], v[4:7]\n"
"ds_read_b128 v[224:227], v112 offset:768\n"
"s_add_u32 s96, s96, 8192\n"
"s_add_u32 m0, s81, 139264\n"
"s_nop 0\n"
"buffer_load_dwordx4 v126, s[36:39], s96 offen offset:0 sc1 lds\n"
"v_mfma_f32_16x16x32_bf16 v[8:11], v[232:235], v[148:151], v[8:11]\n"
"ds_read_b128 v[228:231], v112 offset:17152\n"
"v_mfma_f32_16x16x32_bf16 v[12:15], v[236:239], v[148:151], v[12:15]\n"
"ds_read_b128 v[232:235], v112 offset:33536\n"
"s_waitcnt vmcnt(8) lgkmcnt(7)\n"
"v_mfma_f32_16x16x32_bf16 v[0:3], v[240:243], v[152:155], v[0:3]\n"
"ds_read_b128 v[236:239], v112 offset:49920\n"
"v_mfma_f32_16x16x32_bf16 v[4:7], v[244:247], v[152:155], v[4:7]\n"
"ds_read_b128 v[240:243], v113 offset:768\n"
"buffer_load_dwordx4 v126, s[36:39], s96 offen offset:1024 sc1 lds\n"
"v_mfma_f32_16x16x32_bf16 v[8:11], v[248:251], v[152:155], v[8:11]\n"
"ds_read_b128 v[244:247], v113 offset:17152\n"
"v_mfma_f32_16x16x32_bf16 v[12:15], v[252:255], v[152:155], v[12:15]\n"
"ds_read_b128 v[248:251], v113 offset:33536\n"
"s_waitcnt vmcnt(8) lgkmcnt(7)\n"
"v_mfma_f32_16x16x32_bf16 v[0:3], v[208:211], v[156:159], v[0:3]\n"
"ds_read_b128 v[252:255], v113 offset:49920\n"
"v_mfma_f32_16x16x32_bf16 v[4:7], v[212:215], v[156:159], v[4:7]\n"
"ds_read_b128 v[208:211], v114 offset:768\n"
"v_mfma_f32_16x16x32_bf16 v[8:11], v[216:219], v[156:159], v[8:11]\n"
"ds_read_b128 v[212:215], v114 offset:17152\n"
"v_mfma_f32_16x16x32_bf16 v[12:15], v[220:223], v[156:159], v[12:15]\n"
"ds_read_b128 v[216:219], v114 offset:33536\n"
"s_waitcnt vmcnt(7) lgkmcnt(7)\n"
"v_mfma_f32_16x16x32_bf16 v[0:3], v[224:227], v[160:163], v[0:3]\n"
"ds_read_b128 v[220:223], v114 offset:49920\n"
"v_mfma_f32_16x16x32_bf16 v[4:7], v[228:231], v[160:163], v[4:7]\n"
"ds_read_b128 v[224:227], v115 offset:768\n"
"v_mfma_f32_16x16x32_bf16 v[8:11], v[232:235], v[160:163], v[8:11]\n"
"ds_read_b128 v[228:231], v115 offset:17152\n"
"v_mfma_f32_16x16x32_bf16 v[12:15], v[236:239], v[160:163], v[12:15]\n"
"ds_read_b128 v[232:235], v115 offset:33536\n"
"s_waitcnt vmcnt(6) lgkmcnt(7)\n"
"v_mfma_f32_16x16x32_bf16 v[0:3], v[240:243], v[164:167], v[0:3]\n"
"ds_read_b128 v[236:239], v115 offset:49920\n"
"v_mfma_f32_16x16x32_bf16 v[4:7], v[244:247], v[164:167], v[4:7]\n"
"v_mfma_f32_16x16x32_bf16 v[8:11], v[248:251], v[164:167], v[8:11]\n"
"v_mfma_f32_16x16x32_bf16 v[12:15], v[252:255], v[164:167], v[12:15]\n"
"s_waitcnt vmcnt(5) lgkmcnt(4)\n"
"v_mfma_f32_16x16x32_bf16 v[0:3], v[208:211], v[128:131], v[0:3]\n"
"v_mfma_f32_16x16x32_bf16 v[4:7], v[212:215], v[128:131], v[4:7]\n"
"v_mfma_f32_16x16x32_bf16 v[8:11], v[216:219], v[128:131], v[8:11]\n"
"v_mfma_f32_16x16x32_bf16 v[12:15], v[220:223], v[128:131], v[12:15]\n"
"s_waitcnt vmcnt(4) lgkmcnt(0)\n"
"v_mfma_f32_16x16x32_bf16 v[0:3], v[224:227], v[132:135], v[0:3]\n"
"v_mfma_f32_16x16x32_bf16 v[4:7], v[228:231], v[132:135], v[4:7]\n"
"v_mfma_f32_16x16x32_bf16 v[8:11], v[232:235], v[132:135], v[8:11]\n"
"v_mfma_f32_16x16x32_bf16 v[12:15], v[236:239], v[132:135], v[12:15]\n"
"s_nop 4\n"
"v_pk_add_f32 v[20:21], v[0:1], v[2:3]\n"
"v_pk_mul_f32 v[22:23], v[0:1], v[0:1]\n"
"v_pk_mul_f32 v[24:25], v[0:1], v[48:49]\n"
"v_pk_fma_f32 v[22:23], v[2:3], v[2:3], v[22:23]\n"
"v_pk_fma_f32 v[24:25], v[2:3], v[50:51], v[24:25]\n"
"v_pk_add_f32 v[20:21], v[20:21], v[4:5]\n"
"v_pk_fma_f32 v[22:23], v[4:5], v[4:5], v[22:23]\n"
"v_pk_fma_f32 v[24:25], v[4:5], v[52:53], v[24:25]\n"
"v_pk_add_f32 v[20:21], v[20:21], v[6:7]\n"
"v_pk_fma_f32 v[22:23], v[6:7], v[6:7], v[22:23]\n"
"v_pk_fma_f32 v[24:25], v[6:7], v[54:55], v[24:25]\n"
"v_pk_add_f32 v[20:21], v[20:21], v[8:9]\n"
"v_pk_fma_f32 v[22:23], v[8:9], v[8:9], v[22:23]\n"
"v_pk_fma_f32 v[24:25], v[8:9], v[56:57], v[24:25]\n"
"v_pk_add_f32 v[20:21], v[20:21], v[10:11]\n"
"v_pk_fma_f32 v[22:23], v[10:11], v[10:11], v[22:23]\n"
"v_pk_fma_f32 v[24:25], v[10:11], v[58:59], v[24:25]\n"
"v_pk_add_f32 v[20:21], v[20:21], v[12:13]\n"
"v_pk_fma_f32 v[22:23], v[12:13], v[12:13], v[22:23]\n"
"v_pk_fma_f32 v[24:25], v[12:13], v[60:61], v[24:25]\n"
"v_pk_add_f32 v[20:21], v[20:21], v[14:15]\n"
"v_pk_fma_f32 v[22:23], v[14:15], v[14:15], v[22:23]\n"
"v_pk_fma_f32 v[24:25], v[14:15], v[62:63], v[24:25]\n"
"v_add_f32 v16, v20, v21\n"
"v_add_f32 v17, v22, v23\n"
"v_add_f32 v18, v24, v25\n"
"v_mov_b32 v20, v16\n"
"v_mov_b32 v21, v17\n"
"v_mov_b32 v22, v18\n"
"v_permlane32_swap_b32 v16, v20\n"
"v_permlane32_swap_b32 v17, v21\n"
"v_permlane32_swap_b32 v18, v22\n"
"v_add_f32 v16, v16, v20\n"
"v_add_f32 v17, v17, v21\n"
"v_add_f32 v18, v18, v22\n"
"v_mov_b32 v20, v16\n"
"v_mov_b32 v21, v17\n"
"v_mov_b32 v22, v18\n"
"v_permlane16_swap_b32 v16, v20\n"
"v_permlane16_swap_b32 v17, v21\n"
"v_permlane16_swap_b32 v18, v22\n"
"v_add_f32 v16, v16, v20\n"
"v_add_f32 v17, v17, v21\n"
"v_add_f32 v18, v18, v22\n"
"v_mov_b32 v19, s68\n"
"s_mov_b64 exec, 0xffff\n"
"buffer_store_dwordx4 v[16:19], v126, s[36:39], s74 offen sc1\n"
"s_mov_b64 exec, -1\n"
"s_lshl_b32 s96, s3, 4\n"
"v_and_b32 v28, 15, v127\n"
"v_add_u32 v28, s96, v28\n"
"v_xor_b32 v28, 0x7ff, v28\n"
"s_add_u32 s96, s81, 155648\n"
"v_and_b32 v29, 48, v127\n"
"v_add_u32 v29, s96, v29\n"
"v_add_u32 v30, 128, v29\n"
"v_lshl_add_u32 v89, v127, 2, s96\n"
"v_and_b32 v95, 32, v127\n"
"v_mul_u32_u24 v95, 112, v95\n"
"v_add_u32 v95, v95, v126\n"
"s_lshr_b32 s96, s64, s70\n"
"s_and_b32 s96, s96, 1\n"
"s_lshl_b32 s97, s70, 1\n"
"s_add_u32 s96, s96, s97\n"
"s_lshl_b32 s96, s96, 7\n"
"s_add_u32 s96, s96, s3\n"
"s_lshl_b32 s96, s96, 6\n"
"s_add_u32 s96, s96, 0xa0000\n"
"s_lshl_b32 s97, s3, 14\n"
"s_lshl_b32 s98, s70, 9\n"
"s_add_u32 s97, s97, s98\n"
"s_add_u32 s97, s97, 0x800000\n"
"s_mov_b32 s98, 0xfffff800\n"
"v_and_b32 v104, 15, v127\n"
"v_lshlrev_b32 v104, 10, v104\n"
"v_and_or_b32 v104, v127, 48, v104\n"
"s_lshl_b32 s1, s3, 14\n"
"s_lshl_b32 s99, s70, 7\n"
"s_add_u32 s1, s1, s99\n"
"s_lshl_b32 s30, s25, 4\n"
"s_add_u32 s60, s30, 0x10000\n"
"s_cmp_eq_u32 s71, 3\n"
"s_cselect_b32 s60, s60, 0x2800000\n"
"s_add_u32 s31, s30, 0x800000\n"
"s_add_u32 s30, s30, 0x1000000\n"
"s_waitcnt vmcnt(1)\n"
"v_bfe_u32 v216, v127, 1, 1\n"
"v_lshlrev_b32 v216, 13, v216\n"
"v_lshrrev_b32 v217, 4, v127\n"
"v_lshl_add_u32 v216, v217, 7, v216\n"
"v_bfe_u32 v217, v127, 2, 2\n"
"v_lshl_add_u32 v216, v217, 4, v216\n"
"v_and_b32 v218, 1, v127\n"
"v_lshl_add_u32 v216, v218, 3, v216\n"
"s_add_u32 s99, s81, 131072\n"
"v_add_u32 v216, s99, v216\n"
"ds_read_b64_tr_b16 v[176:177], v216 offset:0\n"
"ds_read_b64_tr_b16 v[178:179], v216 offset:64\n"
"ds_read_b64_tr_b16 v[180:181], v216 offset:512\n"
"ds_read_b64_tr_b16 v[182:183], v216 offset:576\n"
"ds_read_b64_tr_b16 v[184:185], v216 offset:1024\n"
"ds_read_b64_tr_b16 v[186:187], v216 offset:1088\n"
"ds_read_b64_tr_b16 v[188:189], v216 offset:1536\n"
"ds_read_b64_tr_b16 v[190:191], v216 offset:1600\n"
"s_lshl_b32 s99, s71, 8\n"
"s_add_u32 s99, s99, 147456\n"
"v_lshl_add_u32 v216, v127, 2, s99\n"
"ds_read2st64_b32 v[208:209], v216 offset0:0 offset1:4\n"
"ds_read2st64_b32 v[210:211], v216 offset0:8 offset1:12\n"
"ds_read2st64_b32 v[212:213], v216 offset0:16 offset1:20\n"
"ds_read2st64_b32 v[214:215], v216 offset0:24 offset1:28\n"
"s_add_u32 s99, s81, 155648\n"
"v_lshl_add_u32 v217, v127, 2, s99\n"
"v_mul_u32_u24 v218, 320, v218\n"
"v_and_b32 v219, 48, v127\n"
"v_add3_u32 v218, v218, v219, s99\n"
"v_and_b32 v219, 15, v127\n"
"v_lshlrev_b32 v219, 3, v219\n"
"v_add_u32 v219, 156672, v219\n"
"v_add_u32 v216, s81, v219\n"
"s_waitcnt lgkmcnt(0)\n"
"v_lshlrev_b32 v192, 16, v208\n"
"v_and_b32 v193, 0xffff0000, v208\n"
"v_lshlrev_b32 v194, 16, v209\n"
"v_and_b32 v195, 0xffff0000, v209\n"
"v_lshlrev_b32 v196, 16, v210\n"
"v_and_b32 v197, 0xffff0000, v210\n"
"v_lshlrev_b32 v198, 16, v211\n"
"v_and_b32 v199, 0xffff0000, v211\n"
"v_lshlrev_b32 v200, 16, v212\n"
"v_and_b32 v201, 0xffff0000, v212\n"
"v_lshlrev_b32 v202, 16, v213\n"
"v_and_b32 v203, 0xffff0000, v213\n"
"v_lshlrev_b32 v204, 16, v214\n"
"v_and_b32 v205, 0xffff0000, v214\n"
"v_lshlrev_b32 v206, 16, v215\n"
"v_and_b32 v207, 0xffff0000, v215\n"
"s_waitcnt vmcnt(0)\n"
"s_mov_b32 s77, 200000\n"
"P32_%=:\n"
"buffer_load_dwordx4 v[16:19], v126, s[36:39], s73 offen offset:0 sc1\n"
"buffer_load_dwordx4 v[20:23], v126, s[36:39], s73 offen offset:1024 sc1\n"
"s_sub_u32 s77, s77, 1\n"
"s_cselect_b64 s[26:27], 0, -1\n"
"s_waitcnt vmcnt(0)\n"
"v_xor_b32 v24, s68, v19\n"
"v_bitop3_b32 v24, v24, v23, s68 bitop3:0xf6\n"
"v_cmp_ne_u32 vcc, 0, v24\n"
"s_nop 0\n"
"s_and_b64 vcc, vcc, s[26:27]\n"
"s_cbranch_vccnz P32_%=\n"
"v_add_f32 v16, v16, v20\n"
"v_add_f32 v17, v17, v21\n"
"v_add_f32 v18, v18, v22\n"
"v_mov_b32 v20, v16\n"
"v_mov_b32 v21, v17\n"
"v_mov_b32 v22, v18\n"
"v_permlane32_swap_b32 v16, v20\n"
"v_permlane32_swap_b32 v17, v21\n"
"v_permlane32_swap_b32 v18, v22\n"
"v_add_f32 v16, v16, v20\n"
"v_add_f32 v17, v17, v21\n"
"v_add_f32 v18, v18, v22\n"
"v_mov_b32 v20, v16\n"
"v_mov_b32 v21, v17\n"
"v_mov_b32 v22, v18\n"
"v_permlane16_swap_b32 v16, v20\n"
"v_permlane16_swap_b32 v17, v21\n"
"v_permlane16_swap_b32 v18, v22\n"
"v_add_f32 v16, v16, v20\n"
"v_add_f32 v17, v17, v21\n"
"v_add_f32 v18, v18, v22\n"
"v_mul_f32 v16, 0x3b000000, v16\n"
"v_mul_f32 v17, 0x3b000000, v17\n"
"v_fma_f32 v19, -v16, s82, v18\n"
"v_fma_f32 v17, -v16, v16, v17\n"
"v_max_f32 v17, 0, v17\n"
"v_add_f32 v18, 0x3727c5ac, v17\n"
"v_rsq_f32 v18, v18\n"
"s_nop 0\n"
"v_mul_f32 v19, v19, v18\n"
"v_ashrrev_i32 v21, 31, v19\n"
"v_or_b32 v21, 0x80000000, v21\n"
"v_xor_b32 v21, v19, v21\n"
"v_and_or_b32 v88, v21, s98, v28\n"
"v_sub_f32 v32, v0, v16\n"
"v_sub_f32 v33, v1, v16\n"
"v_max_u32_dpp v24, v88, v88 row_ror:8 row_mask:0xf bank_mask:0xf\n"
"v_sub_f32 v34, v2, v16\n"
"v_sub_f32 v35, v3, v16\n"
"v_max_u32_dpp v24, v24, v24 row_ror:4 row_mask:0xf bank_mask:0xf\n"
"v_sub_f32 v36, v4, v16\n"
"v_sub_f32 v37, v5, v16\n"
"v_max_u32_dpp v24, v24, v24 row_ror:2 row_mask:0xf bank_mask:0xf\n"
"v_sub_f32 v38, v6, v16\n"
"v_sub_f32 v39, v7, v16\n"
"v_max_u32_dpp v24, v24, v24 row_ror:1 row_mask:0xf bank_mask:0xf\n"
"v_cmp_ne_u32 vcc, v24, v88\n"
"v_mov_b32 v25, s68\n"
"v_mov_b32 v27, s68\n"
"v_cndmask_b32 v26, 0, v88, vcc\n"
"v_cndmask_b32 v29, v29, v30, vcc\n"
"v_sub_f32 v40, v8, v16\n"
"v_sub_f32 v41, v9, v16\n"
"v_max_u32_dpp v26, v26, v26 row_ror:8 row_mask:0xf bank_mask:0xf\n"
"v_sub_f32 v42, v10, v16\n"
"v_sub_f32 v43, v11, v16\n"
"v_max_u32_dpp v26, v26, v26 row_ror:4 row_mask:0xf bank_mask:0xf\n"
"v_sub_f32 v44, v12, v16\n"
"v_sub_f32 v45, v13, v16\n"
"v_max_u32_dpp v26, v26, v26 row_ror:2 row_mask:0xf bank_mask:0xf\n"
"v_sub_f32 v46, v14, v16\n"
"v_sub_f32 v47, v15, v16\n"
"v_max_u32_dpp v26, v26, v26 row_ror:1 row_mask:0xf bank_mask:0xf\n"
"s_mov_b64 exec, 1\n"
"buffer_store_dwordx4 v[24:27], off, s[36:39], s96 sc1\n"
"s_mov_b64 exec, -1\n"
"v_cmp_ge_u32 s[94:95], v88, v26\n"
"v_mul_f32 v32, v32, v18\n"
"v_mul_f32 v33, v33, v18\n"
"v_mul_f32 v34, v34, v18\n"
"v_mul_f32 v35, v35, v18\n"
"v_mul_f32 v36, v36, v18\n"
"v_mul_f32 v37, v37, v18\n"
"v_mul_f32 v38, v38, v18\n"
"v_mul_f32 v39, v39, v18\n"
"v_mul_f32 v40, v40, v18\n"
"v_mul_f32 v41, v41, v18\n"
"v_mul_f32 v42, v42, v18\n"
"v_mul_f32 v43, v43, v18\n"
"v_mul_f32 v44, v44, v18\n"
"v_mul_f32 v45, v45, v18\n"
"v_mul_f32 v46, v46, v18\n"
"v_mul_f32 v47, v47, v18\n"
"v_cvt_pk_bf16_f32 v48, v32, v33\n"
"v_cvt_pk_bf16_f32 v49, v34, v35\n"
"v_cvt_pk_bf16_f32 v50, v36, v37\n"
"v_cvt_pk_bf16_f32 v51, v38, v39\n"
"v_cvt_pk_bf16_f32 v52, v40, v41\n"
"v_cvt_pk_bf16_f32 v53, v42, v43\n"
"v_cvt_pk_bf16_f32 v54, v44, v45\n"
"v_cvt_pk_bf16_f32 v55, v46, v47\n"
"v_add_f32 v0, v0, v32\n"
"v_add_f32 v1, v1, v33\n"
"v_add_f32 v2, v2, v34\n"
"v_add_f32 v3, v3, v35\n"
"v_add_f32 v4, v4, v36\n"
"v_add_f32 v5, v5, v37\n"
"v_add_f32 v6, v6, v38\n"
"v_add_f32 v7, v7, v39\n"
"v_add_f32 v8, v8, v40\n"
"v_add_f32 v9, v9, v41\n"
"v_add_f32 v10, v10, v42\n"
"v_add_f32 v11, v11, v43\n"
"v_add_f32 v12, v12, v44\n"
"v_add_f32 v13, v13, v45\n"
"v_add_f32 v14, v14, v46\n"
"v_add_f32 v15, v15, v47\n"
"v_cvt_pk_bf16_f32 v96, v0, v1\n"
"v_cvt_pk_bf16_f32 v97, v2, v3\n"
"v_cvt_pk_bf16_f32 v98, v4, v5\n"
"v_cvt_pk_bf16_f32 v99, v6, v7\n"
"v_cvt_pk_bf16_f32 v100, v8, v9\n"
"v_cvt_pk_bf16_f32 v101, v10, v11\n"
"v_cvt_pk_bf16_f32 v102, v12, v13\n"
"v_cvt_pk_bf16_f32 v103, v14, v15\n"
"s_mov_b64 exec, s[94:95]\n"
"ds_write_b128 v29, v[48:51] offset:0\n"
"ds_write_b128 v29, v[52:55] offset:64\n"
"ds_write_b128 v29, v[96:99] offset:256\n"
"ds_write_b128 v29, v[100:103] offset:320\n"
"s_mov_b64 exec, -1\n"
"ds_read2st64_b32 v[24:25], v89 offset0:0 offset1:1\n"
"s_waitcnt lgkmcnt(0)\n"
"buffer_store_dwordx4 v[24:27], v95, s[36:39], s97 offen sc1\n"
"s_add_u32 s97, s97, 8192\n"
"buffer_store_dwordx4 v[24:27], v95, s[36:39], s97 offen sc1\n"
"v_mov_b32 v72, 157184\n"
"s_cmp_lg_u32 s71, 2\n"
"s_cbranch_scc1 R33_%=\n"
"s_lshl_b32 s73, s85, 14\n"
"s_add_u32 s73, s73, s65\n"
"s_add_u32 s73, s73, 0xa0000\n"
"s_add_u32 s74, s73, 4096\n"
"v_lshlrev_b32 v74, 6, v127\n"
"s_waitcnt vmcnt(0)\n"
"s_mov_b32 s77, 200000\n"
"P35_%=:\n"
"buffer_load_dwordx4 v[64:67], v74, s[36:39], s73 offen sc1\n"
"buffer_load_dwordx4 v[68:71], v74, s[36:39], s74 offen sc1\n"
"s_sub_u32 s77, s77, 1\n"
"s_cselect_b64 s[26:27], 0, -1\n"
"s_waitcnt vmcnt(0)\n"
"v_xor_b32 v73, s68, v65\n"
"v_bitop3_b32 v73, v73, v67, s68 bitop3:0xf6\n"
"v_bitop3_b32 v73, v73, v69, s68 bitop3:0xf6\n"
"v_bitop3_b32 v73, v73, v71, s68 bitop3:0xf6\n"
"v_cmp_ne_u32 vcc, 0, v73\n"
"s_nop 0\n"
"s_and_b64 vcc, vcc, s[26:27]\n"
"s_cbranch_vccnz P35_%=\n"
"v_max_u32 v73, v64, v68\n"
"s_nop 1\n"
"v_max_u32_dpp v73, v73, v73 row_shr:1 row_mask:0xf bank_mask:0xf bound_ctrl:1\n"
"s_nop 1\n"
"v_max_u32_dpp v73, v73, v73 row_shr:2 row_mask:0xf bank_mask:0xf bound_ctrl:1\n"
"s_nop 1\n"
"v_max_u32_dpp v73, v73, v73 row_shr:4 row_mask:0xf bank_mask:0xf bound_ctrl:1\n"
"s_nop 1\n"
"v_max_u32_dpp v73, v73, v73 row_shr:8 row_mask:0xf bank_mask:0xf bound_ctrl:1\n"
"s_nop 1\n"
"v_max_u32_dpp v73, v73, v73 row_bcast:15 row_mask:0xa bank_mask:0xf\n"
"s_nop 1\n"
"v_max_u32_dpp v73, v73, v73 row_bcast:31 row_mask:0xc bank_mask:0xf\n"
"s_nop 0\n"
"v_readlane_b32 s86, v73, 63\n"
"s_nop 1\n"
"v_cmp_eq_u32 vcc, s86, v64\n"
"v_cmp_eq_u32 s[94:95], s86, v68\n"
"s_nop 0\n"
"v_cndmask_b32 v64, v64, v66, vcc\n"
"v_cndmask_b32_e64 v68, v68, v70, s[94:95]\n"
"v_max_u32 v73, v64, v68\n"
"s_nop 1\n"
"v_max_u32_dpp v73, v73, v73 row_shr:1 row_mask:0xf bank_mask:0xf bound_ctrl:1\n"
"s_nop 1\n"
"v_max_u32_dpp v73, v73, v73 row_shr:2 row_mask:0xf bank_mask:0xf bound_ctrl:1\n"
"s_nop 1\n"
"v_max_u32_dpp v73, v73, v73 row_shr:4 row_mask:0xf bank_mask:0xf bound_ctrl:1\n"
"s_nop 1\n"
"v_max_u32_dpp v73, v73, v73 row_shr:8 row_mask:0xf bank_mask:0xf bound_ctrl:1\n"
"s_nop 1\n"
"v_max_u32_dpp v73, v73, v73 row_bcast:15 row_mask:0xa bank_mask:0xf\n"
"s_nop 1\n"
"v_max_u32_dpp v73, v73, v73 row_bcast:31 row_mask:0xc bank_mask:0xf\n"
"s_nop 0\n"
"v_readlane_b32 s87, v73, 63\n"
"v_mov_b32 v64, s86\n"
"s_nop 0\n"
"v_mov_b32 v65, s87\n"
"ds_write_b64 v72, v[64:65]\n"
"s_waitcnt lgkmcnt(0)\n"
"R33_%=:\n"
"s_barrier\n"
"ds_read_b64 v[64:65], v72\n"
"s_waitcnt lgkmcnt(0)\n"
"v_readfirstlane_b32 s86, v64\n"
"v_readfirstlane_b32 s87, v65\n"
"s_and_b32 s88, s86, 0x7ff\n"
"s_xor_b32 s88, s88, 0x7ff\n"
"s_and_b32 s89, s87, 0x7ff\n"
"s_xor_b32 s89, s89, 0x7ff\n"
"s_lshr_b32 s22, s88, 4\n"
"s_lshr_b32 s23, s89, 4\n"
"s_cmp_eq_u32 s22, s23\n"
"s_cselect_b32 s73, 4096, 0\n"
"s_lshl_b32 s22, s22, 14\n"
"s_lshl_b32 s23, s23, 14\n"
"s_add_u32 s73, s73, s65\n"
"s_add_u32 s22, s22, s65\n"
"s_add_u32 s23, s23, s73\n"
"s_lshl_b32 s96, s71, 10\n"
"s_add_u32 s96, s96, 0x800000\n"
"s_add_u32 s76, s96, s23\n"
"s_add_u32 s96, s96, s22\n"
"buffer_load_dwordx4 v[48:51], v126, s[36:39], s96 offen sc1\n"
"buffer_load_dwordx4 v[52:55], v126, s[36:39], s76 offen sc1\n"
"s_and_b32 s73, s86, 0xfffff800\n"
"s_or_b32 s73, s73, 0x400\n"
"s_ashr_i32 s74, s73, 31\n"
"s_not_b32 s74, s74\n"
"s_or_b32 s74, s74, 0x80000000\n"
"s_xor_b32 s73, s73, s74\n"
"v_mov_b32 v74, s73\n"
"s_and_b32 s73, s87, 0xfffff800\n"
"s_or_b32 s73, s73, 0x400\n"
"s_ashr_i32 s74, s73, 31\n"
"s_not_b32 s74, s74\n"
"s_or_b32 s74, s74, 0x80000000\n"
"s_xor_b32 s73, s73, s74\n"
"v_mov_b32 v75, s73\n"
"v_mul_f32 v74, 0xbfb8aa3b, v74\n"
"v_mul_f32 v75, 0xbfb8aa3b, v75\n"
"v_exp_f32 v74, v74\n"
"v_exp_f32 v75, v75\n"
"v_add_f32 v74, 1.0, v74\n"
"v_add_f32 v75, 1.0, v75\n"
"v_rcp_f32 v74, v74\n"
"v_rcp_f32 v75, v75\n"
"v_readfirstlane_b32 s20, v74\n"
"v_readfirstlane_b32 s21, v75\n"
"v_cmp_gt_u32 vcc, s87, v88\n"
"s_nop 0\n"
"s_mov_b64 exec, vcc\n"
"buffer_store_dwordx4 v[96:99], v104, s[48:51], s1 offen sc1\n"
"buffer_store_dwordx4 v[100:103], v104, s[48:51], s1 offen offset:64 sc1\n"
"s_mov_b64 exec, -1\n"
"v_and_b32 v144, 62, v127\n"
"v_add_u32 v144, v144, v127\n"
"v_lshl_add_u32 v144, v144, 12, s65\n"
"v_mov_b32 v145, s22\n"
"v_mov_b32 v147, s20\n"
"s_mov_b32 exec_lo, 0xffff0000\n"
"s_mov_b32 exec_hi, 0xffff0000\n"
"v_mov_b32 v145, s23\n"
"v_mov_b32 v147, s21\n"
"s_mov_b64 exec, -1\n"
"s_lshr_b32 s97, s25, 1\n"
"s_lshl_b32 s97, s97, 2\n"
"s_and_b32 s98, s25, 1\n"
"s_add_u32 s97, s97, s98\n"
"s_lshl_b32 s97, s97, 2\n"
"s_add_u32 s97, s97, s71\n"
"s_lshl_b32 s97, s97, 10\n"
"s_add_u32 s97, s97, 0x1000000\n"
"s_cmp_ge_u32 s69, 28\n"
"s_cselect_b32 s97, 0x2800000, s97\n"
"s_lshl_b32 s98, s71, 1\n"
"s_lshr_b32 s98, s64, s98\n"
"v_lshrrev_b32 v93, 5, v127\n"
"v_bfe_u32 v93, s98, v93, 1\n"
"v_lshl_add_u32 v93, v93, 13, v126\n"
"s_movk_i32 s98, 0xff\n"
"v_mov_b32 v47, s68\n"
"s_waitcnt vmcnt(2)\n"
"v_xor_b32 v45, s68, v51\n"
"v_bitop3_b32 v45, v45, v55, s68 bitop3:0xf6\n"
"v_cmp_ne_u32 vcc, 0, v45\n"
"s_cbranch_vccnz S36_%=\n"
"B37_%=:\n"
"ds_write2_b32 v217, v48, v52 offset0:0 offset1:80\n"
"ds_read_b128 v[48:51], v218 offset:0\n"
"ds_read_b128 v[52:55], v218 offset:64\n"
"ds_read_b128 v[56:59], v218 offset:128\n"
"ds_read_b128 v[60:63], v218 offset:192\n"
"s_waitcnt lgkmcnt(3)\n"
"v_mfma_f32_16x16x32_bf16 v[32:35], v[48:51], v[176:179], 0\n"
"s_waitcnt lgkmcnt(2)\n"
"v_mfma_f32_16x16x32_bf16 v[32:35], v[52:55], v[180:183], v[32:35]\n"
"s_waitcnt lgkmcnt(1)\n"
"v_mfma_f32_16x16x32_bf16 v[32:35], v[56:59], v[184:187], v[32:35]\n"
"s_waitcnt lgkmcnt(0)\n"
"v_mfma_f32_16x16x32_bf16 v[32:35], v[60:63], v[188:191], v[32:35]\n"
"s_nop 7\n"
"ds_write_b64 v216, v[32:33]\n"
"s_waitcnt lgkmcnt(0)\n"
"s_barrier\n"
"ds_read_b64 v[48:49], v219 offset:0\n"
"ds_read_b64 v[50:51], v219 offset:2048\n"
"ds_read_b64 v[52:53], v219 offset:4096\n"
"ds_read_b64 v[54:55], v219 offset:6144\n"
"s_waitcnt lgkmcnt(0)\n"
"v_add_f32 v48, v48, v50\n"
"v_add_f32 v49, v49, v51\n"
"v_add_f32 v52, v52, v54\n"
"v_add_f32 v53, v53, v55\n"
"v_add_f32 v48, v48, v52\n"
"v_add_f32 v49, v49, v53\n"
"v_mul_f32 v50, 0xbfb8aa3b, v48\n"
"v_mul_f32 v51, 0xbfb8aa3b, v49\n"
"v_exp_f32 v50, v50\n"
"v_exp_f32 v51, v51\n"
"v_mul_f32_dpp v52, v48, v48 row_ror:8 row_mask:0xf bank_mask:0xf\n"
"v_mul_f32_dpp v53, v49, v49 row_ror:8 row_mask:0xf bank_mask:0xf\n"
"v_add_f32 v50, 1.0, v50\n"
"v_add_f32 v51, 1.0, v51\n"
"v_rcp_f32 v50, v50\n"
"v_rcp_f32 v51, v51\n"
"v_mul_f32 v52, v52, v50\n"
"v_mul_f32 v53, v53, v51\n"
"v_readlane_b32 s4, v52, 0\n"
"v_readlane_b32 s5, v52, 1\n"
"v_readlane_b32 s6, v52, 2\n"
"v_readlane_b32 s7, v52, 3\n"
"v_readlane_b32 s8, v52, 4\n"
"v_readlane_b32 s9, v52, 5\n"
"v_readlane_b32 s10, v52, 6\n"
"v_readlane_b32 s11, v52, 7\n"
"v_readlane_b32 s12, v53, 0\n"
"v_readlane_b32 s13, v53, 1\n"
"v_readlane_b32 s14, v53, 2\n"
"v_readlane_b32 s15, v53, 3\n"
"v_readlane_b32 s16, v53, 4\n"
"v_readlane_b32 s17, v53, 5\n"
"v_readlane_b32 s18, v53, 6\n"
"v_readlane_b32 s19, v53, 7\n"
"v_mul_f32 v48, s4, v192\n"
"v_mul_f32 v49, s4, v193\n"
"v_mul_f32 v50, s12, v192\n"
"v_mul_f32 v51, s12, v193\n"
"v_fmac_f32 v48, s5, v194\n"
"v_fmac_f32 v49, s5, v195\n"
"v_fmac_f32 v50, s13, v194\n"
"v_fmac_f32 v51, s13, v195\n"
"v_fmac_f32 v48, s6, v196\n"
"v_fmac_f32 v49, s6, v197\n"
"v_fmac_f32 v50, s14, v196\n"
"v_fmac_f32 v51, s14, v197\n"
"v_fmac_f32 v48, s7, v198\n"
"v_fmac_f32 v49, s7, v199\n"
"v_fmac_f32 v50, s15, v198\n"
"v_fmac_f32 v51, s15, v199\n"
"v_fmac_f32 v48, s8, v200\n"
"v_fmac_f32 v49, s8, v201\n"
"v_fmac_f32 v50, s16, v200\n"
"v_fmac_f32 v51, s16, v201\n"
"v_fmac_f32 v48, s9, v202\n"
"v_fmac_f32 v49, s9, v203\n"
"v_fmac_f32 v50, s17, v202\n"
"v_fmac_f32 v51, s17, v203\n"
"v_fmac_f32 v48, s10, v204\n"
"v_fmac_f32 v49, s10, v205\n"
"v_fmac_f32 v50, s18, v204\n"
"v_fmac_f32 v51, s18, v205\n"
"v_fmac_f32 v48, s11, v206\n"
"v_fmac_f32 v49, s11, v207\n"
"v_fmac_f32 v50, s19, v206\n"
"v_fmac_f32 v51, s19, v207\n"
"v_bfi_b32 v48, s98, v47, v48\n"
"v_bfi_b32 v49, s98, v47, v49\n"
"v_bfi_b32 v50, s98, v47, v50\n"
"v_bfi_b32 v51, s98, v47, v51\n"
"buffer_store_dwordx4 v[48:51], v93, s[36:39], s97 offen sc1\n"
"s_cmp_lg_u32 s71, 3\n"
"s_cbranch_scc1 R38_%=\n"
"v_mov_b32 v64, s88\n"
"s_mov_b32 exec_lo, 0xffff0000\n"
"s_mov_b32 exec_hi, 0xffff0000\n"
"v_mov_b32 v64, s89\n"
"s_mov_b64 exec, -1\n"
"v_lshrrev_b32 v65, 5, v127\n"
"v_lshlrev_b32 v65, 1, v65\n"
"v_lshl_add_u32 v64, v64, 10, v65\n"
"s_lshl_b32 s96, s25, 2\n"
"s_waitcnt vmcnt(0)\n"
"s_mov_b32 s77, 200000\n"
"P40_%=:\n"
"v_mov_b32 v140, s0\n"
"v_mov_b32 v141, s0\n"
"v_mov_b32 v142, s0\n"
"v_mov_b32 v143, s0\n"
"s_mov_b64 exec, -1\n"
"s_add_u32 s99, s30, 0\n"
"buffer_load_dwordx4 v[128:131], v144, s[36:39], s99 offen sc1\n"
"s_mov_b64 exec, -1\n"
"s_mov_b64 exec, -1\n"
"s_add_u32 s99, s30, 524288\n"
"buffer_load_dwordx4 v[132:135], v144, s[36:39], s99 offen sc1\n"
"s_mov_b64 exec, -1\n"
"s_mov_b64 exec, -1\n"
"s_add_u32 s99, s30, 1048576\n"
"buffer_load_dwordx4 v[136:139], v144, s[36:39], s99 offen sc1\n"
"s_mov_b64 exec, -1\n"
"s_mov_b64 exec, -1\n"
"s_mov_b32 exec_hi, 0\n"
"s_add_u32 s99, s30, 1572864\n"
"buffer_load_dwordx4 v[140:143], v144, s[36:39], s99 offen sc1\n"
"s_mov_b64 exec, -1\n"
"s_mov_b64 exec, -1\n"
"buffer_load_dwordx4 v[148:151], v145, s[36:39], s31 offen sc1\n"
"s_mov_b64 exec, -1\n"
"s_sub_u32 s77, s77, 1\n"
"s_cselect_b64 s[26:27], 0, -1\n"
"s_waitcnt vmcnt(0)\n"
"v_xor_b32 v128, s0, v128\n"
"v_xor_b32 v129, s0, v129\n"
"v_xor_b32 v130, s0, v130\n"
"v_xor_b32 v131, s0, v131\n"
"v_xor_b32 v132, s0, v132\n"
"v_xor_b32 v133, s0, v133\n"
"v_xor_b32 v134, s0, v134\n"
"v_xor_b32 v135, s0, v135\n"
"v_xor_b32 v136, s0, v136\n"
"v_xor_b32 v137, s0, v137\n"
"v_xor_b32 v138, s0, v138\n"
"v_xor_b32 v139, s0, v139\n"
"v_xor_b32 v140, s0, v140\n"
"v_xor_b32 v141, s0, v141\n"
"v_xor_b32 v142, s0, v142\n"
"v_xor_b32 v143, s0, v143\n"
"v_or_b32 v146, v128, v129\n"
"v_or3_b32 v146, v146, v130, v131\n"
"v_or3_b32 v146, v146, v132, v133\n"
"v_or3_b32 v146, v146, v134, v135\n"
"v_or3_b32 v146, v146, v136, v137\n"
"v_or3_b32 v146, v146, v138, v139\n"
"v_or3_b32 v146, v146, v140, v141\n"
"v_or3_b32 v146, v146, v142, v143\n"
"v_and_b32 v146, 0xff, v146\n"
"v_bitop3_b32 v146, v146, v151, s68 bitop3:0xf6\n"
"v_cmp_ne_u32 vcc, 0, v146\n"
"s_nop 0\n"
"s_and_b64 vcc, vcc, s[26:27]\n"
"s_cbranch_vccnz P40_%=\n"
"v_add_f32 v128, v128, v132\n"
"v_add_f32 v129, v129, v133\n"
"v_add_f32 v130, v130, v134\n"
"v_add_f32 v131, v131, v135\n"
"v_add_f32 v136, v136, v140\n"
"v_add_f32 v137, v137, v141\n"
"v_add_f32 v138, v138, v142\n"
"v_add_f32 v139, v139, v143\n"
"v_and_b32 v148, 0xffff0000, v149\n"
"v_add_f32 v128, v128, v136\n"
"v_add_f32 v129, v129, v137\n"
"v_add_f32 v130, v130, v138\n"
"v_add_f32 v131, v131, v139\n"
"s_mov_b32 exec_hi, 0\n"
"v_lshlrev_b32 v148, 16, v149\n"
"s_mov_b64 exec, -1\n"
"v_permlane32_swap_b32 v128, v129\n"
"v_permlane32_swap_b32 v130, v131\n"
"v_add_f32 v128, v128, v129\n"
"v_add_f32 v130, v130, v131\n"
"s_nop 1\n"
"v_permlane16_swap_b32 v128, v130\n"
"v_add_f32 v128, v128, v130\n"
"s_nop 1\n"
"v_add_f32_dpp v128, v128, v128 row_ror:8 row_mask:0xf bank_mask:0xf\n"
"s_nop 1\n"
"v_add_f32_dpp v128, v128, v128 row_ror:4 row_mask:0xf bank_mask:0xf\n"
"s_nop 1\n"
"v_add_f32_dpp v128, v128, v128 row_ror:2 row_mask:0xf bank_mask:0xf\n"
"s_nop 1\n"
"v_add_f32_dpp v128, v128, v128 row_ror:1 row_mask:0xf bank_mask:0xf\n"
"v_fma_f32 v128, v128, v147, v148\n"
"v_cvt_pk_bf16_f32 v65, v128, v128\n"
"s_mov_b32 exec_lo, 0x10001\n"
"s_mov_b32 exec_hi, 0x10001\n"
"buffer_store_short v65, v64, s[48:51], s96 offen\n"
"s_mov_b64 exec, -1\n"
"R38_%=:\n"
"END_%=:\n"
"s_endpgm\n"
"S3_%=:\n"
"s_mov_b32 s77, 200000\n"
"P41_%=:\n"
"buffer_load_dwordx2 v[62:63], v34, s[36:39], s75 offen sc1\n"
"s_sub_u32 s77, s77, 1\n"
"s_cselect_b64 s[26:27], 0, -1\n"
"s_waitcnt vmcnt(0)\n"
"v_cmp_ne_u32 vcc, s68, v62\n"
"s_nop 0\n"
"s_and_b64 vcc, vcc, s[26:27]\n"
"s_cbranch_vccnz P41_%=\n"
"s_branch B4_%=\n"
"S9_%=:\n"
"s_mov_b32 s77, 200000\n"
"P42_%=:\n"
"s_mov_b64 exec, 0xff\n"
"buffer_load_dword v39, v38, s[36:39], s73 offen sc1\n"
"s_mov_b64 exec, -1\n"
"s_sub_u32 s77, s77, 1\n"
"s_cselect_b64 s[26:27], 0, -1\n"
"s_waitcnt vmcnt(0)\n"
"v_cmp_ne_u32 vcc, s68, v39\n"
"s_nop 0\n"
"s_and_b64 vcc, vcc, s[26:27]\n"
"s_cbranch_vccnz P42_%=\n"
"s_branch B10_%=\n"
"S18_%=:\n"
"s_mov_b32 s77, 200000\n"
"P43_%=:\n"
"buffer_load_dwordx4 v[48:51], v126, s[36:39], s96 offen sc1\n"
"buffer_load_dwordx4 v[52:55], v126, s[36:39], s76 offen sc1\n"
"s_sub_u32 s77, s77, 1\n"
"s_cselect_b64 s[26:27], 0, -1\n"
"s_waitcnt vmcnt(0)\n"
"v_xor_b32 v45, s68, v51\n"
"v_bitop3_b32 v45, v45, v55, s68 bitop3:0xf6\n"
"v_cmp_ne_u32 vcc, 0, v45\n"
"s_nop 0\n"
"s_and_b64 vcc, vcc, s[26:27]\n"
"s_cbranch_vccnz P43_%=\n"
"s_branch B19_%=\n"
"S20_%=:\n"
"s_mov_b32 s77, 200000\n"
"P44_%=:\n"
"v_mov_b32 v140, s0\n"
"v_mov_b32 v141, s0\n"
"v_mov_b32 v142, s0\n"
"v_mov_b32 v143, s0\n"
"s_mov_b64 exec, -1\n"
"s_add_u32 s99, s30, 0\n"
"buffer_load_dwordx4 v[128:131], v144, s[36:39], s99 offen sc1\n"
"s_mov_b64 exec, -1\n"
"s_mov_b64 exec, -1\n"
"s_add_u32 s99, s30, 524288\n"
"buffer_load_dwordx4 v[132:135], v144, s[36:39], s99 offen sc1\n"
"s_mov_b64 exec, -1\n"
"s_mov_b64 exec, -1\n"
"s_add_u32 s99, s30, 1048576\n"
"buffer_load_dwordx4 v[136:139], v144, s[36:39], s99 offen sc1\n"
"s_mov_b64 exec, -1\n"
"s_mov_b64 exec, -1\n"
"s_mov_b32 exec_hi, 0\n"
"s_add_u32 s99, s30, 1572864\n"
"buffer_load_dwordx4 v[140:143], v144, s[36:39], s99 offen sc1\n"
"s_mov_b64 exec, -1\n"
"s_mov_b64 exec, -1\n"
"buffer_load_dwordx4 v[148:151], v145, s[36:39], s31 offen sc1\n"
"s_mov_b64 exec, -1\n"
"s_sub_u32 s77, s77, 1\n"
"s_cselect_b64 s[26:27], 0, -1\n"
"s_waitcnt vmcnt(0)\n"
"v_xor_b32 v128, s0, v128\n"
"v_xor_b32 v129, s0, v129\n"
"v_xor_b32 v130, s0, v130\n"
"v_xor_b32 v131, s0, v131\n"
"v_xor_b32 v132, s0, v132\n"
"v_xor_b32 v133, s0, v133\n"
"v_xor_b32 v134, s0, v134\n"
"v_xor_b32 v135, s0, v135\n"
"v_xor_b32 v136, s0, v136\n"
"v_xor_b32 v137, s0, v137\n"
"v_xor_b32 v138, s0, v138\n"
"v_xor_b32 v139, s0, v139\n"
"v_xor_b32 v140, s0, v140\n"
"v_xor_b32 v141, s0, v141\n"
"v_xor_b32 v142, s0, v142\n"
"v_xor_b32 v143, s0, v143\n"
"v_or_b32 v146, v128, v129\n"
"v_or3_b32 v146, v146, v130, v131\n"
"v_or3_b32 v146, v146, v132, v133\n"
"v_or3_b32 v146, v146, v134, v135\n"
"v_or3_b32 v146, v146, v136, v137\n"
"v_or3_b32 v146, v146, v138, v139\n"
"v_or3_b32 v146, v146, v140, v141\n"
"v_or3_b32 v146, v146, v142, v143\n"
"v_and_b32 v146, 0xff, v146\n"
"v_bitop3_b32 v146, v146, v151, s68 bitop3:0xf6\n"
"v_cmp_ne_u32 vcc, 0, v146\n"
"s_nop 0\n"
"s_and_b64 vcc, vcc, s[26:27]\n"
"s_cbranch_vccnz P44_%=\n"
"s_branch B21_%=\n"
"S22_%=:\n"
"s_mov_b32 s77, 200000\n"
"P45_%=:\n"
"buffer_load_dwordx4 v[128:131], v126, s[36:39], s61 offen sc1\n"
"buffer_load_dwordx2 v[132:133], v126, s[36:39], s62 offen offset:8 sc1\n"
"buffer_load_dwordx2 v[134:135], v126, s[36:39], s63 offen offset:8 sc1\n"
"s_sub_u32 s77, s77, 1\n"
"s_cselect_b64 s[26:27], 0, -1\n"
"s_waitcnt vmcnt(0)\n"
"v_xor_b32 v128, s0, v128\n"
"v_xor_b32 v129, s0, v129\n"
"v_xor_b32 v130, s0, v130\n"
"v_xor_b32 v131, s0, v131\n"
"v_or_b32 v137, v128, v129\n"
"v_or3_b32 v137, v137, v130, v131\n"
"v_and_b32 v137, 0xff, v137\n"
"v_bitop3_b32 v137, v137, v133, s68 bitop3:0xf6\n"
"v_bitop3_b32 v137, v137, v135, s68 bitop3:0xf6\n"
"v_cmp_ne_u32 vcc, 0, v137\n"
"s_nop 0\n"
"s_and_b64 vcc, vcc, s[26:27]\n"
"s_cbranch_vccnz P45_%=\n"
"s_branch B23_%=\n"
"S30_%=:\n"
"s_mov_b32 s77, 200000\n"
"P46_%=:\n"
"s_mov_b64 exec, 0xff\n"
"buffer_load_dword v39, v38, s[36:39], s73 offen sc1\n"
"s_mov_b64 exec, -1\n"
"s_sub_u32 s77, s77, 1\n"
"s_cselect_b64 s[26:27], 0, -1\n"
"s_waitcnt vmcnt(0)\n"
"v_cmp_ne_u32 vcc, s68, v39\n"
"s_nop 0\n"
"s_and_b64 vcc, vcc, s[26:27]\n"
"s_cbranch_vccnz P46_%=\n"
"s_branch B31_%=\n"
"S36_%=:\n"
"s_mov_b32 s77, 200000\n"
"P47_%=:\n"
"buffer_load_dwordx4 v[48:51], v126, s[36:39], s96 offen sc1\n"
"buffer_load_dwordx4 v[52:55], v126, s[36:39], s76 offen sc1\n"
"s_sub_u32 s77, s77, 1\n"
"s_cselect_b64 s[26:27], 0, -1\n"
"s_waitcnt vmcnt(0)\n"
"v_xor_b32 v45, s68, v51\n"
"v_bitop3_b32 v45, v45, v55, s68 bitop3:0xf6\n"
"v_cmp_ne_u32 vcc, 0, v45\n"
"s_nop 0\n"
"s_and_b64 vcc, vcc, s[26:27]\n"
"s_cbranch_vccnz P47_%=\n"
"s_branch B37_%=\n"
    : "+{v0}"(tid), "+{s3}"(lbase)
    : "{s[0:1]}"(__builtin_amdgcn_kernarg_segment_ptr()), "{s2}"(blockIdx.x)
    : "v1", "v2", "v3", "v4", "v5", "v6", "v7", "v8", "v9", "v10", "v11", "v12", "v13", "v14", "v15", "v16", "v17", "v18", "v19", "v20", "v21", "v22", "v23", "v24", "v25", "v26", "v27", "v28", "v29", "v30", "v31", "v32", "v33", "v34", "v35", "v36", "v37", "v38", "v39", "v40", "v41", "v42", "v43", "v44", "v45", "v46", "v47", "v48", "v49", "v50", "v51", "v52", "v53", "v54", "v55", "v56", "v57", "v58", "v59", "v60", "v61", "v62", "v63", "v64", "v65", "v66", "v67", "v68", "v69", "v70", "v71", "v72", "v73", "v74", "v75", "v76", "v77", "v78", "v79", "v80", "v81", "v82", "v83", "v84", "v85", "v86", "v87", "v88", "v89", "v90", "v91", "v92", "v93", "v94", "v95", "v96", "v97", "v98", "v99", "v100", "v101", "v102", "v103", "v104", "v105", "v106", "v107", "v108", "v109", "v110", "v111", "v112", "v113", "v114", "v115", "v116", "v117", "v118", "v119", "v120", "v121", "v122", "v123", "v124", "v125", "v126", "v127", "v128", "v129", "v130", "v131", "v132", "v133", "v134", "v135", "v136", "v137", "v138", "v139", "v140", "v141", "v142", "v143", "v144", "v145", "v146", "v147", "v148", "v149", "v150", "v151", "v152", "v153", "v154", "v155", "v156", "v157", "v158", "v159", "v160", "v161", "v162", "v163", "v164", "v165", "v166", "v167", "v168", "v169", "v170", "v171", "v172", "v173", "v174", "v175", "v176", "v177", "v178", "v179", "v180", "v181", "v182", "v183", "v184", "v185", "v186", "v187", "v188", "v189", "v190", "v191", "v192", "v193", "v194", "v195", "v196", "v197", "v198", "v199", "v200", "v201", "v202", "v203", "v204", "v205", "v206", "v207", "v208", "v209", "v210", "v211", "v212", "v213", "v214", "v215", "v216", "v217", "v218", "v219", "v220", "v221", "v222", "v223", "v224", "v225", "v226", "v227", "v228", "v229", "v230", "v231", "v232", "v233", "v234", "v235", "v236", "v237", "v238", "v239", "v240", "v241", "v242", "v243", "v244", "v245", "v246", "v247", "v248", "v249", "v250", "v251", "v252", "v253", "v254", "v255", "a0", "a1", "a2", "a3", "a4", "a5", "a6", "a7", "a8", "a9", "a10", "a11", "a12", "a13", "a14", "a15", "a16", "a17", "a18", "a19", "a20", "a21", "a22", "a23", "a24", "a25", "a26", "a27", "a28", "a29", "a30", "a31", "a32", "a33", "a34", "a35", "a36", "a37", "a38", "a39", "a40", "a41", "a42", "a43", "a44", "a45", "a46", "a47", "a48", "a49", "a50", "a51", "a52", "a53", "a54", "a55", "a56", "a57", "a58", "a59", "a60", "a61", "a62", "a63", "a64", "a65", "a66", "a67", "a68", "a69", "a70", "a71", "a72", "a73", "a74", "a75", "a76", "a77", "a78", "a79", "a80", "a81", "a82", "a83", "a84", "a85", "a86", "a87", "a88", "a89", "a90", "a91", "a92", "a93", "a94", "a95", "a96", "a97", "a98", "a99", "a100", "a101", "a102", "a103", "a104", "a105", "a106", "a107", "a108", "a109", "a110", "a111", "a112", "a113", "a114", "a115", "a116", "a117", "a118", "a119", "a120", "a121", "a122", "a123", "a124", "a125", "a126", "a127", "a128", "a129", "a130", "a131", "a132", "a133", "a134", "a135", "a136", "a137", "a138", "a139", "a140", "a141", "a142", "a143", "a144", "a145", "a146", "a147", "a148", "a149", "a150", "a151", "a152", "a153", "a154", "a155", "a156", "a157", "a158", "a159", "a160", "a161", "a162", "a163", "a164", "a165", "a166", "a167", "a168", "a169", "a170", "a171", "a172", "a173", "a174", "a175", "a176", "a177", "a178", "a179", "a180", "a181", "a182", "a183", "a184", "a185", "a186", "a187", "a188", "a189", "a190", "a191", "a192", "a193", "a194", "a195", "a196", "a197", "a198", "a199", "a200", "a201", "a202", "a203", "a204", "a205", "a206", "a207", "a208", "a209", "a210", "a211", "a212", "a213", "a214", "a215", "a216", "a217", "a218", "a219", "a220", "a221", "a222", "a223", "a224", "a225", "a226", "a227", "a228", "a229", "a230", "a231", "a232", "a233", "a234", "a235", "a236", "a237", "a238", "a239", "a240", "a241", "a242", "a243", "a244", "a245", "a246", "a247", "a248", "a249", "a250", "a251", "a252", "a253", "a254", "a255", "s4", "s5", "s6", "s7", "s8", "s9", "s10", "s11", "s12", "s13", "s14", "s15", "s16", "s17", "s18", "s19", "s20", "s21", "s22", "s23", "s24", "s25", "s26", "s27", "s28", "s29", "s30", "s31", "s33", "s34", "s35", "s36", "s37", "s38", "s39", "s40", "s41", "s42", "s43", "s44", "s45", "s46", "s47", "s48", "s49", "s50", "s51", "s52", "s53", "s54", "s55", "s56", "s57", "s58", "s59", "s60", "s61", "s62", "s63", "s64", "s65", "s66", "s67", "s68", "s69", "s70", "s71", "s72", "s73", "s74", "s75", "s76", "s77", "s78", "s79", "s80", "s81", "s82", "s83", "s84", "s85", "s86", "s87", "s88", "s89", "s90", "s91", "s92", "s93", "s94", "s95", "s96", "s97", "s98", "s99", "vcc", "scc", "memory");
}

extern "C" void kernel_launch(void* const* d_in, const int* in_sizes, int n_in, void* d_out, int out_size, void* d_ws, size_t ws_size,
                              hipStream_t stream) {
  Args a;
  a.x = d_in[0]; a.cr = d_in[1]; a.ci = d_in[2]; a.dre = d_in[3]; a.dim = d_in[4];
  a.wr = d_in[6]; a.wg = d_in[7]; a.wu = d_in[8]; a.wd = d_in[9]; a.out = d_out; a.ws = d_ws;
  fnet_mod_kernel<<<256, 256, 0, stream>>>(a);
}
```
